```python
import torch

def _softmax(x, axis=-1):
    return torch.softmax(x, dim=axis)
def _gelu(x):
    return torch.nn.functional.gelu(x, approximate="tanh")
def _silu(x):
    return torch.nn.functional.silu(x)
def _sigmoid(x):
    return torch.sigmoid(x)
def _layernorm(x, axis=-1, eps=1e-5):
    m = x.mean(dim=axis, keepdim=True)
    v = x.var(dim=axis, keepdim=True, unbiased=False)
    return (x - m) / torch.sqrt(v + eps)
def _scatter_add(tgt, idx, src, axis=0):
    return tgt.clone().index_add_(axis, idx, src)
def _segment_sum(src, seg, n):
    out = torch.zeros((n,) + src.shape[1:], dtype=src.dtype)
    return out.index_add_(0, seg, src)
def _pad(x, pw):
    flat = tuple(v for pair in reversed(pw) for v in pair)
    return torch.nn.functional.pad(x, flat)

kernel_name = 'recurrent_scan__linear_kernel_hedgehog__moe_arch_mod__prompt'

def setup_inputs():

    _w = (lambda a: (lambda t: t.to(torch.bfloat16) if t.is_floating_point() else t)(torch.as_tensor(a)))
    def _A_setup():
        B, S, D, H = 1, 2048, 1024, 32
        Dh = D // H
        return {
            "x": _w((lambda *s: torch.randn(*s, dtype=torch.bfloat16) * 0.05)(B, S, D)),
            "wqkv": _w((lambda *s: torch.randn(*s, dtype=torch.bfloat16) * 0.2)(D, 3 * D)),
            "w_phi": _w((lambda *s: torch.randn(*s, dtype=torch.bfloat16) * 0.2)(H, Dh, Dh) * (Dh ** -0.5)),
            "wo": _w((lambda *s: torch.randn(*s, dtype=torch.bfloat16) * 0.05)(D, D)),
        }
    _A_in = _A_setup()

    def _B_setup():
        B, S, D, F, C = 1, 2048, 1024, 3584, 1
        return {
            "x": _w((lambda *s: torch.randn(*s, dtype=torch.bfloat16) * 0.05)(B, S, D)),
            "w_r": _w((lambda *s: torch.randn(*s, dtype=torch.bfloat16) * 0.05)(D)),
            "w_gate": _w((lambda *s: torch.randn(*s, dtype=torch.bfloat16) * 0.05)(D, F)),
            "w_up": _w((lambda *s: torch.randn(*s, dtype=torch.bfloat16) * 0.05)(D, F)),
            "w_down": _w((lambda *s: torch.randn(*s, dtype=torch.bfloat16) * 0.05)(F, D)),
        }
    _B_in = _B_setup()

    return {
        "A_x": _A_in["x"],
        "A_wqkv": _A_in["wqkv"],
        "A_w_phi": _A_in["w_phi"],
        "A_wo": _A_in["wo"],
        "B_x": _B_in["x"],
        "B_w_r": _B_in["w_r"],
        "B_w_gate": _B_in["w_gate"],
        "B_w_up": _B_in["w_up"],
        "B_w_down": _B_in["w_down"],
    }

def reference(A_x, A_wqkv, A_w_phi, A_wo, B_x, B_w_r, B_w_gate, B_w_up, B_w_down):
    def _A_ref(x, wqkv, w_phi, wo):
        B, S, D = x.shape
        H = 32; Dh = D // H
        qkv = (lambda x, *d: x.permute(*d))((x @ wqkv).reshape(B, S, 3, H, Dh), 2, 0, 3, 1, 4)
        q, k, v = qkv[0], qkv[1], qkv[2]
        pq = torch.einsum("bhsd,hdf->bhsf", q, w_phi)
        pk = torch.einsum("bhsd,hdf->bhsf", k, w_phi)
        phi_q = torch.concatenate([_softmax(pq, axis=-1), _softmax(-pq, axis=-1)], axis=-1)
        phi_k = torch.concatenate([_softmax(pk, axis=-1), _softmax(-pk, axis=-1)], axis=-1)
        kv = torch.einsum("bhsf,bhsd->bhfd", phi_k, v)
        z  = torch.einsum("bhsf->bhf", phi_k)
        num = torch.einsum("bhqf,bhfd->bhqd", phi_q, kv)
        den = torch.einsum("bhqf,bhf->bhq", phi_q, z)[..., None] + 1e-6
        o = num / den
        return (lambda x, *d: x.permute(*d))(o, 0, 2, 1, 3).reshape(B, S, D) @ wo

    def _B_ref(x, w_r, w_gate, w_up, w_down):
        B, S, D = x.shape
        C = 1
        r = x @ w_r
        idx = torch.argsort(-r, axis=-1)[:, :C]
        idx_e = (lambda x, n, axis: torch.repeat_interleave(x, n, dim=axis))(idx.reshape(B, C, 1), D, axis=2)
        x_sel = (lambda x, i, axis: torch.take_along_dim(x, i, dim=axis))(x, idx_e, axis=1)
        g = _silu(x_sel @ w_gate)
        u = x_sel @ w_up
        y = (g * u) @ w_down
        r_sel = _sigmoid((lambda x, i, axis: torch.take_along_dim(x, i, dim=axis))(r, idx, axis=1))
        y = y * r_sel.reshape(B, C, 1)
        b_off = (torch.arange(B) * S).reshape(B, 1)
        flat_idx = (idx + b_off).reshape(B * C)
        zero = (x * 0.0).reshape(B * S, D)
        upd = _scatter_add(zero, flat_idx, y.reshape(B * C, D), axis=0)
        return x + upd.reshape(B, S, D)

    x = A_x
    xn = _layernorm(x, axis=-1)
    s = _A_ref(xn, A_wqkv, A_w_phi, A_wo)
    g = _silu(_B_ref(xn, B_w_r, B_w_gate, B_w_up, B_w_down))
    return x + s * g
```

```cpp
#include <hip/hip_runtime.h>
#include <stdint.h>
#include <utility>

typedef __attribute__((ext_vector_type(8))) short bf16x8;
typedef __attribute__((ext_vector_type(4))) short s16x4;
typedef __attribute__((ext_vector_type(4))) float f32x4;
typedef __attribute__((ext_vector_type(2))) float f32x2;
typedef __attribute__((ext_vector_type(4))) unsigned u32x4;
typedef __attribute__((ext_vector_type(2))) unsigned u32x2;
typedef __attribute__((ext_vector_type(2))) __bf16 bfx2;
typedef unsigned short u16;

#define DI __device__ __forceinline__
#define MFMA(a, b, c) __builtin_amdgcn_mfma_f32_16x16x32_bf16((a), (b), (c), 0, 0, 0)
#define SB() __builtin_amdgcn_sched_barrier(0)
#define IC(x) std::integral_constant<int, (x)>{}

DI unsigned pk2(float a, float b) { f32x2 v = {a, b}; return __builtin_bit_cast(unsigned, __builtin_convertvector(v, bfx2)); }
DI float bflo(unsigned u) { return __uint_as_float(u << 16); }
DI float bfhi(unsigned u) { return __uint_as_float(u & 0xffff0000u); }
DI float rbf(float x) { return (float)(__bf16)x; }
DI f32x2 rb2(f32x2 v) { return f32x2{__uint_as_float(pk2(0.f, v[0])), __uint_as_float(pk2(0.f, v[1]))}; }
DI bf16x8 cat8(s16x4 lo, s16x4 hi) { return __builtin_shufflevector(lo, hi, 0, 1, 2, 3, 4, 5, 6, 7); }
DI bf16x8 as8(u32x4 v) { return __builtin_bit_cast(bf16x8, v); }
template <int... I, class F> DI void static_for_impl(std::integer_sequence<int, I...>, F&& f) { (f(std::integral_constant<int, I>{}), ...); }
template <int N, class F> DI void static_for(F&& f) { static_for_impl(std::make_integer_sequence<int, N>{}, f); }
template <int N> DI void waitv() { asm volatile("s_waitcnt vmcnt(%0)" ::"n"(N) : "memory"); }
#define RAW_BARRIER() do { asm volatile("s_waitcnt lgkmcnt(0)" ::: "memory"); __builtin_amdgcn_s_barrier(); asm volatile("" ::: "memory"); } while (0)
#define TICK() do { SB(); RAW_BARRIER(); SB(); } while (0)
#define DS_B128(dst, addr, off) asm volatile("ds_read_b128 %0, %1 offset:%2" : "=v"(dst) : "v"(addr), "n"(off))
#define DS_TR(dst, addr, off) asm volatile("ds_read_b64_tr_b16 %0, %1 offset:%2" : "=v"(dst) : "v"(addr), "n"(off))
#define LDS_OFF(p) ((unsigned)(uintptr_t)(__attribute__((address_space(3))) const char*)(p))
enum { P_DEF = 0, P_SC1 = 1, P_NT = 2 };
template <int OFF, int POL> DI void gl4(u32x4& d, unsigned voff, const char* sb) {
  if constexpr (POL == P_SC1) asm volatile("global_load_dwordx4 %0, %1, %2 offset:%3 sc1" : "=v"(d) : "v"(voff), "s"(sb), "n"(OFF));
  else if constexpr (POL == P_NT) asm volatile("global_load_dwordx4 %0, %1, %2 offset:%3 nt" : "=v"(d) : "v"(voff), "s"(sb), "n"(OFF));
  else asm volatile("global_load_dwordx4 %0, %1, %2 offset:%3" : "=v"(d) : "v"(voff), "s"(sb), "n"(OFF));
}
#define GL4(dst, voff, sb, off, pol) gl4<off, pol>(dst, voff, sb)
#define GL2(dst, voff, sb, off) asm volatile("global_load_dwordx2 %0, %1, %2 offset:%3" : "=v"(dst) : "v"(voff), "s"(sb), "n"(off))
#define GL1(dst, voff, sb, off) asm volatile("global_load_dword %0, %1, %2 offset:%3 sc1" : "=v"(dst) : "v"(voff), "s"(sb), "n"(off))
#define GLH(dst, voff, sb, off) asm volatile("global_load_ushort %0, %1, %2 offset:%3" : "=v"(dst) : "v"(voff), "s"(sb), "n"(off))
DI void bl4nt(u32x4& d, unsigned voff, u32x4 rs, unsigned soff) {
  asm volatile("buffer_load_dwordx4 %0, %1, %2, %3 offen nt" : "=v"(d) : "v"(voff), "s"(rs), "s"(soff));
}
DI void mfma_t(f32x4& c, bf16x8 a, u32x4 bb) { asm volatile("v_mfma_f32_16x16x32_bf16 %0, %1, %2, %0" : "+v"(c) : "v"(a), "v"(bb)); }
template <int N> DI void tie8(f32x4 (&a)[2][4]) {
  asm volatile("s_nop %8" : "+v"(a[0][0]), "+v"(a[0][1]), "+v"(a[0][2]), "+v"(a[0][3]), "+v"(a[1][0]), "+v"(a[1][1]), "+v"(a[1][2]), "+v"(a[1][3]) : "n"(N));
}
template <int OFF> DI void ds_tr(s16x4& d, unsigned a) { DS_TR(d, a, OFF); }
template <int N> DI void waitv4(u32x4 (&v)[4]) { asm volatile("s_waitcnt vmcnt(%4)" : "+v"(v[0]), "+v"(v[1]), "+v"(v[2]), "+v"(v[3]) : "n"(N)); }
template <int N> DI void waitl8(s16x4 (&a)[4], s16x4 (&c)[4]) {
  asm volatile("s_waitcnt lgkmcnt(%8)" : "+v"(a[0]), "+v"(c[0]), "+v"(a[1]), "+v"(c[1]), "+v"(a[2]), "+v"(c[2]), "+v"(a[3]), "+v"(c[3]) : "n"(N));
}
DI void gs4(unsigned voff, const char* sb, u32x4 v) { asm volatile("global_store_dwordx4 %0, %1, %2 sc1\n\ts_nop 1" :: "v"(voff), "v"(v), "s"(sb) : "memory"); }
DI void gs1(unsigned voff, const char* sb, unsigned v) { asm volatile("global_store_dword %0, %1, %2 sc1" :: "v"(voff), "v"(v), "s"(sb) : "memory"); }
DI void glds(unsigned voff, const char* sbase, unsigned lds) {
  asm volatile("s_mov_b32 m0, %2\n\ts_nop 0\n\tglobal_load_lds_dwordx4 %0, %1" :: "v"(voff), "s"(sbase), "s"(lds) : "memory");
}
DI void gldsn(unsigned voff, const char* sbase, unsigned lds) {
  asm volatile("s_mov_b32 m0, %2\n\ts_nop 0\n\tglobal_load_lds_dwordx4 %0, %1 nt" :: "v"(voff), "s"(sbase), "s"(lds) : "memory");
}
DI void touch(unsigned voff, const char* sbase, unsigned lds) {
  asm volatile("s_mov_b32 m0, %2\n\ts_nop 0\n\tglobal_load_lds_dword %0, %1" :: "v"(voff), "s"(sbase), "s"(lds) : "memory");
}
DI void touch16(unsigned voff, const char* sbase, unsigned lds) {
  asm volatile("s_mov_b64 exec, 0xffff\n\ts_mov_b32 m0, %2\n\ts_nop 0\n\tglobal_load_lds_dword %0, %1\n\ts_mov_b64 exec, -1" :: "v"(voff), "s"(sbase), "s"(lds) : "memory");
}
DI float silu_f(float x) { return x / (1.0f + __expf(-x)); }
DI float silu_r(float x) { return x * __builtin_amdgcn_rcpf(1.0f + __expf(-x)); }
DI float dot2(unsigned a, unsigned b, float c) { return __builtin_amdgcn_fdot2_f32_bf16(__builtin_bit_cast(bfx2, a), __builtin_bit_cast(bfx2, b), c, false); }

template <int OFF> DI void gs4o(unsigned voff, const char* sb, u32x4 v) {
  asm volatile("global_store_dwordx4 %0, %1, %2 offset:%3 sc1\n\ts_nop 1" :: "v"(voff), "v"(v), "s"(sb), "n"(OFF) : "memory");
}
DI void gs4m(unsigned voff, const char* sb, u32x4 v) {
  asm volatile("s_mov_b64 exec, 0xff\n\tglobal_store_dwordx4 %0, %1, %2 sc1\n\ts_mov_b64 exec, -1\n\ts_nop 0" :: "v"(voff), "v"(v), "s"(sb) : "memory");
}
DI void gs2(unsigned voff, const char* sb, u32x2 v) { asm volatile("global_store_dwordx2 %0, %1, %2 sc1" :: "v"(voff), "v"(v), "s"(sb) : "memory"); }
#define GL1P(dst, voff, sb) asm volatile("global_load_dword %0, %1, %2" : "=v"(dst) : "v"(voff), "s"(sb))
DI void ldsr4(u32x4 (&v)[4], unsigned a) {
  asm volatile("ds_read_b128 %0, %4\n\tds_read_b128 %1, %4 offset:1024\n\tds_read_b128 %2, %4 offset:2048\n\tds_read_b128 %3, %4 offset:3072\n\ts_waitcnt lgkmcnt(0)"
               : "=&v"(v[0]), "=&v"(v[1]), "=&v"(v[2]), "=&v"(v[3]) : "v"(a) : "memory");
}
DI u32x4 ldsr1(unsigned a) { u32x4 v; asm volatile("ds_read_b128 %0, %1\n\ts_waitcnt lgkmcnt(0)" : "=v"(v) : "v"(a) : "memory"); return v; }
DI void ldsw1(unsigned a, u32x4 v) { asm volatile("ds_write_b128 %0, %1" :: "v"(a), "v"(v) : "memory"); }
DI unsigned umax3(unsigned a, unsigned b, unsigned c) { return max(max(a, b), c); }
DI unsigned nosent(unsigned v) { return min(v, 0xfffffffeu); }

constexpr int DM = 1024, FF = 3584;
constexpr unsigned WS_ST = 4096;
constexpr unsigned WS_CA = WS_ST + 2048 * 8;
constexpr unsigned WS_CS = WS_CA + 256 * 16;
constexpr unsigned WS_KVP = WS_CS + 256 * 1024;
constexpr unsigned WS_ZP = WS_KVP + 2048 * 1024;
constexpr unsigned WS_KVR = WS_ZP + 1024 * 128;
constexpr unsigned WS_O = WS_KVR + 16 * 8192;
constexpr unsigned WS_GU = WS_O + 4096 * 1024;
constexpr unsigned WS_Y = WS_GU + 56 * 4096;

constexpr int ABYTES = 16384, WSLAB = 8192, SLOT = 40960;
constexpr int L_CA = 3 * SLOT;
constexpr int L_STG = L_CA + 4096;
constexpr int L_REC = L_STG + 1024;
constexpr int L_S = 131072;
constexpr int L_Z = L_S + 16384 + 8192;
constexpr int L_WP = L_Z + 256;
constexpr int L_MN = L_WP + 4096;
constexpr int L_RS = L_MN + 512;
constexpr int L_DN = L_RS + 512;
constexpr int L_C = L_DN + 256;
constexpr int L_H = L_C + 768;
constexpr int L_GI = L_H + 1024;
constexpr int L_F = L_GI + 128;
constexpr int L_TB = L_F + 320;
constexpr int LDSM = L_TB + 192;
constexpr int F_KV = 0, F_WO = 4, F_PG = 8, F_X = 16, F_T = 24, F_GT = 25, F_SEL = 28, F_BL = 32, F_IM = 36, F_EX = 40, F_ST = 44, F_SD = 48, F_CA = 52, F_OS = 60, F_GO = 64, F_AL = 68, F_SL = 72, F_BQ = 76;
#define SPINV (1u << 20)
constexpr unsigned SPIN = SPINV;
DI void pin1(float& v) { asm volatile("" : "+v"(v)); }
DI void pinf(f32x4& v) { asm volatile("" : "+v"(v)); }
DI void pin2(u32x4 (&v)[2]) { asm volatile("" : "+v"(v[0]), "+v"(v[1])); }
constexpr float L2E = 1.4426950408889634f;

template <int CTRL, int RMASK> DI float dpp_add(float v) {
  return v + __builtin_bit_cast(float, __builtin_amdgcn_update_dpp(0, __builtin_bit_cast(int, v), CTRL, RMASK, 0xf, true));
}
DI float wave_total(float v) {
  v = dpp_add<0x111, 0xf>(v); v = dpp_add<0x112, 0xf>(v); v = dpp_add<0x114, 0xf>(v); v = dpp_add<0x118, 0xf>(v);
  v = dpp_add<0x142, 0xa>(v); v = dpp_add<0x143, 0xc>(v);
  return __builtin_bit_cast(float, __builtin_amdgcn_readlane(__builtin_bit_cast(int, v), 63));
}
template <int CTRL, int RMASK> DI float dpp_max(float v) {
  return fmaxf(v, __builtin_bit_cast(float, __builtin_amdgcn_update_dpp(__builtin_bit_cast(int, v), __builtin_bit_cast(int, v), CTRL, RMASK, 0xf, false)));
}
DI float wave_max(float v) {
  v = dpp_max<0x111, 0xf>(v); v = dpp_max<0x112, 0xf>(v); v = dpp_max<0x114, 0xf>(v); v = dpp_max<0x118, 0xf>(v);
  v = dpp_max<0x142, 0xa>(v); v = dpp_max<0x143, 0xc>(v);
  return __builtin_bit_cast(float, __builtin_amdgcn_readlane(__builtin_bit_cast(int, v), 63));
}
DI f32x2 sw32(float a, float b) {
  auto r = __builtin_amdgcn_permlane32_swap(__float_as_uint(a), __float_as_uint(b), false, false);
  return f32x2{__uint_as_float(r[0]), __uint_as_float(r[1])};
}
DI f32x2 sw16(float a, float b) {
  auto r = __builtin_amdgcn_permlane16_swap(__float_as_uint(a), __float_as_uint(b), false, false);
  return f32x2{__uint_as_float(r[0]), __uint_as_float(r[1])};
}
DI float ror8(float v) { return __builtin_bit_cast(float, __builtin_amdgcn_update_dpp(0, __builtin_bit_cast(int, v), 0x128, 0xf, 0xf, true)); }
DI float sum_g(float v) {
  f32x2 a = sw32(v, v); v = a[0] + a[1];
  a = sw16(v, v); return a[0] + a[1];
}
DI unsigned tag12(float v, unsigned t) { return ((__float_as_uint(v) + 0x800u) & 0xfffff000u) | t; }
DI float untag12(unsigned u) { return __uint_as_float(u & 0xfffff000u); }
DI unsigned bad4(u32x4 v, unsigned t) { return (v[0] ^ t) | (v[1] ^ t) | (v[2] ^ t) | (v[3] ^ t); }
DI bool all0(unsigned v) { return __ballot(v != 0u) == 0ull; }
DI void lds_set(unsigned* f, unsigned v, int l) {
  asm volatile("s_waitcnt lgkmcnt(0)" ::: "memory");
  if (l == 0) __hip_atomic_store(f, v, __ATOMIC_RELAXED, __HIP_MEMORY_SCOPE_WORKGROUP);
}
DI unsigned lds_get(unsigned* f) { return __builtin_amdgcn_readfirstlane(__hip_atomic_load(f, __ATOMIC_RELAXED, __HIP_MEMORY_SCOPE_WORKGROUP)); }
DI void lds_wait4(unsigned* f) {
#pragma unroll 1
  for (unsigned spins = 0; spins < SPIN; ++spins) {
    u32x4 v; asm volatile("ds_read_b128 %0, %1\n\ts_waitcnt lgkmcnt(0)" : "=v"(v) : "v"(LDS_OFF(f)) : "memory");
    if (__builtin_amdgcn_readfirstlane(v[0] & v[1] & v[2] & v[3])) break;
    __builtin_amdgcn_s_sleep(1);
  }
}

template <int NC> struct Frag {
  bf16x8 af[4]; s16x4 wl[NC], wh[NC];
  DI void wait() {
    if constexpr (NC == 4)
      asm volatile("s_waitcnt lgkmcnt(0)" : "+v"(af[0]), "+v"(af[1]), "+v"(af[2]), "+v"(af[3]), "+v"(wl[0]), "+v"(wh[0]), "+v"(wl[1]), "+v"(wh[1]),
                   "+v"(wl[2]), "+v"(wh[2]), "+v"(wl[3]), "+v"(wh[3]));
    else
      asm volatile("s_waitcnt lgkmcnt(0)" : "+v"(af[0]), "+v"(af[1]), "+v"(af[2]), "+v"(af[3]), "+v"(wl[0]), "+v"(wh[0]), "+v"(wl[1]), "+v"(wh[1]));
  }
};
template <int OFF> DI void ds_128(bf16x8& d, unsigned a) { DS_B128(d, a, OFF); }
DI void lds_wait2(unsigned* f) {
#pragma unroll 1
  for (unsigned spins = 0; spins < SPIN; ++spins) {
    u32x2 v; asm volatile("ds_read_b64 %0, %1\n\ts_waitcnt lgkmcnt(0)" : "=v"(v) : "v"(LDS_OFF(f)) : "memory");
    if (__builtin_amdgcn_readfirstlane(v[0] & v[1])) break;
    __builtin_amdgcn_s_sleep(1);
  }
}
DI void reload4(u32x4 (&p)[4], unsigned o, const char* sb, unsigned long long m0, unsigned long long m1, unsigned long long m2, unsigned long long m3) {
  asm volatile("s_mov_b64 exec, %9\n\tglobal_load_dwordx4 %0, %4, %8 sc1\n\ts_mov_b64 exec, %10\n\tglobal_load_dwordx4 %1, %5, %8 sc1\n\t"
               "s_mov_b64 exec, %11\n\tglobal_load_dwordx4 %2, %6, %8 sc1\n\ts_mov_b64 exec, %12\n\tglobal_load_dwordx4 %3, %7, %8 sc1\n\t"
               "s_mov_b64 exec, -1\n\ts_waitcnt vmcnt(0)"
               : "+v"(p[0]), "+v"(p[1]), "+v"(p[2]), "+v"(p[3])
               : "v"(o), "v"(o + 4096u), "v"(o + 8192u), "v"(o + 12288u), "s"(sb), "s"(m0), "s"(m1), "s"(m2), "s"(m3) : "memory");
}
DI void reload2(u32x4 (&p)[2], unsigned o, const char* sb, unsigned long long m0, unsigned long long m1) {
  asm volatile("s_mov_b64 exec, %4\n\tglobal_load_dwordx4 %0, %2, %3 sc1\n\ts_mov_b64 exec, %5\n\tglobal_load_dwordx4 %1, %2, %3 offset:1024 sc1\n\t"
               "s_mov_b64 exec, -1\n\ts_waitcnt vmcnt(0)"
               : "+v"(p[0]), "+v"(p[1]) : "v"(o), "s"(sb), "s"(m0), "s"(m1) : "memory");
}

template <int CTRL> DI unsigned dpp_umax(unsigned v) { return max(v, (unsigned)__builtin_amdgcn_update_dpp((int)v, (int)v, CTRL, 0xf, 0xf, false)); }
template <int CTRL, int RMASK> DI unsigned dpp_umax_r(unsigned v) { return max(v, (unsigned)__builtin_amdgcn_update_dpp((int)v, (int)v, CTRL, RMASK, 0xf, false)); }
DI void lds_wait1(unsigned* f) {
#pragma unroll 1
  for (unsigned spins = 0; spins < SPIN && lds_get(f) == 0u; ++spins) __builtin_amdgcn_s_sleep(1);
  asm volatile("" ::: "memory");
}

DI void cs_read(u32x2 (&c)[4], unsigned a) {
  asm volatile("ds_read_b64_tr_b16 %0, %4\n\tds_read_b64_tr_b16 %1, %4 offset:512\n\tds_read_b64_tr_b16 %2, %4 offset:4096\n\tds_read_b64_tr_b16 %3, %4 offset:4608"
               : "=&v"(c[0]), "=&v"(c[1]), "=&v"(c[2]), "=&v"(c[3]) : "v"(a) : "memory");
}
DI void cs_sum(u32x2 (&c)[4], float& s0, float& s1) {
  asm volatile("s_waitcnt lgkmcnt(0)" : "+v"(c[0]), "+v"(c[1]), "+v"(c[2]), "+v"(c[3]) :: "memory");
#pragma unroll
  for (int i = 0; i < 4; ++i) { s0 = dot2(c[i][0], 0x3f803f80u, s0); s1 = dot2(c[i][1], 0x3f803f80u, s1); }
  asm volatile("" : "+v"(s0), "+v"(s1));
}
#define AB 0
#define QD 0
#define STE 0
#define PPD 0
#define PPS (-1)
#define NTX 0
#define NTW 0
#define KTM 0
#define STI 0
#define STD 2
#define TCH 1
#define PFI 0
#define PFB 0
#define PFS 0
#define HK 0
#define HSL 0
#define HSS 2
#define HSP 7
#define BLP 0
#define HS2 0
#define POW 0
#define WOE 1
#define ROT 1
#define RM 0
#define WIN 10
#define IOP 3
#define PF 12
#define C3 1
#define OCX 1
#define WD 1
#define BPR 0
#define ASL 0
#define WOP 3
#define BW 0
#define BQ 3
#define VL 1
#define NF 0
#define NG 4
#define NH 8
#define GB 3
#define WOR 1
#define WINO WIN
#define PE 3
#define PP1 12
#define PP2 0
#define PP3 16
#define BQP 1
#define WOL 1
#define POE 3
#define WLT 0
#define WTR 0
#define ASP (NS / 2)
#define BPH 0
#define BPE 1
#define FPE 0
#define FPG 3
#define BQ2 0
#define PYW 1
#define PPY 12
#define PPG 0
#define FP0 1
#define FPR 2
#define PD1 8
#define PD3 6
#define PD2 0
#define GAS 11
#define SS0 5
#define SPS 7
#define NFX 8
#define SB0 2
#define PBD 3
#define SBW 1
#define SBI 3
#define SBP 5
static_assert(PBD >= 2 && SBI >= 0 && SBI <= 9 && SBP > SBI && SBP >= 3 && SBP <= 11, "");
constexpr int PUB0 = SBW ? SBP : SS0 + (7 + SPS - 1) / SPS + 2, PUB = PUB0 > SB0 + PBD ? PUB0 : SB0 + PBD;
constexpr int n_se(int S) { return (SBW || S < SS0) ? 0 : ((S - SS0 + 1) * SPS < 7 ? (S - SS0 + 1) * SPS : 7); }
constexpr int n_x(int S) { return n_se(S) - n_se(S - 1) + (S == PUB) + (S == 0) + (S == GAS ? 2 : 0) + (S == 12) + (PPD && S >= PPS && S + 3 + PPD <= 15); }
static_assert(PUB >= 3 && PUB < 12 && NFX <= 16, "");
#define NS 3
DI void wphi_frag(bf16x8& w0, bf16x8& w1, unsigned a) {
  s16x4 t0, t1, t2, t3;
  asm volatile("ds_read_b64_tr_b16 %0, %4\n\tds_read_b64_tr_b16 %1, %4 offset:1024\n\tds_read_b64_tr_b16 %2, %4 offset:32\n\t"
               "ds_read_b64_tr_b16 %3, %4 offset:1056\n\ts_waitcnt lgkmcnt(0)" : "=&v"(t0), "=&v"(t1), "=&v"(t2), "=&v"(t3) : "v"(a) : "memory");
  w0 = cat8(t0, t1); w1 = cat8(t2, t3);
}
DI float mx2(float a, float b) { return __builtin_elementwise_maximum(a, b); }
DI float mx3(float a, float b, float c) { return __builtin_elementwise_maximum(__builtin_elementwise_maximum(a, b), c); }
DI void bcast2(float m, f32x2& c0, f32x2& c1) {
  const f32x2 a = sw16(m, m);
  c0 = sw32(a[0], a[0]); c1 = sw32(a[1], a[1]);
}
template <bool NRM, class F> DI void phi_map(const f32x4 (&p)[4][2], const float (&sc)[4], const float (&nrm)[2], F&& sink) {
  float r[4];
  f32x2 c[4];
#pragma unroll
  for (int t = 0; t < 4; ++t) {
    const float mx = mx2(mx3(mx3(p[t][0][0], p[t][0][1], p[t][0][2]), p[t][0][3], p[t][1][0]), mx3(p[t][1][1], p[t][1][2], p[t][1][3]));
    const float nm = mx2(mx3(mx3(-p[t][0][0], -p[t][0][1], -p[t][0][2]), -p[t][0][3], -p[t][1][0]), mx3(-p[t][1][1], -p[t][1][2], -p[t][1][3]));
    const f32x2 a = sw32(mx, nm);
    r[t] = mx2(a[0], a[1]);
  }
#pragma unroll
  for (int q = 0; q < 2; ++q) {
    const f32x2 a = sw16(r[2 * q], r[2 * q + 1]);
    bcast2(mx2(a[0], a[1]), c[2 * q], c[2 * q + 1]);
  }
#pragma unroll
  for (int t = 0; t < 4; ++t) c[t] *= -sc[t];
#pragma unroll
  for (int q = 0; q < 2; ++q) {
    f32x2 e[2][8], sm[2];
#pragma unroll
    for (int i = 0; i < 8; ++i)
#pragma unroll
      for (int j = 0; j < 2; ++j) {
        const float v = p[2 * q + j][i >> 2][i & 3];
        const f32x2 a = __builtin_elementwise_fma(f32x2{v, v}, f32x2{sc[2 * q + j], -sc[2 * q + j]}, c[2 * q + j]);
        e[j][i] = f32x2{__builtin_amdgcn_exp2f(a[0]), __builtin_amdgcn_exp2f(a[1])};
      }
#pragma unroll
    for (int j = 0; j < 2; ++j) {
      sm[j] = ((e[j][0] + e[j][1]) + (e[j][2] + e[j][3])) + ((e[j][4] + e[j][5]) + (e[j][6] + e[j][7]));
      sm[j] = sw32(sm[j][0], sm[j][1]);
    }
    const f32x2 a = sw16(sm[0][0] + sm[0][1], sm[1][0] + sm[1][1]);
    float rc = __builtin_amdgcn_rcpf(a[0] + a[1]);
    if constexpr (NRM) rc *= nrm[q];
    bcast2(rc, sm[0], sm[1]);
#pragma unroll
    for (int j = 0; j < 2; ++j) {
      unsigned pe[8];
#pragma unroll
      for (int i = 0; i < 8; ++i) e[j][i] *= sm[j];
#pragma unroll
      for (int i = 0; i < 4; ++i) { pe[i] = pk2(e[j][2 * i][0], e[j][2 * i + 1][0]); pe[4 + i] = pk2(e[j][2 * i][1], e[j][2 * i + 1][1]); }
      sink(2 * q + j, pe);
    }
  }
}

struct Sel { unsigned e[4], o[4]; };
DI Sel make_sel(int l) {
  Sel s;
#pragma unroll
  for (int q = 0; q < 4; ++q) {
    const bool on = ((l & 7) >> 1) == q;
    s.e[q] = on ? ((l & 1) ? 0x01000c0cu : 0x0c0c0100u) : 0x0c0c0c0cu;
    s.o[q] = on ? ((l & 1) ? 0x03020c0cu : 0x0c0c0302u) : 0x0c0c0c0cu;
  }
  return s;
}
DI f32x4 gemv2(unsigned xp, u32x4 w0, u32x4 w1, const Sel& s, f32x4 d) {
  const u32x4 a0 = {__builtin_amdgcn_perm(xp, xp, s.e[0]), __builtin_amdgcn_perm(xp, xp, s.e[1]), __builtin_amdgcn_perm(xp, xp, s.e[2]), __builtin_amdgcn_perm(xp, xp, s.e[3])};
  const u32x4 a1 = {__builtin_amdgcn_perm(xp, xp, s.o[0]), __builtin_amdgcn_perm(xp, xp, s.o[1]), __builtin_amdgcn_perm(xp, xp, s.o[2]), __builtin_amdgcn_perm(xp, xp, s.o[3])};
  return MFMA(as8(a1), as8(w1), MFMA(as8(a0), as8(w0), d));
}
DI u32x4 gemv_fin(f32x4 d, unsigned t12) {
  u32x4 v;
#pragma unroll
  for (int e = 0; e < 4; ++e) { const float t = ror8(d[e]); v[e] = tag12(d[e] + sw32(t, t)[1], t12); }
  return v;
}

DI unsigned poll_o(unsigned po, const char* ob, unsigned m) {
  unsigned om = 0;
#pragma unroll 1
  for (unsigned spins = 0; spins < SPIN; ++spins) {
    unsigned f;
    asm volatile("global_load_dword %0, %1, %2 sc1\n\ts_waitcnt vmcnt(0)" : "=v"(f) : "v"(po), "s"(ob) : "memory");
    om = (unsigned)__ballot(f != ~0u);
    om &= om >> 16;
    if ((om & m) == m) break;
  }
  return om;
}
DI void opair(u32x4 (&o)[2], unsigned v0, unsigned v1, const char* p) { GL4(o[0], v0, p, 0, P_SC1); GL4(o[1], v1, p, 0, P_SC1); }
template <int N, bool COLD = false> DI void ocheck(u32x4 (&o)[2], unsigned v0, unsigned v1, const char* p) {
  asm volatile("s_waitcnt vmcnt(%2)" : "+v"(o[0]), "+v"(o[1]) : "n"(N));
  if (COLD && __builtin_expect(__ballot(max(o[0][3], o[1][3]) == ~0u) == 0ull, 1)) return;
#pragma unroll 1
  for (unsigned spins = 0; spins < SPIN && __ballot(max(o[0][3], o[1][3]) == ~0u) != 0ull; ++spins)
    asm volatile("s_sleep 2\n\tglobal_load_dwordx4 %0, %2, %4 sc1\n\tglobal_load_dwordx4 %1, %3, %4 sc1\n\ts_waitcnt vmcnt(0)"
                 : "+v"(o[0]), "+v"(o[1]) : "v"(v0), "v"(v1), "s"(p) : "memory");
}
DI void sw16u(unsigned& a, unsigned& b) { auto r = __builtin_amdgcn_permlane16_swap(a, b, false, false); a = r[0]; b = r[1]; }
template <int N> DI void waitl2(s16x4& a, s16x4& c) { asm volatile("s_waitcnt lgkmcnt(%2)" : "+v"(a), "+v"(c) : "n"(N)); }
template <int N> DI void tie4(f32x4 (&a)[4]) { asm volatile("s_nop %4" : "+v"(a[0]), "+v"(a[1]), "+v"(a[2]), "+v"(a[3]) : "n"(N)); }
DI void rowst(f32x2 (&m)[4], unsigned a) {
  asm volatile("ds_read2_b32 %0, %4 offset1:128\n\tds_read2_b32 %1, %4 offset0:16 offset1:144\n\tds_read2_b32 %2, %4 offset0:32 offset1:160\n\t"
               "ds_read2_b32 %3, %4 offset0:48 offset1:176\n\ts_waitcnt lgkmcnt(0)" : "=&v"(m[0]), "=&v"(m[1]), "=&v"(m[2]), "=&v"(m[3]) : "v"(a) : "memory");
}
DI void reload4c(u32x4 (&p)[4], unsigned o, const char* sb, unsigned long long m0, unsigned long long m1, unsigned long long m2, unsigned long long m3) {
  asm volatile("s_mov_b64 exec, %6\n\tglobal_load_dwordx4 %0, %4, %5 sc1\n\ts_mov_b64 exec, %7\n\tglobal_load_dwordx4 %1, %4, %5 offset:1024 sc1\n\t"
               "s_mov_b64 exec, %8\n\tglobal_load_dwordx4 %2, %4, %5 offset:2048 sc1\n\ts_mov_b64 exec, %9\n\tglobal_load_dwordx4 %3, %4, %5 offset:3072 sc1\n\t"
               "s_mov_b64 exec, -1\n\ts_waitcnt vmcnt(0)"
               : "+v"(p[0]), "+v"(p[1]), "+v"(p[2]), "+v"(p[3]) : "v"(o), "s"(sb), "s"(m0), "s"(m1), "s"(m2), "s"(m3) : "memory");
}

#define PL(r, o) "global_load_dwordx4 v[" #r ":" #r "+3], %[vo], %[sb] offset:" #o " sc1\n\t"
#define PC(r, m) "v_cmp_eq_u32 " m ", -1, v[" #r "+3]\n\t"
#define PM(d, r) "v_mov_b32 v[" #d "], v[" #r "]\n\tv_mov_b32 v[" #d "+1], v[" #r "+1]\n\tv_mov_b32 v[" #d "+2], v[" #r "+2]\n\tv_mov_b32 v[" #d "+3], v[" #r "+3]\n\t"
template <int D> DI void poll2(u32x4 (&p)[2], u32x4 (&q)[4], unsigned vo, const char* sb) {
  unsigned long long m; unsigned n = SPIN;
  asm volatile(PL(128, 0) PL(132, 1024) "s_sleep %[d]\n\t" PL(136, 0) PL(140, 1024)
               "1:\n\ts_waitcnt vmcnt(2)\n\t" PC(128, "vcc") PC(132, "%[m]") "s_or_b64 exec, vcc, %[m]\n\ts_cbranch_scc0 2f\n\t" PL(128, 0) PL(132, 1024)
               "s_mov_b64 exec, -1\n\ts_waitcnt vmcnt(2)\n\t" PC(136, "vcc") PC(140, "%[m]") "s_or_b64 exec, vcc, %[m]\n\ts_cbranch_scc0 3f\n\t" PL(136, 0) PL(140, 1024)
               "s_mov_b64 exec, -1\n\ts_sub_u32 %[n], %[n], 1\n\ts_cbranch_scc0 1b\n\ts_waitcnt vmcnt(0)\n"
               "3:\n\ts_mov_b64 exec, -1\n\t" PM(120, 136) PM(124, 140) "s_branch 4f\n"
               "2:\n\ts_mov_b64 exec, -1\n\t" PM(120, 128) PM(124, 132) "4:\n\ts_nop 1"
               : "={v[120:123]}"(p[0]), "={v[124:127]}"(p[1]), "=&{v[128:131]}"(q[0]), "=&{v[132:135]}"(q[1]), "=&{v[136:139]}"(q[2]), "=&{v[140:143]}"(q[3]),
                 [m] "=&s"(m), [n] "+s"(n)
               : [vo] "v"(vo), [sb] "s"(sb), [d] "n"(D) : "vcc", "scc", "memory");
}
#define PL4(r) PL(r, 0) PL(r+4, 1024) PL(r+8, 2048) PL(r+12, 3072)
#define PC4(r) "s_waitcnt vmcnt(4)\n\tv_max3_u32 v128, v[" #r "+3], v[" #r "+7], v[" #r "+11]\n\tv_max_u32 v128, v128, v[" #r "+15]\n\tv_cmp_eq_u32 vcc, -1, v128\n\ts_or_b64 exec, vcc, vcc\n\t"
#define PM2(o, r) "v_mov_b64 v[128+" #o ":129+" #o "], v[" #r "+" #o ":" #r "+1+" #o "]\n\t"
#define PM16(r) PM2(0, r) PM2(2, r) PM2(8, r) PM2(10, r) PM2(4, r) PM2(6, r) PM2(12, r) PM2(14, r)
#define PQ8(c) c "{v[96:99]}"(q[0]), c "{v[100:103]}"(q[1]), c "{v[104:107]}"(q[2]), c "{v[108:111]}"(q[3]), c "{v[112:115]}"(q[4]), c "{v[116:119]}"(q[5]), c "{v[120:123]}"(q[6]), c "{v[124:127]}"(q[7])
template <int D> DI void poll4a(u32x4 (&q)[8], unsigned vo, const char* sb) {
  asm volatile(PL4(96) "s_sleep %[d]\n\t" PL4(112) : PQ8("=&") : [vo] "v"(vo), [sb] "s"(sb), [d] "n"(D) : "memory");
}
DI void poll4b(u32x4 (&p)[4], u32x4 (&q)[8], unsigned vo, const char* sb) {
  unsigned n = SPIN;
  asm volatile("1:\n\t" PC4(96) "s_cbranch_scc0 2f\n\t" PL4(96) "s_mov_b64 exec, -1\n\t" PC4(112) "s_cbranch_scc0 3f\n\t" PL4(112)
               "s_mov_b64 exec, -1\n\ts_sub_u32 %[n], %[n], 1\n\ts_cbranch_scc0 1b\n\ts_waitcnt vmcnt(0)\n"
               "3:\n\ts_mov_b64 exec, -1\n\t" PM16(112) "s_branch 4f\n"
               "2:\n\ts_mov_b64 exec, -1\n\t" PM16(96) "4:\n\ts_nop 1"
               : "=&{v[128:131]}"(p[0]), "=&{v[132:135]}"(p[1]), "=&{v[136:139]}"(p[2]), "=&{v[140:143]}"(p[3]), PQ8("+"), [n] "+s"(n)
               : [vo] "v"(vo), [sb] "s"(sb) : "vcc", "scc", "memory");
}
#define PB(a, r) "v_bitop3_b32 " a ", " a ", v[" #r "], %[t] bitop3:0xf6\n\t"
#define PB7(a, r) "v_xor_b32 " a ", %[t], v[" #r "]\n\t" PB(a, r+1) PB(a, r+2) PB(a, r+3) PB(a, r+4) PB(a, r+5) PB(a, r+6) PB(a, r+7)
#define PCT(r) "s_waitcnt vmcnt(4)\n\t" PB7("v128", r) PB7("v129", r+8) "v_and_b32 v128, 0xfff, v128\n\tv_and_or_b32 v128, v129, %[tm], v128\n\tv_cmp_ne_u32 vcc, 0, v128\n\ts_or_b64 exec, vcc, vcc\n\t"
template <int D> DI void pollt(u32x4 (&p)[4], u32x4 (&q)[8], unsigned vo, const char* sb, unsigned t, unsigned tm) {
  unsigned n = SPIN;
  asm volatile(PL4(96) "s_sleep %[d]\n\t" PL4(112)
               "1:\n\t" PCT(96) "s_cbranch_scc0 2f\n\t" PL4(96) "s_mov_b64 exec, -1\n\t" PCT(112) "s_cbranch_scc0 3f\n\t" PL4(112)
               "s_mov_b64 exec, -1\n\ts_sub_u32 %[n], %[n], 1\n\ts_cbranch_scc0 1b\n\ts_waitcnt vmcnt(0)\n"
               "3:\n\ts_mov_b64 exec, -1\n\t" PM16(112) "s_branch 4f\n"
               "2:\n\ts_mov_b64 exec, -1\n\t" PM16(96) "4:\n\ts_nop 1"
               : "=&{v[128:131]}"(p[0]), "=&{v[132:135]}"(p[1]), "=&{v[136:139]}"(p[2]), "=&{v[140:143]}"(p[3]), PQ8("=&"), [n] "+s"(n)
               : [vo] "v"(vo), [sb] "s"(sb), [t] "s"(t), [tm] "s"(tm), [d] "n"(D) : "vcc", "scc", "memory");
}
#define PKEEP(pl) "v"(pl[0]), "v"(pl[1]), "v"(pl[2]), "v"(pl[3]), "v"(pl[4]), "v"(pl[5]), "v"(pl[6]), "v"(pl[7])
template <int D> DI void poll_o2(unsigned& a, unsigned& c, unsigned po, const char* ob, unsigned long long m) {
  unsigned n = SPIN;
  asm volatile("s_mov_b64 exec, %[m]\n\tglobal_load_dword %0, %[po], %[ob] sc1\n\ts_sleep %[d]\n\tglobal_load_dword %1, %[po], %[ob] sc1\n"
               "1:\n\ts_waitcnt vmcnt(1)\n\tv_cmp_eq_u32 vcc, -1, %0\n\ts_cbranch_vccz 2f\n\tglobal_load_dword %0, %[po], %[ob] sc1\n\t"
               "s_waitcnt vmcnt(1)\n\tv_cmp_eq_u32 vcc, -1, %1\n\ts_cbranch_vccz 2f\n\tglobal_load_dword %1, %[po], %[ob] sc1\n\t"
               "s_sub_u32 %[n], %[n], 1\n\ts_cbranch_scc0 1b\n"
               "2:\n\ts_mov_b64 exec, -1"
               : "=&v"(a), "=&v"(c), [n] "+s"(n) : [po] "v"(po), [ob] "s"(ob), [m] "s"(m), [d] "n"(D) : "vcc", "scc", "memory");
}
DI bool sent4(u32x4 v) { return max(umax3(v[0], v[1], v[2]), v[3]) == ~0u; }

__global__ __launch_bounds__(768) void kf_main(const u16* __restrict__ x, const u16* __restrict__ wqkv, char* ws, const u16* __restrict__ w_r,
                                               const u16* __restrict__ wphi, const u16* __restrict__ wo, const u16* __restrict__ w_gate,
                                               const u16* __restrict__ w_up, const u16* __restrict__ w_down, u16* __restrict__ out) {
  extern __shared__ __attribute__((aligned(16))) char smem[];
  const int b = blockIdx.x;
  const int xcd = b & 7, jj = b >> 3;
  const int panel = (xcd >> 2) * 8 + (jj & 7), hp = (xcd & 3) * 4 + (jj >> 3);
  const int tid = threadIdx.x;
  const int w = __builtin_amdgcn_readfirstlane(tid >> 6), lw = w & 3;
  int l = tid & 63, r15 = l & 15, g = l >> 4, tq = r15 >> 2, tp = l & 3;
  unsigned xo;
  auto relane = [&]() {
    asm volatile("v_mbcnt_lo_u32_b32 %0, -1, 0\n\tv_mbcnt_hi_u32_b32 %0, -1, %0" : "=v"(l));
    r15 = l & 15; g = l >> 4; tq = r15 >> 2; tp = l & 3; xo = r15 * 2048u + g * 16u;
  };
  const unsigned lbase = LDS_OFF(smem);
  auto pf3 = [&](int half) {
    touch(l * 6144u, (const char*)wqkv + (size_t)(half * 512 + (jj & 7) * 64) * 6144 + (jj >> 3) * 2048 + (xcd & 3) * 512 + 384, lbase + L_S + 20480);
  };
  const int kt0 = KTM == 1 ? 8 * (panel >> 3) : KTM == 2 ? 0 : 4 * (panel >> 2);
  auto pfi = [&](int S) {
    const int kt = (S + kt0) & 15;
    const int m = l & 31;
    const char* p = l < 32 ? (const char*)x + (size_t)(panel * 128 + (jj >> 3) * 32 + m) * 2048 + kt * 128
                           : (const char*)wqkv + (size_t)(kt * 64 + (jj & 7) * 8 + (m & 7)) * 6144 + min(m >> 3, 2) * 2048 + hp * 128;
    asm volatile("s_mov_b32 m0, %1\n\ts_nop 0\n\tglobal_load_lds_dword %0, off" :: "v"(p), "s"(lbase + L_S + 20480) : "memory");
  };
  if (__builtin_expect(w < 4, 1)) {
    if (!(AB & 16) && __builtin_expect(w >= 2, 1)) {
      const char* pc0; unsigned clen;
      asm volatile("s_getpc_b64 %0\n.Lkf_here:\n\ts_mov_b32 %1, .Lkf_last-.Lkf_here" : "=s"(pc0), "=s"(clen));
      touch(min(((jj * 2 + w) & 15) * 4096u + l * 64u, clen), pc0, lbase + L_S + 20480 + w * 256);
    }
    if (!(AB & 8) && (PF & 4) && __builtin_expect(w == 0 && !(xcd & 1) && jj < 24, 1)) pf3(xcd >> 2);
  }
  if (PFI && w >= 8 && lw < PFI) pfi(2 + lw);
  if (PFI > 4 && w >= 8 && lw + 4 < PFI) pfi(6 + lw);
  unsigned* flg = (unsigned*)(smem + L_F);
  const bf16x8 ones = {0x3f80, 0x3f80, 0x3f80, 0x3f80, 0x3f80, 0x3f80, 0x3f80, 0x3f80};
  const f32x4 z4 = {0.f, 0.f, 0.f, 0.f};
  const int row0 = panel * 128 + hp * 8;
  const unsigned oseq = (unsigned)(0x21076542107654ull >> (4 * (ROT == 1 ? (hp & 4) : ROT == 2 ? (hp & 4) + (hp & 1) : ROT == 3 ? 0 : (((hp - 4) & 7) + 1) % 7)));
  auto otile = [&](int u) { return u < 7 ? (int)((oseq >> (4 * u)) & 7u) : 3; };
  auto kvo = [&]() { return (l >> 3) * 2048u + (l & 7) * 16u + ((panel ^ (l >> 3)) & 15) * 128u; };
  if (__builtin_expect((w >> 2) == 1, 1)) {
    const int srow = l >> 3, sch = l & 7, cg = sch, rg = srow;
    unsigned G, Tw, t12;
    unsigned voA[2], voW[2];
    const char* sA = (const char*)(x + (size_t)panel * 128 * DM);
    const char* sQ = (const char*)(wqkv + hp * 64);
#pragma unroll
    for (int i = 0; i < 2; ++i) {
      const int row = lw * 32 + i * 8 + srow, wr = lw * 16 + i * 8 + srow;
      voA[i] = (unsigned)(row * 2048 + ((sch ^ ((row >> 1) & 7)) * 16));
      voW[i] = (unsigned)(wr * 6144 + ((sch ^ ((((wr >> 1) & 1) << 2) | (((wr >> 3) & 1) << 1))) * 16));
    }
    auto stage = [&](int S) {
      const int kt = (S + kt0) & 15;
      const char* a = sA + kt * 128; const char* wv = sQ + (size_t)kt * 64 * 6144;
      const unsigned d = lbase + (S % 3) * SLOT;
#pragma unroll
      for (int i = 0; i < 4; ++i) { if (S < NTX) gldsn(voA[i & 1], a + (i >> 1) * 32768, d + lw * 4096 + i * 1024); else glds(voA[i & 1], a + (i >> 1) * 32768, d + lw * 4096 + i * 1024); }
#pragma unroll
      for (int i = 0; i < 6; ++i) { if (S < NTW) gldsn(voW[i & 1], wv + ((i >> 1) == 2 ? 0 : ((i >> 1) + 1) * 2048), d + ABYTES + (i >> 1) * WSLAB + lw * 2048 + (i & 1) * 1024);
        else glds(voW[i & 1], wv + ((i >> 1) == 2 ? 0 : ((i >> 1) + 1) * 2048), d + ABYTES + (i >> 1) * WSLAB + lw * 2048 + (i & 1) * 1024); }
    };
    __builtin_amdgcn_s_setprio(IOP);
    stage(0); stage(1);
    GL1(G, 0u, (const char*)ws, 0);
    const bool gu = (hp & 7) != 3;
    const int bb = gu ? (hp - (hp > 3) - (hp > 11)) * 16 + panel : 0;
    const int ke = bb & 7, cb = ((bb >> 3) * 2 + (lw & 1)) ^ ((C3 & 1) ? (lw >> 1) * 4 : 0);
    const int kc = (C3 & 1) ? (bb >> 4) * 4 + ((bb >> 1) & 3) : bb >> 2, cq = (C3 & 1) ? ((bb >> 3) & 1) | ((bb & 1) << 1) : bb & 3;
    u32x4 gv[16], dv[8];
    const uintptr_t gb = (uintptr_t)(((lw >> 1) ? w_up : w_gate) + (size_t)(ke * 128) * FF + cb * 64);
    const u32x4 grs = {(unsigned)gb, (unsigned)(gb >> 32) & 0xffffu, gu ? 127u * FF * 2 + 128u : 0u, 0x00020000u};
    const unsigned go = rg * (16u * FF * 2) + cg * 16u;
    const uintptr_t db = (uintptr_t)(w_down + (size_t)(kc * 64) * DM + cq * 256 + lw * 64);
    const u32x4 drs = {(unsigned)db, (unsigned)(db >> 32) & 0xffffu, gu ? 63u * DM * 2 + 128u : 0u, 0x00020000u};
    const unsigned dof = rg * 16384u + cg * 16u;
    auto strm = [&](auto I0, auto I1, auto W) {
      static_for<(decltype(I1)::value > decltype(I0)::value ? decltype(I1)::value - decltype(I0)::value : 0)>([&](auto I) {
        constexpr int i = decltype(I)::value + decltype(I0)::value;
        if constexpr (decltype(W)::value) waitv<decltype(W)::value>();
        if constexpr (i < 16) bl4nt(gv[i], go, grs, i * (FF * 2u)); else bl4nt(dv[i - 16], dof, drs, (i - 16) * 2048u);
      });
    };
    const int wrow = 8 * g + tq, wsw = (((wrow >> 1) & 1) << 2) | (((wrow >> 3) & 1) << 1);
    const unsigned csa = lbase + ABYTES + (lw < 3 ? lw : 0) * WSLAB + wrow * 128 + (((2 * (panel & 3) + (tp >> 1)) ^ wsw) * 16) + 8 * (tp & 1);
    const int ppk = (lw < 2 || KTM) ? kt0 : (kt0 | 4);
    const unsigned pps = lw < 2 ? 128u : 64u * 6144u;
    const char* ppb = lw < 2 ? sA + ((jj >> 3) * 32 + (lw & 1) * 16) * 2048 : sQ + ((jj & 7) * 8 + (lw & 1) * 4) * 6144;
    const unsigned ppo = lw < 2 ? r15 * 2048u : (l & 3) * 6144u + min(tq, 2) * 2048u;
    waitv<11>();
    RAW_BARRIER();
    float mean = 0.f, var = 1.f, c0 = 0.f, c1 = 0.f;
    f32x4 sm = z4;
    const u32x4 ff = {~0u, ~0u, ~0u, ~0u};
    u32x4 ga[2], ca;
    const char* gab = ws + (lw < 3 ? WS_CS + (hp * 3 + lw) * 2048 : WS_ST + panel * 1024 - 1024);
    const unsigned go0 = l * 16u + (lw < 3 ? 0u : 1024u);
    static_for<14>([&](auto I) {
      constexpr int S = decltype(I)::value - 1;
      if constexpr (S >= 0) {
        waitv<10 + n_x(S - 2) + n_x(S - 1)>();
        RAW_BARRIER();
      }
      if constexpr (S < 3 && !(AB & 4)) {
        u32x2 c[4];
        cs_read(c, csa + ((S + 1) % 3) * SLOT);
        stage(S + 3);
        cs_sum(c, c0, c1);
      } else stage(S + 3);
      static_for<n_se(S) - n_se(S - 1)>([&](auto J) {
        constexpr int j = decltype(J)::value + n_se(S - 1);
        if constexpr (j < 4) gs4o<j * 1024>(l * 16u, ws + WS_O + (size_t)(panel * 4 + lw) * 65536 + hp * 4096, ff);
        else if constexpr (j < 6) gs4(kvo() ^ ((j - 4) * 17408u), ws + WS_KVP + (hp * 4 + lw) * 32768, ff);
        else gs4m(l * 16u, ws + WS_KVR + hp * 8192 + lw * 2048 + panel * 128, ff);
      });
      if constexpr (S == 0) {
        asm volatile("" : "+v"(G));
        Tw = __builtin_amdgcn_readfirstlane(G) + 1u;
        if ((Tw & 0xfffu) == 0u) ++Tw;
        t12 = Tw & 0xfffu;
        if (tid == 256) flg[F_T] = Tw;
      }
      if constexpr (S == SB0 && !(AB & 32)) {
        const f32x4* rp = (const f32x4*)(smem + L_REC) + (l & 7);
        sm = ((rp[0] + rp[8]) + (rp[16] + rp[24])) + ((rp[32] + rp[40]) + (rp[48] + rp[56]));
        pinf(sm);
      }
      if constexpr (S == SB0 + 1 && !(AB & 32)) {
        mean = sm[0] * (1.0f / 1024.0f);
        var = __builtin_fmaf(-mean, mean, sm[2] * (1.0f / 1024.0f));
        if (lw == 3 && __ballot(!(var * 64.0f >= mean * mean)) != 0ull) {
#pragma unroll 1
          for (int r = 0; r < 8; ++r) {
            const float m = __builtin_bit_cast(float, __builtin_amdgcn_readlane(__builtin_bit_cast(int, mean), r));
            const u32x4* p = (const u32x4*)(x + (size_t)(row0 + r) * DM) + l * 2;
            const u32x4 a = p[0], c = p[1];
            float q = 0.f;
#pragma unroll
            for (int i = 0; i < 8; ++i) {
              const unsigned u = i < 4 ? a[i & 3] : c[i & 3];
              const float e0 = bflo(u) - m, e1 = bfhi(u) - m;
              q = __builtin_fmaf(e0, e0, q); q = __builtin_fmaf(e1, e1, q);
            }
            q = wave_total(q) * (1.0f / 1024.0f);
            if ((l & 7) == r) var = q;
          }
        }
        mean = rbf(mean); var = rbf(rbf(var) + 1e-5f);
        pin1(mean); pin1(var);
      }
      if constexpr (S == SB0 + 2 && !(AB & 32)) { var = rbf(sqrtf(var)); pin1(var); }
      if constexpr (S == SB0 + (PBD < 3 ? 2 : 3) && !(AB & 32)) { sm[1] = rbf(__builtin_fmaf(-mean, sm[3], sm[1]) / var); pinf(sm); }
      if constexpr (S == PUB) {
        if (lw == 3) {
          const float r = sm[1];
          const unsigned u = __float_as_uint(r);
          const unsigned key = (r == r ? ((u & 0x80000000u) ? (~u & 0xffff0000u) : (u | 0x80000000u)) : 0u) | (unsigned)(2047 - row0 - (l & 7));
          const unsigned km = dpp_umax<0x141>(dpp_umax<0x4e>(dpp_umax<0xb1>(key)));
          const unsigned md = __float_as_uint(mean) | (__float_as_uint(var) >> 16);
          if (l < 8) {
            gs2(WS_ST + (row0 + l) * 8, ws, u32x2{md, Tw});
            if (key == km) gs4(WS_CA + (panel * 16 + hp) * 16, ws, u32x4{key, md, Tw, Tw});
          }
        } else {
          const float c = sum_g(c0 + c1);
          if (l < 16) gs2(WS_CS + (((hp * 3 + lw) * 4 + (panel >> 2)) * 64 + (panel & 3) * 16 + l) * 8, ws, u32x2{__float_as_uint(c), Tw});
        }
      }
      if constexpr (S == GAS) { GL4(ga[0], go0, gab, 0, P_SC1); GL4(ga[1], l * 16u, gab, 1024, P_SC1); }
      if constexpr (S == 0) glds(l * 16u, (const char*)(wphi + hp * 2048 + lw * 512), lbase + L_WP + lw * 1024);
      if constexpr (PPD && S >= PPS && S + 3 + PPD <= 15) {
        const int kt = (S + 3 + PPD + ppk) & 15;
        touch16(ppo, ppb + (size_t)kt * pps, lbase + L_S + 20480);
      }
    });
    GL4(ca, WS_CA + lw * 1024 + l * 16, ws, 0, P_SC1);
    strm(IC(0), IC(NF), IC(0));
    waitv<10 + n_x(11) + n_x(12) + NF>(); RAW_BARRIER();
    strm(IC(NF), IC(NF + NG), IC(0));
    auto ga_bad = [&]() { return (ga[0][1] ^ Tw) | (ga[0][3] ^ Tw) | (ga[1][1] ^ Tw) | (ga[1][3] ^ Tw); };
    auto ga_put = [&]() {
      if (lw < 3) {
        const float u0 = __uint_as_float(ga[0][0]) + __uint_as_float(ga[1][0]), u1 = __uint_as_float(ga[0][2]) + __uint_as_float(ga[1][2]);
        const f32x2 a = sw32(u0, u1);
        ((float*)(smem + L_C))[lw * 64 + (l & 31) * 2 + (l >> 5)] = a[0] + a[1];
      } else {
        *(f32x2*)(smem + L_MN + l * 8) = f32x2{-bfhi(ga[0][0]), -bfhi(ga[0][2])};
        *(f32x2*)(smem + L_RS + l * 8) = f32x2{__builtin_amdgcn_rcpf(bflo(ga[0][0])), __builtin_amdgcn_rcpf(bflo(ga[0][2]))};
        *(unsigned*)(smem + L_DN + l * 4) = (ga[0][0] & 0xffffu) | (ga[0][2] << 16);
      }
      lds_set(flg + F_ST + lw, 1u, l);
    };
    asm volatile("s_waitcnt vmcnt(%2)" : "+v"(ga[0]), "+v"(ga[1]) : "n"((GAS == 12 ? 1 : 11) + NF + NG) : "memory");
    if (all0(ga_bad())) {
      ga_put();
      waitv<1 + NF + NG>(); RAW_BARRIER();
    } else {
      GL4(ga[0], go0, gab, 0, P_SC1); GL4(ga[1], l * 16u, gab, 1024, P_SC1);
      waitv<3 + NF + NG>(); RAW_BARRIER();
      asm volatile("s_waitcnt vmcnt(0)" : "+v"(ga[0]), "+v"(ga[1]) :: "memory");
#pragma unroll 1
      for (unsigned spins = 0; spins < SPIN; ++spins) {
        if (all0(ga_bad())) break;
        asm volatile("global_load_dwordx4 %0, %2, %4 sc1\n\tglobal_load_dwordx4 %1, %3, %4 offset:1024 sc1\n\ts_waitcnt vmcnt(0)"
                     : "+v"(ga[0]), "+v"(ga[1]) : "v"(go0), "v"(l * 16u), "s"(gab) : "memory");
      }
      ga_put();
    }
    strm(IC(NF + NG), IC(NF + NG + NH), IC(0));
    asm volatile("s_waitcnt vmcnt(%1)" : "+v"(ca) : "n"(NF + NG + NH) : "memory");
    __builtin_amdgcn_s_setprio(FP0);
#pragma unroll 1
    for (unsigned spins = 0; spins < SPIN; ++spins) {
      if (all0((ca[2] ^ Tw) | (ca[3] ^ Tw))) break;
      asm volatile("global_load_dwordx4 %0, %1, %2 sc1\n\ts_waitcnt vmcnt(0)" : "+v"(ca) : "v"(WS_CA + lw * 1024 + l * 16), "s"(ws) : "memory");
    }
    {
      unsigned m = dpp_umax<0x140>(dpp_umax<0x141>(dpp_umax<0x4e>(dpp_umax<0xb1>(ca[0]))));
      m = dpp_umax_r<0x143, 0xc>(dpp_umax_r<0x142, 0xa>(m));
      m = __builtin_amdgcn_readlane(m, 63);
      if (ca[0] == m) *(u32x2*)(flg + F_CA + 2 * lw) = u32x2{m, ca[1]};
      lds_set(flg + F_SD + lw, 1u, l);
    }
    strm(IC(NF + NG + NH), IC(NFX), IC(WIN));
    const Sel sl = make_sel(l);
    unsigned voO[2];
#pragma unroll
    for (int i = 0; i < 2; ++i) voO[i] = (unsigned)((lw * 16 + i * 8 + srow) * 2048 + (sch ^ (((srow >> 1) & 1) | ((((srow >> 2) ^ i) & 1) << 2))) * 16);
    auto wotile = [&](int p) {
      const char* wv = (const char*)(wo + (size_t)((otile(p >> 1) + 8 * (p & 1)) * 64) * DM + hp * 64);
      waitv<WINO>();
      glds(voO[0], wv, lbase + p * 8192 + lw * 2048); glds(voO[1], wv, lbase + p * 8192 + lw * 2048 + 1024);
    };
    lds_wait4(flg + F_SD);
    u32x2 sel;
    {
      const u32x4 e0 = ldsr1(LDS_OFF(flg + F_CA)), e1 = ldsr1(LDS_OFF(flg + F_CA + 4));
      const bool p = e0[2] > e0[0], q = e1[2] > e1[0];
      const unsigned k0 = p ? e0[2] : e0[0], m0 = p ? e0[3] : e0[1], k1 = q ? e1[2] : e1[0], m1 = q ? e1[3] : e1[1];
      sel = k1 > k0 ? u32x2{k1, m1} : u32x2{k0, m0};
    }
    const unsigned key = __builtin_amdgcn_readfirstlane(sel[0]);
    const int idx = 2047 - (int)(key & 2047u);
    if (tid == 256) flg[F_SEL] = key;
    unsigned xd;
    GL1P(xd, l * 4u, (const char*)(x + (size_t)idx * DM + ke * 128));
    strm(IC(NFX > NF + NG + NH ? NFX : NF + NG + NH), IC(24), IC(WIN));
    asm volatile("s_waitcnt vmcnt(8)" : "+v"(xd), "+v"(gv[0]), "+v"(gv[1]), "+v"(gv[2]), "+v"(gv[3]), "+v"(gv[4]), "+v"(gv[5]), "+v"(gv[6]), "+v"(gv[7]) :: "memory");
    asm volatile("" : "+v"(gv[8]), "+v"(gv[9]), "+v"(gv[10]), "+v"(gv[11]), "+v"(gv[12]), "+v"(gv[13]), "+v"(gv[14]), "+v"(gv[15]));
    char* hs = smem + L_H + lw * 256;
    {
      const float sm = bfhi(sel[1]), rden = 1.0f / bflo(sel[1]);
      const unsigned d = pk2(bflo(xd) - sm, bfhi(xd) - sm);
      *(unsigned*)(hs + l * 4) = pk2(bflo(d) * rden, bfhi(d) * rden);
      const u32x4 x0 = *(const u32x4*)(hs + rg * 32), x1 = *(const u32x4*)(hs + rg * 32 + 16);
      f32x4 d0 = gemv2(x0[0], gv[0], gv[1], sl, z4), d1 = gemv2(x0[1], gv[2], gv[3], sl, z4);
      d0 = gemv2(x0[2], gv[4], gv[5], sl, d0); d1 = gemv2(x0[3], gv[6], gv[7], sl, d1);
      d0 = gemv2(x1[0], gv[8], gv[9], sl, d0); d1 = gemv2(x1[1], gv[10], gv[11], sl, d1);
      d0 = gemv2(x1[2], gv[12], gv[13], sl, d0); d1 = gemv2(x1[3], gv[14], gv[15], sl, d1);
      const u32x4 v = gemv_fin(d0 + d1, t12);
      if (gu && (l & 40) == 0) gs4(WS_GU + (cb * 16 + (lw >> 1) * 8 + ke) * 256 + cg * 32 + g * 16, ws, v);
    }
    if (FPR) __builtin_amdgcn_s_setprio(FPR);
    wotile(12); wotile(13); wotile(15);
    lds_wait4(flg + F_IM);
    wotile(2); wotile(3); wotile(7); wotile(8);
    lds_wait4(flg + F_BL);
    u32x4 p[4];
    const char* gp = ws + WS_GU + kc * 4096;
    static_for<10>([&](auto I) {
      constexpr int i = decltype(I)::value;
      if constexpr (i == PE) { GL4(p[0], l * 16u, gp, 0, P_SC1); GL4(p[1], l * 16u, gp, 1024, P_SC1); GL4(p[2], l * 16u, gp, 2048, P_SC1); GL4(p[3], l * 16u, gp, 3072, P_SC1); }
      if constexpr (i < 9 - WLT) wotile(((WLT ? 0xeba910654ull : 0x10eba9654ull) >> (4 * i)) & 15);
    });
    if (PE == 9) { waitv<4>(); lds_set(flg + F_WO + lw, 1u, l); }
    if (WOE) { waitv<0>(); lds_set(flg + F_WO + lw, 1u, l); }
    if (FPR != FPE) __builtin_amdgcn_s_setprio(FPE);
    waitv4<18 - 2 * WLT - 2 * PE>(p);
    asm volatile("" : "+v"(dv[0]), "+v"(dv[1]), "+v"(dv[2]), "+v"(dv[3]), "+v"(dv[4]), "+v"(dv[5]), "+v"(dv[6]), "+v"(dv[7]) :: "memory");
    if (gu) {
#pragma unroll 1
      for (unsigned spins = 0; spins < SPIN; ++spins) {
        const unsigned long long m0 = __ballot(bad4(p[0], t12) & 0xfffu), m1 = __ballot(bad4(p[1], t12) & 0xfffu), m2 = __ballot(bad4(p[2], t12) & 0xfffu),
                                 m3 = __ballot(bad4(p[3], t12) & 0xfffu);
        if ((m0 | m1 | m2 | m3) == 0ull) break;
        reload4c(p, l * 16u, gp, m0, m1, m2, m3);
      }
      float hv[4];
#pragma unroll
      for (int e = 0; e < 4; ++e) {
        const float gg = rbf(sum_g(untag12(p[0][e]) + untag12(p[1][e]))), uu = rbf(sum_g(untag12(p[2][e]) + untag12(p[3][e])));
        hv[e] = rbf(silu_f(gg)) * uu;
      }
      if (l < 16) *(u32x2*)(hs + l * 8) = u32x2{pk2(hv[0], hv[1]), pk2(hv[2], hv[3])};
      const u32x4 hp4 = *(const u32x4*)(hs + rg * 16);
      f32x4 d0 = gemv2(hp4[0], dv[0], dv[1], sl, z4), d1 = gemv2(hp4[1], dv[2], dv[3], sl, z4);
      d0 = gemv2(hp4[2], dv[4], dv[5], sl, d0); d1 = gemv2(hp4[3], dv[6], dv[7], sl, d1);
      const u32x4 v = gemv_fin(d0 + d1, t12);
      if ((l & 40) == 0) gs4(WS_Y + ((cq * 4 + lw) * 56 + kc) * 256 + cg * 32 + g * 16, ws, v);
      if (PE < 9 && !WOE) waitv<1>();
    } else if (PE < 9 && !WOE) waitv<0>();
    if (PE < 9 && !WOE) lds_set(flg + F_WO + lw, 1u, l);
    if (WLT) {
      if (WTR == 0) lds_wait4(flg + F_OS);
      static_for<WLT>([&](auto I) { wotile((0xeba910654ull >> (4 * (9 - WLT + decltype(I)::value))) & 15); });
      waitv<0>();
      lds_set(flg + F_BQ + lw, 1u, l);
    }
    if (FPG != FPE) __builtin_amdgcn_s_setprio(FPG);
    if ((idx >> 7) == panel) {
      const char* yb = ws + WS_Y + hp * 56 * 256 + lw * 4096;
      u32x2 xr;
      GL2(xr, r15 * 8u, (const char*)(x + (size_t)idx * DM + hp * 64), 0);
      const unsigned tm = lw < 3 ? 0xfffu : 0u;
      u32x4 pl[8];
      if (PYW == 1) lds_wait4(flg + F_OS);
      if (PYW == 2) lds_wait4(flg + F_GO);
      if (PYW == 3) { lds_wait4(flg + F_GO); __builtin_amdgcn_s_sleep(16); }
      pollt<PPY>(p, pl, l * 16u, yb, t12, tm);
      asm volatile("" : "+v"(xr));
      f32x4 y;
#pragma unroll
      for (int e = 0; e < 4; ++e) y[e] = sum_g((untag12(p[0][e]) + untag12(p[1][e])) + (lw < 3 ? untag12(p[2][e]) + untag12(p[3][e]) : 0.f));
      if (lw) {
        if (l < 16) *(f32x4*)(hs + l * 16) = y;
        lds_set(flg + F_AL + lw, 1u, l);
      } else {
      if (l == 0) flg[F_AL] = 1u;
      lds_wait4(flg + F_AL);
      y = (y + *(const f32x4*)(hs + 256 + r15 * 16)) + (*(const f32x4*)(hs + 512 + r15 * 16) + *(const f32x4*)(hs + 768 + r15 * 16));
      const int row = idx & 127;
      const float mn = ((const float*)(smem + L_MN))[row], rs = ((const float*)(smem + L_RS))[row];
      const float r = __uint_as_float((key & 0x80000000u) ? (key & 0x7fff0000u) : (~key & 0xffff0000u));
      const float sg = rbf(1.0f / (1.0f + __expf(-r)));
      float gi[4];
#pragma unroll
      for (int e = 0; e < 4; ++e) {
        const float xe = (e & 1) ? bfhi(xr[e >> 1]) : bflo(xr[e >> 1]);
        gi[e] = silu_r(rbf(rbf(rbf(xe + mn) * rs) + rbf(rbf(y[e]) * sg)));
      }
      if (l < 16) *(u32x2*)(smem + L_GI + l * 8) = u32x2{pk2(gi[0], gi[1]), pk2(gi[2], gi[3])};
      lds_set(flg + F_GT, 1u, l);
      }
      asm volatile("s_waitcnt vmcnt(0)" :: PKEEP(pl) : "memory");
    }
    return;
  }
  const int s = w >> 3;
  const int wt = lw & 1, wc = lw >> 1;
  const char* ob = ws + WS_O + (size_t)((RM == 1 ? xcd * 2 + (jj & 1) : RM == 2 ? (xcd >> 1) * 4 + (jj & 3) : panel) * 4 + lw) * 65536;
  const char* xfb = (const char*)(x + (size_t)(panel * 128 + lw * 32 + s * 16) * DM + hp * 64);
  u32x4 xv[2], gq[2];
  unsigned T;
  auto gate = [&]() {
    const float mn = ((const float*)(smem + L_MN))[lw * 32 + s * 16 + r15], rs = ((const float*)(smem + L_RS))[lw * 32 + s * 16 + r15];
#pragma unroll
    for (int hf = 0; hf < 2; ++hf) {
      unsigned x0 = xv[hf][0], x1 = xv[hf][1], x2 = xv[hf][2], x3 = xv[hf][3];
      sw16u(x0, x2); sw16u(x1, x3);
      xv[hf] = u32x4{x0, x1, x2, x3};
#pragma unroll
      for (int e = 0; e < 4; ++e) {
        const unsigned d = pk2(bflo(xv[hf][e]) + mn, bfhi(xv[hf][e]) + mn);
        const unsigned y = pk2(bflo(d) * rs, bfhi(d) * rs);
        gq[hf][e] = pk2(silu_r(bflo(y)), silu_r(bfhi(y)));
      }
    }
    pin2(gq); pin2(xv);
  };
  const int wrow = 8 * g + tq, wsw = (((wrow >> 1) & 1) << 2) | (((wrow >> 3) & 1) << 1);
  unsigned asw[3][2], wa[3][2];
#pragma unroll
  for (int q = 0; q < 3; ++q) {
#pragma unroll
    for (int ks = 0; ks < 2; ++ks) asw[q][ks] = lbase + (wt * 64 + r15) * 128 + (((4 * ks + g) ^ ((r15 >> 1) & 7)) * 16) + q * SLOT;
#pragma unroll
    for (int c = 0; c < 2; ++c) wa[q][c] = lbase + ABYTES + wrow * 128 + (((wc * 4 + 2 * c + (tp >> 1)) ^ wsw) * 16) + 8 * (tp & 1) + q * SLOT;
  }
  if (s) {
    u32x4 xa[4], wr[4];
    auto st_load = [&]() {
      const char* xb = (const char*)(x + (size_t)row0 * DM + lw * 256);
      const unsigned o = (l & 7) * 2048u + ((l >> 3) & 1) * 64u + g * 16u;
      GL4(xa[0], o, xb, 0, P_DEF); GL4(xa[1], o, xb, 128, P_DEF); GL4(xa[2], o, xb, 256, P_DEF); GL4(xa[3], o, xb, 384, P_DEF);
      const char* rb = (const char*)(w_r + lw * 256);
      const unsigned ro = (l & 1) * 64u + g * 16u;
      GL4(wr[0], ro, rb, 0, P_DEF); GL4(wr[1], ro, rb, 128, P_DEF); GL4(wr[2], ro, rb, 256, P_DEF); GL4(wr[3], ro, rb, 384, P_DEF);
    };
    auto st_load2 = [&](auto J) {
      constexpr int j = decltype(J)::value;
      const char* xb = (const char*)(x + (size_t)row0 * DM + lw * 256);
      const unsigned o = (l & 7) * 2048u + ((l >> 3) & 1) * 64u + g * 16u;
      GL4(xa[j], o, xb, j * 128, P_DEF);
      const char* rb = (const char*)(w_r + lw * 256);
      const unsigned ro = (l & 1) * 64u + g * 16u;
      GL4(wr[j], ro, rb, j * 128, P_DEF);
    };
    auto st_sum = [&](auto NV) {
      waitv4<decltype(NV)::value>(xa); waitv4<decltype(NV)::value>(wr);
      f32x4 d0 = z4, d1 = z4, d2 = z4, d3 = z4;
#pragma unroll
      for (int j = 0; j < 4; ++j) {
        d0 = MFMA(ones, as8(xa[j]), d0); d1 = MFMA(as8(wr[j]), as8(xa[j]), d1);
        d2 = MFMA(as8(xa[j]), as8(xa[j]), d2); d3 = MFMA(as8(wr[j]), ones, d3);
      }
      const bool hf = l & 8;
      const float q2 = (l & 2) ? ((l & 1) ? d2[3] : d2[2]) : ((l & 1) ? d2[1] : d2[0]);
      if (g == tq) ldsw1(lbase + L_REC + (lw * 16 + r15) * 16, __builtin_bit_cast(u32x4, f32x4{d0[0], hf ? d1[1] : d1[0], q2, hf ? d3[1] : d3[0]}));
      SB();
    };
    f32x4 acc[4][2];
    {
#pragma unroll
      for (int t = 0; t < 4; ++t) { acc[t][0] = z4; acc[t][1] = z4; }
      Frag<2> f[2];
      auto rd = [&](Frag<2>& y, auto I, auto OW, unsigned pa, const unsigned (&pw)[2]) {
        constexpr int i = decltype(I)::value, ow = decltype(OW)::value + 2 * WSLAB;
        if constexpr (i < 4) ds_128<i * 2048>(y.af[i], pa);
        else if constexpr (i & 1) ds_tr<ow + 512>(y.wh[(i - 4) >> 1], pw[(i - 4) >> 1]);
        else ds_tr<ow>(y.wl[(i - 4) >> 1], pw[(i - 4) >> 1]);
      };
      auto step = [&](Frag<2>& c, Frag<2>& y, auto OW, unsigned pa, const unsigned (&pw)[2], auto RD) {
        static_for<8>([&](auto I) {
          constexpr int i = decltype(I)::value;
          acc[i & 3][i >> 2] = MFMA(cat8(c.wl[i >> 2], c.wh[i >> 2]), c.af[i & 3], acc[i & 3][i >> 2]);
          SB();
          if constexpr (i < 4 && decltype(RD)::value) { rd(y, IC(2 * i), OW, pa, pw); rd(y, IC(2 * i + 1), OW, pa, pw); SB(); }
        });
      };
      auto tile = [&](auto J, auto BAR, auto RD) {
        constexpr int j = decltype(J)::value, n = (j + 1) % 3;
        f[0].wait();
        step(f[0], f[1], IC(4096), asw[j][1], wa[j], IC(1));
        f[1].wait();
        if constexpr (decltype(BAR)::value) { __builtin_amdgcn_s_barrier(); asm volatile("" ::: "memory"); }
        if constexpr (!decltype(RD)::value) { if (l == 0) __hip_atomic_store(flg + F_BL + lw, 1u, __ATOMIC_RELAXED, __HIP_MEMORY_SCOPE_WORKGROUP); SB(); }
        step(f[1], f[0], IC(0), asw[n][0], wa[n], RD);
      };
      auto sents = [&]() {
        unsigned ln;
        asm volatile("v_mbcnt_lo_u32_b32 %0, -1, 0\n\tv_mbcnt_hi_u32_b32 %0, -1, %0" : "=v"(ln));
        u32x4 ff = {~0u, ~0u, ~0u, ~0u};
        asm volatile("" : "+v"(ff));
        const unsigned ko = (ln >> 3) * 2048u + (ln & 7) * 16u + ((panel ^ (ln >> 3)) & 15) * 128u;
        if (!(AB & 1)) {
        static_for<4>([&](auto J) { gs4o<decltype(J)::value * 1024>(ln * 16u, ws + WS_O + (size_t)(panel * 4 + lw) * 65536 + hp * 4096, ff); });
        gs4(ko, ws + WS_KVP + (hp * 4 + lw) * 32768, ff);
        gs4(ko ^ 17408u, ws + WS_KVP + (hp * 4 + lw) * 32768, ff);
        gs4m(ln * 16u, ws + WS_KVR + hp * 8192 + lw * 2048 + panel * 128, ff);
        }
        SB();
      };
      if (STE) st_load();
      TICK();
      static_for<8>([&](auto I) { rd(f[0], I, IC(0), asw[0][0], wa[0]); });
      static_for<13>([&](auto J) {
        constexpr int t = decltype(J)::value, j = t % 3;
        if constexpr (t == STI && !STE && !(AB & 2)) st_load();
        if constexpr (t == SBP) { waitv<0>(); SB(); }
        if constexpr (t < 13 - QD) tile(IC(j), IC(1), IC(1));
        else {
          f[0].wait();
          static_for<4>([&](auto I) { rd(f[1], I, IC(0), asw[j][1], wa[j]); });
          f[1].wait();
          __builtin_amdgcn_s_barrier(); asm volatile("" ::: "memory");
        }
        if constexpr (t == STI + STD - 1 && !(AB & 2)) st_sum(IC(0));
        if constexpr (t == TCH) {
          if (!(AB & 8) && (PF & 8) && lw == 0 && !(xcd & 1) && jj < 24) pf3((xcd >> 2) ^ 1);
          if (!(AB & 8) && WOP == 3 && (lw & 1)) touch((l >> 2) * 2048u + (l & 3) * 128u, (const char*)wo + jj * 65536 + (lw >> 1) * 32768 + (xcd & 3) * 512, lbase + L_S + 20480);
        }
        if constexpr (PFB && t >= PFS && t + 3 + PFB <= 15) { if (lw == (t & 3)) pfi(t + 3 + PFB); }
        if constexpr (t == SBI) sents();
      });
      if (WOP == 2 && lw == 1 && !(xcd & 1) && jj < 16) { relane(); touch(l * 2048u, (const char*)wo + jj * 131072 + (xcd & 3) * 512 + 384, lbase + L_S + 20480); }
      if (WOP == 4 && (lw & 1)) { relane(); touch((l >> 2) * 2048u + (l & 3) * 128u, (const char*)wo + jj * 65536 + (lw >> 1) * 32768 + (xcd & 3) * 512, lbase + L_S + 20480); }
      relane();
      if (WOP == 5 && (lw & 1)) touch((l >> 2) * 2048u + (l & 3) * 128u, (const char*)wo + jj * 65536 + (lw >> 1) * 32768 + (xcd & 3) * 512, lbase + L_S + 20480);
      GL4(xv[0], xo, xfb, 0, P_DEF); GL4(xv[1], xo, xfb, 64, P_DEF);
      __builtin_amdgcn_s_barrier(); __builtin_amdgcn_s_barrier(); asm volatile("" ::: "memory");
      if (GB == 3 || GB == 4) {
        if (GB == 4) lds_wait1(flg + F_BQ + lw);
        lds_wait4(flg + F_ST);
        asm volatile("s_waitcnt vmcnt(0)" : "+v"(xv[0]), "+v"(xv[1]));
        gate();
        SB();
      }
      if (BQ == 3) lds_wait1(flg + F_IM + lw); else if (BQ == 4) lds_wait1(flg + F_EX + lw); else if (BQ) lds_wait1(flg + F_BQ + lw);
      if (BQP) __builtin_amdgcn_s_setprio(BQP);
      tile(IC(1), IC(0), IC(1)); tile(IC(2), IC(0), IC(1)); tile(IC(0), IC(0), IC(0));
    }
    T = lds_get(flg + F_T);
    bf16x8 w0, w1;
    wphi_frag(w0, w1, lbase + L_WP + wc * 2048 + (4 * g + tq) * 64 + tp * 8);
    lds_wait4(flg + F_ST);
    if (BW == 1) lds_wait4(flg + F_IM);
    if (BW == 2) lds_wait1(flg + F_IM + lw);
    if (BW == 3) lds_wait1(flg + F_EX + lw);
    bf16x8 pq[4][2];
    {
      const f32x4 c0 = *(const f32x4*)(smem + L_C + 512 + (wc * 32 + 4 * g) * 4), c1 = *(const f32x4*)(smem + L_C + 512 + (wc * 32 + 16 + 4 * g) * 4);
      f32x4 p[4][2];
      float sc[4];
      const float one[2] = {1.f, 1.f};
      f32x2 mr[4];
      rowst(mr, lbase + L_MN + (wt * 64 + r15) * 4);
#pragma unroll
      for (int t = 0; t < 4; ++t) {
        sc[t] = mr[t][1] * L2E;
        const f32x4 v0 = mr[t][0] * c0 + acc[t][0], v1 = mr[t][0] * c1 + acc[t][1];
        const bf16x8 qp = as8(u32x4{pk2(v0[0], v0[1]), pk2(v0[2], v0[3]), pk2(v1[0], v1[1]), pk2(v1[2], v1[3])});
        p[t][0] = MFMA(w0, qp, z4); p[t][1] = MFMA(w1, qp, z4);
      }
      phi_map<false>(p, sc, one, [&](int t, const unsigned (&pe)[8]) {
        pq[t][0] = as8(u32x4{pe[0], pe[1], pe[2], pe[3]}); pq[t][1] = as8(u32x4{pe[4], pe[5], pe[6], pe[7]});
      });
    }
    asm volatile("s_waitcnt vmcnt(0)" : "+v"(pq[0][0]), "+v"(pq[0][1]), "+v"(pq[1][0]), "+v"(pq[1][1]), "+v"(pq[2][0]), "+v"(pq[2][1]), "+v"(pq[3][0]), "+v"(pq[3][1]),
                 "+v"(xv[0]), "+v"(xv[1]));
    if (!GB) gate();
    lds_wait2(flg + F_SL + 2 * wc);
    u32x4 pl[8];
    {
      u32x4 kf[4];
      const char* kb = ws + WS_KVR + hp * 8192 + wc * 4096;
      if (PD2) __builtin_amdgcn_s_sleep(PD2);
      GL4(kf[0], l * 16u, kb, 0, P_SC1); GL4(kf[1], l * 16u, kb, 1024, P_SC1); GL4(kf[2], l * 16u, kb, 2048, P_SC1); GL4(kf[3], l * 16u, kb, 3072, P_SC1);
      float rd[4];
      lds_wait2(flg + F_KV + 2 * wc);
      {
        const bf16x8 z0 = *(const bf16x8*)(smem + L_Z + wc * 128 + g * 16), z1 = *(const bf16x8*)(smem + L_Z + wc * 128 + 64 + g * 16);
#pragma unroll
        for (int t = 0; t < 4; ++t) { rd[t] = __builtin_amdgcn_rcpf(rbf(rbf(MFMA(z1, pq[t][1], MFMA(z0, pq[t][0], z4))[0]) + 1e-6f)); pin1(rd[t]); }
      }
      waitv4<0>(kf);
#pragma unroll 1
      for (unsigned spins = 0; spins < SPIN; ++spins) {
        const unsigned long long m0 = __ballot(kf[0][3] == ~0u), m1 = __ballot(kf[1][3] == ~0u), m2 = __ballot(kf[2][3] == ~0u), m3 = __ballot(kf[3][3] == ~0u);
        if ((m0 | m1 | m2 | m3) == 0ull) break;
        reload4c(kf, l * 16u, kb, m0, m1, m2, m3);
      }
      if (POE == 2) { if (l == 0) __hip_atomic_store(flg + F_BQ + lw, 1u, __ATOMIC_RELAXED, __HIP_MEMORY_SCOPE_WORKGROUP); SB(); }
      if (BQ2) __builtin_amdgcn_s_setprio(BQ2);
      const char* pb = ws + WS_O + (size_t)(panel * 4 + wt * 2) * 65536 + hp * 4096 + wc * 2048;
#pragma unroll
      for (int t = 0; t < 4; ++t) {
        const f32x4 n0 = MFMA(as8(kf[2]), pq[t][1], MFMA(as8(kf[0]), pq[t][0], z4));
        const f32x4 n1 = MFMA(as8(kf[3]), pq[t][1], MFMA(as8(kf[1]), pq[t][0], z4));
        const f32x2 a0 = rb2(f32x2{n0[0], n0[1]}) * rd[t], a1 = rb2(f32x2{n0[2], n0[3]}) * rd[t], a2 = rb2(f32x2{n1[0], n1[1]}) * rd[t], a3 = rb2(f32x2{n1[2], n1[3]}) * rd[t];
        gs4((t >> 1) * 65536 + (t & 1) * 1024 + l * 16, pb, u32x4{pk2(a0[0], a0[1]), pk2(a1[0], a1[1]), pk2(a2[0], a2[1]), nosent(pk2(a3[0], a3[1]))});
      }
      if (l == 0) __hip_atomic_store(flg + F_OS + lw, 1u, __ATOMIC_RELAXED, __HIP_MEMORY_SCOPE_WORKGROUP);
    }
    if (BQP) __builtin_amdgcn_s_setprio(0);
    if (GB == 1) gate();
  } else {
    if (tid < 20) ((u32x4*)flg)[tid] = u32x4{0, 0, 0, 0};
    f32x4 acc[4][4];
    Frag<4> f[2];
    u32x4 fs;
    f32x4 c0, c1;
    f32x2 mr[4];
    s16x4 wq[4];
    unsigned mra, cca;
    {
#pragma unroll
      for (int t = 0; t < 4; ++t)
#pragma unroll
        for (int c = 0; c < 4; ++c) acc[t][c] = z4;
      auto rd = [&](Frag<4>& y, auto I, auto OW, unsigned pa, const unsigned (&pw)[2]) {
        constexpr int i = decltype(I)::value, ow = decltype(OW)::value;
        if constexpr (i < 4) ds_128<i * 2048>(y.af[i], pa);
        else {
          constexpr int c = (i - 4) >> 1;
          if constexpr (i & 1) ds_tr<ow + (c >> 1) * WSLAB + 512>(y.wh[c], pw[c & 1]);
          else ds_tr<ow + (c >> 1) * WSLAB>(y.wl[c], pw[c & 1]);
        }
      };
      auto step = [&](Frag<4>& c, Frag<4>& y, auto OW, unsigned pa, const unsigned (&pw)[2], auto RD) {
        static_for<decltype(RD)::value ? 16 : 8>([&](auto I) {
          constexpr int i = decltype(I)::value, t = i & 3, cb = i >> 2;
          if constexpr (cb < 2) acc[t][cb] = MFMA(cat8(c.wl[cb], c.wh[cb]), c.af[t], acc[t][cb]);
          else acc[t][cb] = MFMA(c.af[t], cat8(c.wl[cb], c.wh[cb]), acc[t][cb]);
          SB();
          if constexpr (i < 12 && decltype(RD)::value) { rd(y, I, OW, pa, pw); SB(); }
        });
      };
      __builtin_amdgcn_s_setprio(2);
      TICK();
      static_for<12>([&](auto I) { rd(f[0], I, IC(0), asw[0][0], wa[0]); });
#pragma unroll 1
      for (int it = 0; it < 5; ++it) {
        static_for<3>([&](auto J) {
          constexpr int j = decltype(J)::value, n = (j + 1) % 3;
          f[0].wait();
          step(f[0], f[1], IC(4096), asw[j][1], wa[j], IC(1));
          f[1].wait();
          __builtin_amdgcn_s_barrier(); asm volatile("" ::: "memory");
          step(f[1], f[0], IC(0), asw[n][0], wa[n], IC(1));
        });
      }
      relane();
      mra = lbase + L_MN + (wt * 64 + r15) * 4; cca = lbase + L_C + (wc * 32 + 4 * g) * 4;
      unsigned wpa = lbase + L_WP + wc * 2048 + (4 * g + tq) * 64 + tp * 8, fa = lbase + L_F;
      asm volatile("" : "+v"(mra), "+v"(cca), "+v"(wpa), "+v"(fa));
      f[0].wait();
      step(f[0], f[1], IC(4096), asw[0][1], wa[0], IC(1));
      f[1].wait();
      asm volatile("ds_read_b128 %0, %12 offset:%16\n\tds_read_b128 %1, %13\n\tds_read_b128 %2, %13 offset:64\n\t"
                   "ds_read2_b32 %3, %14 offset1:128\n\tds_read2_b32 %4, %14 offset0:16 offset1:144\n\tds_read2_b32 %5, %14 offset0:32 offset1:160\n\t"
                   "ds_read2_b32 %6, %14 offset0:48 offset1:176\n\tds_read_b64_tr_b16 %7, %15\n\tds_read_b64_tr_b16 %8, %15 offset:1024\n\t"
                   "ds_read_b64_tr_b16 %9, %15 offset:32\n\tds_read_b64_tr_b16 %10, %15 offset:1056\n\tds_read_b32 %11, %12 offset:%17"
                   : "=&v"(fs), "=&v"(c0), "=&v"(c1), "=&v"(mr[0]), "=&v"(mr[1]), "=&v"(mr[2]), "=&v"(mr[3]), "=&v"(wq[0]), "=&v"(wq[1]), "=&v"(wq[2]), "=&v"(wq[3]), "=&v"(T)
                   : "v"(fa), "v"(cca), "v"(mra), "v"(wpa), "n"(F_ST * 4), "n"(F_T * 4) : "memory");
      SB();
      step(f[1], f[0], IC(0), asw[0][0], wa[0], IC(0));
      if (BQ == 2) { if (l == 0) __hip_atomic_store(flg + F_BQ + lw, 1u, __ATOMIC_RELAXED, __HIP_MEMORY_SCOPE_WORKGROUP); SB(); }
    }
    asm volatile("s_waitcnt lgkmcnt(0)" : "+v"(fs), "+v"(c0), "+v"(c1), "+v"(mr[0]), "+v"(mr[1]), "+v"(mr[2]), "+v"(mr[3]), "+v"(wq[0]), "+v"(wq[1]), "+v"(wq[2]), "+v"(wq[3]), "+v"(T));
    if (__builtin_expect(!__builtin_amdgcn_readfirstlane(fs[0] & fs[1] & fs[2] & fs[3]), 1)) {
      lds_wait4(flg + F_ST);
      asm volatile("ds_read_b128 %0, %2\n\tds_read_b128 %1, %2 offset:64\n\ts_waitcnt lgkmcnt(0)" : "=&v"(c0), "=&v"(c1) : "v"(cca) : "memory");
      rowst(mr, mra);
    }
    T = __builtin_amdgcn_readfirstlane(T);
    const unsigned t12 = T & 0xfffu;
    const bf16x8 w0 = cat8(wq[0], wq[1]), w1 = cat8(wq[2], wq[3]);
    char* img = smem + (wc ? SLOT + ABYTES : L_S) + wt * 8192;
    {
      const int sw = (((r15 >> 1) & 3) << 2) | (r15 & 1) | (((r15 >> 3) & 1) << 1);
      f32x4 p[4][2];
      float sc[4], nrm[2];
#pragma unroll
      for (int t = 0; t < 4; ++t) {
        sc[t] = mr[t][1] * L2E;
        if (t < 2) nrm[t] = (g & 1) ? mr[2 * t + 1][1] : mr[2 * t][1];
        const f32x4 v0 = mr[t][0] * c0 + acc[t][0], v1 = mr[t][0] * c1 + acc[t][1];
        const bf16x8 kc = as8(u32x4{pk2(v0[0], v0[1]), pk2(v0[2], v0[3]), pk2(v1[0], v1[1]), pk2(v1[2], v1[3])});
        p[t][0] = MFMA(w0, kc, z4); p[t][1] = MFMA(w1, kc, z4);
      }
      auto vl = [&](int i0, int i1) {
#pragma unroll
        for (int i = i0; i < i1; ++i)
          acc[i & 3][2 + (i >> 2)] = MFMA(f[1].af[i & 3], cat8(f[1].wl[2 + (i >> 2)], f[1].wh[2 + (i >> 2)]), acc[i & 3][2 + (i >> 2)]);
      };
      if (BQ == 1) { if (l == 0) __hip_atomic_store(flg + F_BQ + lw, 1u, __ATOMIC_RELAXED, __HIP_MEMORY_SCOPE_WORKGROUP); }
      if (!VL) vl(0, 8);
      SB();
      phi_map<true>(p, sc, nrm, [&](int t, const unsigned (&pe)[8]) {
        if (VL == 1 && t < 2) { SB(); vl(4 * t, 4 * t + 4); SB(); }
        if (VL == 2) { SB(); vl(2 * t, 2 * t + 2); SB(); }
#pragma unroll
        for (int fb = 0; fb < 4; ++fb) *(u32x2*)(img + (t * 16 + r15) * 128 + (((4 * fb + g) ^ sw) * 8)) = u32x2{pe[2 * fb], pe[2 * fb + 1]};
      });
    }
    asm volatile("" ::: "memory");
    if (GB == 4) { if (l == 0) __hip_atomic_store(flg + F_BQ + lw, 1u, __ATOMIC_RELAXED, __HIP_MEMORY_SCOPE_WORKGROUP); SB(); }
    f32x4 kva[4][2], za[4];
    {
      const int row = 4 * g + tq, sw = (((row >> 1) & 3) << 2) | (row & 1) | (((row >> 3) & 1) << 1);
      s16x4 tl[2][4], th[2][4];
#pragma unroll
      for (int fl = 0; fl < 4; ++fl) {
        const unsigned a = LDS_OFF(img) + row * 128 + (((4 * (fl ^ (2 * wt)) + tp) ^ sw) * 8);
        DS_TR(tl[0][fl], a, 0); DS_TR(th[0][fl], a, 2048); DS_TR(tl[1][fl], a, 4096); DS_TR(th[1][fl], a, 6144);
      }
      bf16x8 vp[2][2];
      {
        const float cv0 = ((const float*)(smem + L_C))[64 + wc * 32 + r15], cv1 = ((const float*)(smem + L_C))[64 + wc * 32 + 16 + r15];
        unsigned vw[4][2][2];
#pragma unroll
        for (int t = 0; t < 4; ++t) {
          const f32x4 m4 = *(const f32x4*)(smem + L_MN + (wt * 64 + t * 16 + 4 * g) * 4);
          float v[8];
#pragma unroll
          for (int i = 0; i < 4; ++i) { v[i] = __builtin_fmaf(m4[i], cv0, acc[t][2][i]); v[4 + i] = __builtin_fmaf(m4[i], cv1, acc[t][3][i]); }
          vw[t][0][0] = pk2(v[0], v[1]); vw[t][0][1] = pk2(v[2], v[3]); vw[t][1][0] = pk2(v[4], v[5]); vw[t][1][1] = pk2(v[6], v[7]);
        }
#pragma unroll
        for (int q = 0; q < 2; ++q)
#pragma unroll
          for (int d = 0; d < 2; ++d) vp[q][d] = as8(u32x4{vw[2 * q][d][0], vw[2 * q][d][1], vw[2 * q + 1][d][0], vw[2 * q + 1][d][1]});
      }
      const s16x4* dp = (const s16x4*)(smem + L_DN + wt * 128 + g * 8);
      bf16x8 dn0 = cat8(dp[0], dp[4]), dn1 = cat8(dp[8], dp[12]);
      asm volatile("s_waitcnt lgkmcnt(0)" : "+v"(tl[0][0]), "+v"(tl[0][1]), "+v"(tl[0][2]), "+v"(tl[0][3]), "+v"(th[0][0]), "+v"(th[0][1]), "+v"(th[0][2]), "+v"(th[0][3]),
                   "+v"(tl[1][0]), "+v"(tl[1][1]), "+v"(tl[1][2]), "+v"(tl[1][3]), "+v"(th[1][0]), "+v"(th[1][1]), "+v"(th[1][2]), "+v"(th[1][3]) :: "memory");
#pragma unroll
      for (int i = 0; i < 4; ++i) {
        const int fl = (i + 2) & 3;
        const bf16x8 a0 = cat8(tl[0][fl], th[0][fl]), a1 = cat8(tl[1][fl], th[1][fl]);
        kva[fl][0] = MFMA(a1, vp[1][0], MFMA(a0, vp[0][0], z4));
        kva[fl][1] = MFMA(a1, vp[1][1], MFMA(a0, vp[0][1], z4));
        za[fl] = MFMA(a1, dn1, MFMA(a0, dn0, z4));
      }
      f32x4* xb = (f32x4*)img + l;
      xb[0] = kva[2][0]; xb[64] = kva[2][1]; xb[128] = kva[3][0]; xb[192] = kva[3][1]; xb[256] = (l & 1) ? za[3] : za[2];
      lds_set(flg + F_EX + lw, 1u, l);
    }
    lds_wait1(flg + F_EX + (lw ^ 1));
    const char* kb = ws + WS_KVP + (hp * 4 + lw) * 32768;
    {
      const f32x4* xb = (const f32x4*)(img + (1 - 2 * wt) * 8192) + l;
      const unsigned o = kvo();
#pragma unroll
      for (int d = 0; d < 2; ++d) {
        const f32x4 u = kva[0][d] + xb[d * 64], v = kva[1][d] + xb[128 + d * 64];
        gs4(o ^ (d * 17408u), kb, u32x4{pk2(u[0], u[1]), pk2(u[2], u[3]), pk2(v[0], v[1]), nosent(pk2(v[2], v[3]))});
      }
      const f32x4 z = ((l & 1) ? za[1] : za[0]) + xb[256];
      if (r15 < 2) gs4(WS_ZP + ((hp * 4 + lw) * 16 + panel) * 128 + (r15 * 4 + g) * 16, ws,
                       u32x4{tag12(z[0], t12), tag12(z[1], t12), tag12(z[2], t12), tag12(z[3], t12)});
      lds_set(flg + F_IM + lw, 1u, l);
    }
    GL4(xv[0], xo, xfb, 0, P_DEF); GL4(xv[1], xo, xfb, 64, P_DEF);
    const char* zb = ws + WS_ZP + (hp * 4 + lw) * 2048;
    u32x4 zp[2], pl[4];
    {
      u32x4 p[2];
      const unsigned one = (l & 1) ? 0x3f800000u : 0x3f80u;
      u32x4 s0 = {(l & 2) ? 0u : one, (l & 2) ? one : 0u, 0u, 0u}, s1 = {0u, 0u, s0[0], s0[1]};
      asm volatile("" : "+v"(s0), "+v"(s1));
      if (PD1) __builtin_amdgcn_s_sleep(PD1);
      poll2<PP1>(p, pl, l * 16u, kb + panel * 2048);
      asm volatile("" : "+v"(xv[0]), "+v"(xv[1]));
      GL4(zp[0], l * 16u, zb, 0, P_SC1); GL4(zp[1], l * 16u, zb, 1024, P_SC1);
      f32x4 u = MFMA(as8(s0), as8(p[1]), MFMA(as8(s0), as8(p[0]), z4)), v = MFMA(as8(s1), as8(p[1]), MFMA(as8(s1), as8(p[0]), z4));
#pragma unroll
      for (int e = 0; e < 4; ++e) { u[e] += ror8(u[e]); v[e] += ror8(v[e]); }
      gs4m(l * 16u, ws + WS_KVR + hp * 8192 + lw * 2048 + panel * 128, u32x4{pk2(u[0], u[1]), pk2(u[2], u[3]), pk2(v[0], v[1]), nosent(pk2(v[2], v[3]))});
      if (l == 0) __hip_atomic_store(flg + F_SL + lw, 1u, __ATOMIC_RELAXED, __HIP_MEMORY_SCOPE_WORKGROUP);
    }
    {
      asm volatile("s_waitcnt vmcnt(1)" : "+v"(zp[0]), "+v"(zp[1]) :: "memory");
      asm volatile("" :: "v"(pl[0]), "v"(pl[1]), "v"(pl[2]), "v"(pl[3]));
#pragma unroll 1
      for (unsigned spins = 0; spins < SPIN; ++spins) {
        const unsigned long long m0 = __ballot(bad4(zp[0], t12) & 0xfffu), m1 = __ballot(bad4(zp[1], t12) & 0xfffu);
        if ((m0 | m1) == 0ull) break;
        reload2(zp, l * 16u, zb, m0, m1);
      }
      float z[4];
#pragma unroll
      for (int e = 0; e < 4; ++e) { const float q = sum_g(untag12(zp[0][e]) + untag12(zp[1][e])); z[e] = q + ror8(q); }
      if (l < 8) *(u32x2*)(smem + L_Z + lw * 64 + (l & 3) * 16 + (l >> 2) * 8) = u32x2{pk2(z[0], z[1]), pk2(z[2], z[3])};
      lds_set(flg + F_KV + lw, 1u, l);
    }
    __builtin_amdgcn_s_setprio(0);
    gate();
  }
  {
    u32x4 of[NS][2];
    const unsigned po = r15 * 4096u + ((g & 1) ? 2044u : 4092u);
    f32x4 acc[2][4];
#pragma unroll
    for (int t = 0; t < 2; ++t)
#pragma unroll
      for (int c = 0; c < 4; ++c) acc[t][c] = z4;
    tie8<1>(acc);
    unsigned wb[4], wx[4];
    {
      const int row = 4 * g + tq, f = ((row >> 1) & 1) | ((((row >> 2) ^ (row >> 3)) & 1) << 2);
#pragma unroll
      for (int nb = 0; nb < 4; ++nb) {
        wb[nb] = lbase + s * 8192 + row * 128 + ((((tp & 2) + (nb & 1) + 4 * (nb >> 1)) ^ f) * 16) + (tp & 1) * 8;
      }
    }
    const unsigned v0 = l * 16u + s * 1024u, v1 = l * 16u + (1 - s) * 1024u, v2 = v0 + s * 32768u, v3 = v0 + (1 - s) * 32768u;
    auto oadr = [&](int h) { return ob + (otile(h >> 1) + (h < 14 ? 8 * s : 0)) * 4096 + (h & 1) * 2048; };
    s16x4 wl[2][4], wh[2][4], el[2][4], eh[2][4];
    auto wrd = [&](auto H, auto NB) {
      constexpr int h = decltype(H)::value, nb = decltype(NB)::value, o = ((h >> 1) & 3) * 16384 + (h & 1) * 4096;
      if constexpr (h == 8) { wb[nb] += 65536u; asm volatile("" : "+v"(wb[nb])); }
      ds_tr<o>(wl[h % WD][nb], wb[nb]); ds_tr<o + 2048>(wh[h % WD][nb], wb[nb]);
    };
    const unsigned xa = lbase + (2 * (lw >> 1) + s) * 8192 + (lw & 1) * 4096 + l * 16;
    int idx;
    auto wo0 = [&]() {
      lds_wait4(flg + F_WO);
      idx = 2047 - (int)(lds_get(flg + F_SEL) & 2047u);
      static_for<4>([&](auto NB) { wrd(IC(0), NB); });
      if constexpr (WD == 2) static_for<4>([&](auto NB) { wrd(IC(1), NB); });
    };
    if (!WOL) wo0();
    unsigned pf0, pf1;
    asm volatile("" : "=v"(pf0), "=v"(pf1));
    if (s) lds_wait1(flg + F_GO + lw);
    else {
      if (!POE || POE == 3) lds_wait4(flg + F_OS);
      if (POE == 2) lds_wait1(flg + F_BQ + lw);
      if (PD3) __builtin_amdgcn_s_sleep(PD3);
      if (POE != 3) poll_o2<PP3>(pf0, pf1, po, ob, (unsigned long long)(0x01010101u << otile(0)));
      if (POE && POE < 3 && !POW) lds_wait4(flg + F_OS);
      if (l == 0) __hip_atomic_store(flg + F_GO + lw, 1u, __ATOMIC_RELAXED, __HIP_MEMORY_SCOPE_WORKGROUP);
    }
    if (BPR && s) __builtin_amdgcn_s_setprio(BPR);
    static_for<NS>([&](auto H) {
      if constexpr (ASL && decltype(H)::value == ASP) { if (!s) __builtin_amdgcn_s_sleep(ASL); }
      opair(of[decltype(H)::value], v0, v1, oadr(decltype(H)::value));
    });
    if (GB == 2 && s) gate();
    if (WOL) wo0();
    ocheck<2 * NS - 2>(of[0], v0, v1, oadr(0));
    asm volatile("" :: "v"(pf0), "v"(pf1));
    static_for<14>([&](auto H) {
      constexpr int h = decltype(H)::value;
      if constexpr (WLT && h == (WLT == 4 ? 7 : 9)) lds_wait4(flg + F_BQ);
      if constexpr (h == BPH) { if (s) __builtin_amdgcn_s_setprio(1); }
      static_for<4>([&](auto NB) {
        constexpr int nb = decltype(NB)::value;
        waitl2<8 * WD - 2>(wl[h % WD][nb], wh[h % WD][nb]);
        mfma_t(acc[1][nb], cat8(wl[h % WD][nb], wh[h % WD][nb]), of[h % NS][1]); mfma_t(acc[0][nb], cat8(wl[h % WD][nb], wh[h % WD][nb]), of[h % NS][0]);
        wrd(IC(h + WD), NB);
      });
      if constexpr (h + NS < 16) opair(of[h % NS], h + NS < 14 ? v0 : v2, h + NS < 14 ? v1 : v3, oadr(h + NS));
      if constexpr (h == 4) { if (l == 0) __hip_atomic_store(flg + F_PG + s * 4 + lw, 1u, __ATOMIC_RELAXED, __HIP_MEMORY_SCOPE_WORKGROUP); SB(); }
      if constexpr (h == 8) {
#pragma unroll
        for (int nb = 0; nb < 4; ++nb) { wx[nb] = wb[nb] + 8192u - s * 16384u; asm volatile("" : "+v"(wx[nb])); }
      }
      if constexpr (h >= 9 && h < 12)
        static_for<4>([&](auto NB) {
          constexpr int nb = decltype(NB)::value, o = 49152 + (h > 9) * 4096;
          if constexpr (h != 10) { ds_tr<o>(el[h > 9][nb], wx[nb]); ds_tr<o + 2048>(eh[h > 9][nb], wx[nb]); }
          else if constexpr (WD == 1) { ds_tr<o>(wl[1][nb], wb[nb]); ds_tr<o + 2048>(wh[1][nb], wb[nb]); }
        });
      if constexpr (h == 12) lds_wait4(flg + F_PG + s * 4);
      if constexpr (h < 13) ocheck<2 * ((h + NS < 15 ? h + NS : 15) - h - 1), OCX>(of[(h + 1) % NS], v0, v1, oadr(h + 1));
    });
    asm volatile("s_nop 4\n\tds_write_b128 %0, %1\n\tds_write_b128 %0, %2 offset:1024\n\tds_write_b128 %0, %3 offset:2048\n\tds_write_b128 %0, %4 offset:3072"
                 :: "v"(xa), "v"(acc[1][0]), "v"(acc[1][1]), "v"(acc[1][2]), "v"(acc[1][3]) : "memory");
    waitl8<0>(wl[0], wh[0]); waitl8<0>(wl[1], wh[1]); waitl8<0>(el[0], eh[0]); waitl8<0>(el[1], eh[1]);
    asm volatile("" ::: "memory");
    if (l == 0) __hip_atomic_store(flg + F_X + s * 4 + lw, 1u, __ATOMIC_RELAXED, __HIP_MEMORY_SCOPE_WORKGROUP);
    if (BPE == 1) __builtin_amdgcn_s_setprio(0);
    ocheck<2, true>(of[14 % NS], v2, v3, oadr(14));
#pragma unroll
    for (int nb = 0; nb < 4; ++nb) mfma_t(acc[0][nb], cat8(wl[0][nb], wh[0][nb]), of[14 % NS][0]);
#pragma unroll
    for (int nb = 0; nb < 4; ++nb) mfma_t(acc[0][nb], cat8(el[0][nb], eh[0][nb]), of[14 % NS][1]);
    if (__builtin_expect((idx >> 4) == panel * 8 + lw * 2 + s, 0)) {
      lds_wait1(flg + F_GT);
      if (r15 == (idx & 15)) {
        const u32x2* gi = (const u32x2*)(smem + L_GI + (g >> 1) * 32 + (g & 1) * 8);
        const u32x2 a = gi[0], c = gi[2], d = gi[8], e = gi[10];
        gq[0] = u32x4{a[0], a[1], c[0], c[1]}; gq[1] = u32x4{d[0], d[1], e[0], e[1]};
      }
    }
    unsigned fx;
    const unsigned fa = LDS_OFF(flg + F_X + (1 - s) * 4 + lw), xr = xa + 8192u - s * 16384u;
    asm volatile("ds_read_b32 %0, %5\n\tds_read_b128 %1, %6\n\tds_read_b128 %2, %6 offset:1024\n\tds_read_b128 %3, %6 offset:2048\n\tds_read_b128 %4, %6 offset:3072"
                 : "=&v"(fx), "=&v"(acc[1][0]), "=&v"(acc[1][1]), "=&v"(acc[1][2]), "=&v"(acc[1][3]) : "v"(fa), "v"(xr) : "memory");
    f32x2 xf[8], gf[8];
#pragma unroll
    for (int i = 0; i < 8; ++i) {
      xf[i] = f32x2{bflo(xv[i >> 2][i & 3]), bfhi(xv[i >> 2][i & 3])}; gf[i] = f32x2{bflo(gq[i >> 2][i & 3]), bfhi(gq[i >> 2][i & 3])};
      asm volatile("" : "+v"(xf[i]), "+v"(gf[i]));
    }
    char* op = (char*)(out + (size_t)(panel * 128 + lw * 32 + s * 16) * DM + hp * 64);
    unsigned oo = (l & 7) * 2048u + (l & 8) * 8u + g * 16u;
    char* op2 = op + 16384;
    asm volatile("" : "+v"(oo), "+s"(op), "+s"(op2));
    ocheck<0, true>(of[15 % NS], v2, v3, oadr(15));
#pragma unroll
    for (int nb = 0; nb < 4; ++nb) mfma_t(acc[0][nb], cat8(wl[1][nb], wh[1][nb]), of[15 % NS][0]);
#pragma unroll
    for (int nb = 0; nb < 4; ++nb) mfma_t(acc[0][nb], cat8(el[1][nb], eh[1][nb]), of[15 % NS][1]);
    if (BPE == 2) __builtin_amdgcn_s_setprio(0);
    if (BPE == 3) __builtin_amdgcn_s_setprio(2);
    if (__builtin_expect(b == 0 && w == 0, 0)) { if (l == 0) gs1(0u, ws, T); }
    asm volatile("s_waitcnt lgkmcnt(0)" : "+v"(fx), "+v"(acc[1][0]), "+v"(acc[1][1]), "+v"(acc[1][2]), "+v"(acc[1][3]) :: "memory");
    if (__builtin_expect(__builtin_amdgcn_readfirstlane(fx) == 0u, 0))
#pragma unroll 1
    for (unsigned spins = 0; spins < SPIN && __builtin_amdgcn_readfirstlane(fx) == 0u; ++spins)
      asm volatile("s_sleep 1\n\tds_read_b32 %0, %5\n\tds_read_b128 %1, %6\n\tds_read_b128 %2, %6 offset:1024\n\tds_read_b128 %3, %6 offset:2048\n\t"
                   "ds_read_b128 %4, %6 offset:3072\n\ts_waitcnt lgkmcnt(0)"
                   : "+v"(fx), "+v"(acc[1][0]), "+v"(acc[1][1]), "+v"(acc[1][2]), "+v"(acc[1][3]) : "v"(fa), "v"(xr) : "memory");
    tie4<7>(acc[0]);
    unsigned rr[8];
#pragma unroll
    for (int i = 0; i < 8; ++i) {
      const f32x2 sv = f32x2{acc[0][i >> 1][2 * (i & 1)], acc[0][i >> 1][2 * (i & 1) + 1]} + f32x2{acc[1][i >> 1][2 * (i & 1)], acc[1][i >> 1][2 * (i & 1) + 1]};
      const f32x2 r = xf[i] + rb2(rb2(sv) * gf[i]);
      rr[i] = pk2(r[0], r[1]);
    }
    {
      sw16u(rr[0], rr[2]); sw16u(rr[1], rr[3]); sw16u(rr[4], rr[6]); sw16u(rr[5], rr[7]);
      unsigned a0, a1, a2, a3, c0, c1, c2, c3;
#define CD(d, x, y) "v_cndmask_b32_dpp " d ", " x ", " y ", vcc row_ror:8 row_mask:0xf bank_mask:0xf\n\t"
      asm volatile("s_mov_b32 vcc_lo, 0x00ff00ff\n\ts_mov_b32 vcc_hi, 0x00ff00ff\n\t" CD("%0", "%12", "%8") CD("%2", "%14", "%10") CD("%1", "%13", "%9") CD("%3", "%15", "%11")
                   "s_not_b64 vcc, vcc\n\t" CD("%4", "%8", "%12") CD("%5", "%9", "%13") CD("%6", "%10", "%14") CD("%7", "%11", "%15")
                   : "=&v"(a0), "=&v"(a1), "=&v"(a2), "=&v"(a3), "=&v"(c0), "=&v"(c1), "=&v"(c2), "=&v"(c3)
                   : "v"(rr[0]), "v"(rr[1]), "v"(rr[2]), "v"(rr[3]), "v"(rr[4]), "v"(rr[5]), "v"(rr[6]), "v"(rr[7]) : "vcc", "scc");
      const u32x4 a = {a0, a1, a2, a3}, c = {c0, c1, c2, c3};
      asm volatile("global_store_dwordx4 %0, %1, %3 sc1\n.Lkf_last:\n\tglobal_store_dwordx4 %0, %2, %4 sc1" :: "v"(oo), "v"(a), "v"(c), "s"(op), "s"(op2) : "memory");
    }
    if (s) {
    } else {
    }
  }
}

extern "C" void kernel_launch(void* const* d_in, const int* in_sizes, int n_in,
                              void* d_out, int out_size, void* d_ws, size_t ws_size,
                              hipStream_t stream) {
  (void)hipFuncSetAttribute((const void*)kf_main, hipFuncAttributeMaxDynamicSharedMemorySize, LDSM);
  kf_main<<<256, 768, LDSM, stream>>>((const u16*)d_in[0], (const u16*)d_in[1], (char*)d_ws, (const u16*)d_in[5], (const u16*)d_in[2],
                                      (const u16*)d_in[3], (const u16*)d_in[6], (const u16*)d_in[7], (const u16*)d_in[8], (u16*)d_out);
}
```

```cpp
#include <hip/hip_runtime.h>
#include <stdint.h>
#include <utility>

typedef __attribute__((ext_vector_type(8))) short bf16x8;
typedef __attribute__((ext_vector_type(4))) short s16x4;
typedef __attribute__((ext_vector_type(4))) float f32x4;
typedef __attribute__((ext_vector_type(2))) float f32x2;
typedef __attribute__((ext_vector_type(4))) unsigned u32x4;
typedef __attribute__((ext_vector_type(2))) unsigned u32x2;
typedef __attribute__((ext_vector_type(2))) __bf16 bfx2;
typedef unsigned short u16;

#define DI __device__ __forceinline__
#define MFMA(a, b, c) __builtin_amdgcn_mfma_f32_16x16x32_bf16((a), (b), (c), 0, 0, 0)
#define SB() __builtin_amdgcn_sched_barrier(0)
#define IC(x) std::integral_constant<int, (x)>{}

DI unsigned pk2(float a, float b) { f32x2 v = {a, b}; return __builtin_bit_cast(unsigned, __builtin_convertvector(v, bfx2)); }
DI float bflo(unsigned u) { return __uint_as_float(u << 16); }
DI float bfhi(unsigned u) { return __uint_as_float(u & 0xffff0000u); }
DI float rbf(float x) { return (float)(__bf16)x; }
DI f32x2 rb2(f32x2 v) { return f32x2{__uint_as_float(pk2(0.f, v[0])), __uint_as_float(pk2(0.f, v[1]))}; }
DI bf16x8 cat8(s16x4 lo, s16x4 hi) { return __builtin_shufflevector(lo, hi, 0, 1, 2, 3, 4, 5, 6, 7); }
DI bf16x8 as8(u32x4 v) { return __builtin_bit_cast(bf16x8, v); }
template <int... I, class F> DI void static_for_impl(std::integer_sequence<int, I...>, F&& f) { (f(std::integral_constant<int, I>{}), ...); }
template <int N, class F> DI void static_for(F&& f) { static_for_impl(std::make_integer_sequence<int, N>{}, f); }
template <int N> DI void waitv() { asm volatile("s_waitcnt vmcnt(%0)" ::"n"(N) : "memory"); }
#define RAW_BARRIER() do { asm volatile("s_waitcnt lgkmcnt(0)" ::: "memory"); __builtin_amdgcn_s_barrier(); asm volatile("" ::: "memory"); } while (0)
#define TICK() do { SB(); RAW_BARRIER(); SB(); } while (0)
#define DS_B128(dst, addr, off) asm volatile("ds_read_b128 %0, %1 offset:%2" : "=v"(dst) : "v"(addr), "n"(off))
#define DS_TR(dst, addr, off) asm volatile("ds_read_b64_tr_b16 %0, %1 offset:%2" : "=v"(dst) : "v"(addr), "n"(off))
#define LDS_OFF(p) ((unsigned)(uintptr_t)(__attribute__((address_space(3))) const char*)(p))
enum { P_DEF = 0, P_SC1 = 1, P_NT = 2 };
template <int OFF, int POL> DI void gl4(u32x4& d, unsigned voff, const char* sb) {
  if constexpr (POL == P_SC1) asm volatile("global_load_dwordx4 %0, %1, %2 offset:%3 sc1" : "=v"(d) : "v"(voff), "s"(sb), "n"(OFF));
  else if constexpr (POL == P_NT) asm volatile("global_load_dwordx4 %0, %1, %2 offset:%3 nt" : "=v"(d) : "v"(voff), "s"(sb), "n"(OFF));
  else asm volatile("global_load_dwordx4 %0, %1, %2 offset:%3" : "=v"(d) : "v"(voff), "s"(sb), "n"(OFF));
}
#define GL4(dst, voff, sb, off, pol) gl4<off, pol>(dst, voff, sb)
#define GL2(dst, voff, sb, off) asm volatile("global_load_dwordx2 %0, %1, %2 offset:%3" : "=v"(dst) : "v"(voff), "s"(sb), "n"(off))
#define GL1(dst, voff, sb, off) asm volatile("global_load_dword %0, %1, %2 offset:%3 sc1" : "=v"(dst) : "v"(voff), "s"(sb), "n"(off))
DI void bl4nt(u32x4& d, unsigned voff, u32x4 rs, unsigned soff) {
  asm volatile("buffer_load_dwordx4 %0, %1, %2, %3 offen nt" : "=v"(d) : "v"(voff), "s"(rs), "s"(soff));
}
DI void mfma_t(f32x4& c, bf16x8 a, u32x4 bb) { asm volatile("v_mfma_f32_16x16x32_bf16 %0, %1, %2, %0" : "+v"(c) : "v"(a), "v"(bb)); }
template <int N> DI void tie8(f32x4 (&a)[2][4]) {
  asm volatile("s_nop %8" : "+v"(a[0][0]), "+v"(a[0][1]), "+v"(a[0][2]), "+v"(a[0][3]), "+v"(a[1][0]), "+v"(a[1][1]), "+v"(a[1][2]), "+v"(a[1][3]) : "n"(N));
}
template <int OFF> DI void ds_tr(s16x4& d, unsigned a) { DS_TR(d, a, OFF); }
template <int N> DI void waitv4(u32x4 (&v)[4]) { asm volatile("s_waitcnt vmcnt(%4)" : "+v"(v[0]), "+v"(v[1]), "+v"(v[2]), "+v"(v[3]) : "n"(N)); }
template <int N> DI void waitl8(s16x4 (&a)[4], s16x4 (&c)[4]) {
  asm volatile("s_waitcnt lgkmcnt(%8)" : "+v"(a[0]), "+v"(c[0]), "+v"(a[1]), "+v"(c[1]), "+v"(a[2]), "+v"(c[2]), "+v"(a[3]), "+v"(c[3]) : "n"(N));
}
DI void gs4(unsigned voff, const char* sb, u32x4 v) { asm volatile("global_store_dwordx4 %0, %1, %2 sc1\n\ts_nop 1" :: "v"(voff), "v"(v), "s"(sb) : "memory"); }
DI void gs1(unsigned voff, const char* sb, unsigned v) { asm volatile("global_store_dword %0, %1, %2 sc1" :: "v"(voff), "v"(v), "s"(sb) : "memory"); }
DI void glds(unsigned voff, const char* sbase, unsigned lds) {
  asm volatile("s_mov_b32 m0, %2\n\ts_nop 0\n\tglobal_load_lds_dwordx4 %0, %1" :: "v"(voff), "s"(sbase), "s"(lds) : "memory");
}
DI void touch(unsigned voff, const char* sbase, unsigned lds) {
  asm volatile("s_mov_b32 m0, %2\n\ts_nop 0\n\tglobal_load_lds_dword %0, %1" :: "v"(voff), "s"(sbase), "s"(lds) : "memory");
}
DI void touch4(unsigned voff, const char* sbase, unsigned lds) {
  asm volatile("s_mov_b64 exec, 15\n\ts_mov_b32 m0, %2\n\ts_nop 0\n\tglobal_load_lds_dword %0, %1\n\ts_mov_b64 exec, -1" :: "v"(voff), "s"(sbase), "s"(lds) : "memory");
}
DI float silu_f(float x) { return x / (1.0f + __expf(-x)); }
DI float silu_r(float x) { return x * __builtin_amdgcn_rcpf(1.0f + __expf(-x)); }
DI float dot2(unsigned a, unsigned b, float c) { return __builtin_amdgcn_fdot2_f32_bf16(__builtin_bit_cast(bfx2, a), __builtin_bit_cast(bfx2, b), c, false); }

template <int OFF> DI void gs4o(unsigned voff, const char* sb, u32x4 v) {
  asm volatile("global_store_dwordx4 %0, %1, %2 offset:%3 sc1\n\ts_nop 1" :: "v"(voff), "v"(v), "s"(sb), "n"(OFF) : "memory");
}
DI void gs4m(unsigned voff, const char* sb, u32x4 v) {
  asm volatile("s_mov_b64 exec, 0xff\n\tglobal_store_dwordx4 %0, %1, %2 sc1\n\ts_mov_b64 exec, -1\n\ts_nop 0" :: "v"(voff), "v"(v), "s"(sb) : "memory");
}
DI void gs2(unsigned voff, const char* sb, u32x2 v) { asm volatile("global_store_dwordx2 %0, %1, %2 sc1" :: "v"(voff), "v"(v), "s"(sb) : "memory"); }
#define GL1P(dst, voff, sb) asm volatile("global_load_dword %0, %1, %2" : "=v"(dst) : "v"(voff), "s"(sb))
DI u32x4 ldsr1(unsigned a) { u32x4 v; asm volatile("ds_read_b128 %0, %1\n\ts_waitcnt lgkmcnt(0)" : "=v"(v) : "v"(a) : "memory"); return v; }
DI void ldsw1(unsigned a, u32x4 v) { asm volatile("ds_write_b128 %0, %1" :: "v"(a), "v"(v) : "memory"); }
DI unsigned nosent(unsigned v) { return min(v, 0xfffffffeu); }

constexpr int DM = 1024, FF = 3584;
constexpr unsigned WS_ST = 4096;
constexpr unsigned WS_CA = WS_ST + 2048 * 8;
constexpr unsigned WS_CS = WS_CA + 256 * 16;
constexpr unsigned WS_KVP = WS_CS + 256 * 1024;
constexpr unsigned WS_ZP = WS_KVP + 2048 * 1024;
constexpr unsigned WS_KVR = WS_ZP + 1024 * 128;
constexpr unsigned WS_O = WS_KVR + 16 * 8192;
constexpr unsigned WS_GU = WS_O + 4096 * 1024;
constexpr unsigned WS_Y = WS_GU + 56 * 4096;

constexpr int ABYTES = 16384, WSLAB = 8192, SLOT = 40960;
constexpr int L_EW = 3 * SLOT;
constexpr int L_EX = L_EW + 2 * WSLAB;
constexpr int L_REC = L_EW;
constexpr int L_Z = L_EX + ABYTES;
constexpr int L_WP = L_Z + 256;
constexpr int L_MN = L_WP + 4096;
constexpr int L_RS = L_MN + 512;
constexpr int L_DN = L_RS + 512;
constexpr int L_C = L_DN + 256;
constexpr int L_H = L_C + 768;
constexpr int L_GI = L_H + 1024;
constexpr int L_F = L_GI + 128;
constexpr int L_DU = L_F + 320;
constexpr int LDSM = L_DU + 256;
constexpr int F_KV = 0, F_WO = 4, F_PG = 8, F_X = 16, F_T = 24, F_GT = 25, F_SEL = 28, F_BL = 32, F_IM = 36, F_EX = 40, F_ST = 44, F_SD = 48, F_CA = 52, F_OS = 60, F_GO = 64, F_AL = 68, F_SL = 72, F_LD = 76;
#define SPINV (1u << 20)
constexpr unsigned SPIN = SPINV;
DI void pin1(float& v) { asm volatile("" : "+v"(v)); }
DI void pinf(f32x4& v) { asm volatile("" : "+v"(v)); }
DI void pin2(u32x4 (&v)[2]) { asm volatile("" : "+v"(v[0]), "+v"(v[1])); }
constexpr float L2E = 1.4426950408889634f;

template <int CTRL, int RMASK> DI float dpp_add(float v) {
  return v + __builtin_bit_cast(float, __builtin_amdgcn_update_dpp(0, __builtin_bit_cast(int, v), CTRL, RMASK, 0xf, true));
}
DI float wave_total(float v) {
  v = dpp_add<0x111, 0xf>(v); v = dpp_add<0x112, 0xf>(v); v = dpp_add<0x114, 0xf>(v); v = dpp_add<0x118, 0xf>(v);
  v = dpp_add<0x142, 0xa>(v); v = dpp_add<0x143, 0xc>(v);
  return __builtin_bit_cast(float, __builtin_amdgcn_readlane(__builtin_bit_cast(int, v), 63));
}
DI f32x2 sw32(float a, float b) {
  auto r = __builtin_amdgcn_permlane32_swap(__float_as_uint(a), __float_as_uint(b), false, false);
  return f32x2{__uint_as_float(r[0]), __uint_as_float(r[1])};
}
DI f32x2 sw16(float a, float b) {
  auto r = __builtin_amdgcn_permlane16_swap(__float_as_uint(a), __float_as_uint(b), false, false);
  return f32x2{__uint_as_float(r[0]), __uint_as_float(r[1])};
}
DI float ror8(float v) { return __builtin_bit_cast(float, __builtin_amdgcn_update_dpp(0, __builtin_bit_cast(int, v), 0x128, 0xf, 0xf, true)); }
DI float sum_g(float v) {
  f32x2 a = sw32(v, v); v = a[0] + a[1];
  a = sw16(v, v); return a[0] + a[1];
}
DI unsigned tag12(float v, unsigned t) { return ((__float_as_uint(v) + 0x800u) & 0xfffff000u) | t; }
DI float untag12(unsigned u) { return __uint_as_float(u & 0xfffff000u); }
DI unsigned bad4(u32x4 v, unsigned t) { return (v[0] ^ t) | (v[1] ^ t) | (v[2] ^ t) | (v[3] ^ t); }
DI bool all0(unsigned v) { return __ballot(v != 0u) == 0ull; }
DI void lds_set(unsigned* f, unsigned v, int l) {
  asm volatile("s_waitcnt lgkmcnt(0)" ::: "memory");
  if (l == 0) __hip_atomic_store(f, v, __ATOMIC_RELAXED, __HIP_MEMORY_SCOPE_WORKGROUP);
}
DI unsigned lds_get(unsigned* f) { return __builtin_amdgcn_readfirstlane(__hip_atomic_load(f, __ATOMIC_RELAXED, __HIP_MEMORY_SCOPE_WORKGROUP)); }
DI void lds_wait4(unsigned* f) {
#pragma unroll 1
  for (unsigned spins = 0; spins < SPIN; ++spins) {
    u32x4 v; asm volatile("ds_read_b128 %0, %1\n\ts_waitcnt lgkmcnt(0)" : "=v"(v) : "v"(LDS_OFF(f)) : "memory");
    if (__builtin_amdgcn_readfirstlane(v[0] & v[1] & v[2] & v[3])) break;
    __builtin_amdgcn_s_sleep(1);
  }
}

template <int NC> struct Frag {
  bf16x8 af[4]; s16x4 wl[NC], wh[NC];
  DI void wait() {
    if constexpr (NC == 4)
      asm volatile("s_waitcnt lgkmcnt(0)" : "+v"(af[0]), "+v"(af[1]), "+v"(af[2]), "+v"(af[3]), "+v"(wl[0]), "+v"(wh[0]), "+v"(wl[1]), "+v"(wh[1]),
                   "+v"(wl[2]), "+v"(wh[2]), "+v"(wl[3]), "+v"(wh[3]));
    else
      asm volatile("s_waitcnt lgkmcnt(0)" : "+v"(af[0]), "+v"(af[1]), "+v"(af[2]), "+v"(af[3]), "+v"(wl[0]), "+v"(wh[0]), "+v"(wl[1]), "+v"(wh[1]));
  }
};
template <int OFF> DI void ds_128(bf16x8& d, unsigned a) { DS_B128(d, a, OFF); }
DI void lds_wait2(unsigned* f) {
#pragma unroll 1
  for (unsigned spins = 0; spins < SPIN; ++spins) {
    u32x2 v; asm volatile("ds_read_b64 %0, %1\n\ts_waitcnt lgkmcnt(0)" : "=v"(v) : "v"(LDS_OFF(f)) : "memory");
    if (__builtin_amdgcn_readfirstlane(v[0] & v[1])) break;
    __builtin_amdgcn_s_sleep(1);
  }
}
DI void reload2(u32x4 (&p)[2], unsigned o, const char* sb, unsigned long long m0, unsigned long long m1) {
  asm volatile("s_mov_b64 exec, %4\n\tglobal_load_dwordx4 %0, %2, %3 sc1\n\ts_mov_b64 exec, %5\n\tglobal_load_dwordx4 %1, %2, %3 offset:1024 sc1\n\t"
               "s_mov_b64 exec, -1\n\ts_waitcnt vmcnt(0)"
               : "+v"(p[0]), "+v"(p[1]) : "v"(o), "s"(sb), "s"(m0), "s"(m1) : "memory");
}

template <int CTRL> DI unsigned dpp_umax(unsigned v) { return max(v, (unsigned)__builtin_amdgcn_update_dpp((int)v, (int)v, CTRL, 0xf, 0xf, false)); }
template <int CTRL, int RMASK> DI unsigned dpp_umax_r(unsigned v) { return max(v, (unsigned)__builtin_amdgcn_update_dpp((int)v, (int)v, CTRL, RMASK, 0xf, false)); }
DI void lds_wait1(unsigned* f) {
#pragma unroll 1
  for (unsigned spins = 0; spins < SPIN && lds_get(f) == 0u; ++spins) __builtin_amdgcn_s_sleep(1);
  asm volatile("" ::: "memory");
}

DI void cs_read(u32x2 (&c)[4], unsigned a) {
  asm volatile("ds_read_b64_tr_b16 %0, %4\n\tds_read_b64_tr_b16 %1, %4 offset:512\n\tds_read_b64_tr_b16 %2, %4 offset:4096\n\tds_read_b64_tr_b16 %3, %4 offset:4608"
               : "=&v"(c[0]), "=&v"(c[1]), "=&v"(c[2]), "=&v"(c[3]) : "v"(a) : "memory");
}
DI void cs_sum(u32x2 (&c)[4], float& s0, float& s1) {
  asm volatile("s_waitcnt lgkmcnt(0)" : "+v"(c[0]), "+v"(c[1]), "+v"(c[2]), "+v"(c[3]) :: "memory");
#pragma unroll
  for (int i = 0; i < 4; ++i) { s0 = dot2(c[i][0], 0x3f803f80u, s0); s1 = dot2(c[i][1], 0x3f803f80u, s1); }
  asm volatile("" : "+v"(s0), "+v"(s1));
}
#define WIN 10
#define NG 4
#define NH 8
#define PE 7
#define PP1 12
#define PD1 16
#define PD3 6
#define PPY 12
#define FP0 1
#define FPR 2
#define FPE 0
#define FPG 3
#define GAS 10
#define SB0 2
#define SBI 3
#define SBP 5
#define CTS 6
#define ES0 7
#define ESL 5
#define NF 4
#define VPT 2
#define WX 0
#define WE 0
#define AP 2
#define BP 1
#define BQ 0
#define WNO WIN
#define PD2 14
#define GB 1
#define PYD 0
#define PRE 0
#define GL 0
#define NS 3
constexpr int PUB = SBP > SB0 + 3 ? SBP : SB0 + 3;
constexpr int n_e(int S) { int n = 0; for (int i = 0; i < 8; ++i) n += ES0 + i * ESL / 8 == S; return n; }
constexpr int n_x(int S) { return (S == PUB) + (S == 0) + (S == GAS ? 2 : 0) + (S == CTS) + n_e(S); }
static_assert(SBP > SBI && PUB >= 3 && PUB < GAS && GAS <= 10 && CTS <= 10 && ES0 > SB0 && ES0 + ESL <= 12, "");
DI void wphi_frag(bf16x8& w0, bf16x8& w1, unsigned a) {
  s16x4 t0, t1, t2, t3;
  asm volatile("ds_read_b64_tr_b16 %0, %4\n\tds_read_b64_tr_b16 %1, %4 offset:1024\n\tds_read_b64_tr_b16 %2, %4 offset:32\n\t"
               "ds_read_b64_tr_b16 %3, %4 offset:1056\n\ts_waitcnt lgkmcnt(0)" : "=&v"(t0), "=&v"(t1), "=&v"(t2), "=&v"(t3) : "v"(a) : "memory");
  w0 = cat8(t0, t1); w1 = cat8(t2, t3);
}
DI float mx2(float a, float b) { return __builtin_elementwise_maximum(a, b); }
DI float mx3(float a, float b, float c) { return __builtin_elementwise_maximum(__builtin_elementwise_maximum(a, b), c); }
DI void bcast2(float m, f32x2& c0, f32x2& c1) {
  const f32x2 a = sw16(m, m);
  c0 = sw32(a[0], a[0]); c1 = sw32(a[1], a[1]);
}
template <bool NRM, class F> DI void phi_map(const f32x4 (&p)[4][2], const float (&sc)[4], const float (&nrm)[2], F&& sink) {
  float r[4];
  f32x2 c[4];
#pragma unroll
  for (int t = 0; t < 4; ++t) {
    const float mx = mx2(mx3(mx3(p[t][0][0], p[t][0][1], p[t][0][2]), p[t][0][3], p[t][1][0]), mx3(p[t][1][1], p[t][1][2], p[t][1][3]));
    const float nm = mx2(mx3(mx3(-p[t][0][0], -p[t][0][1], -p[t][0][2]), -p[t][0][3], -p[t][1][0]), mx3(-p[t][1][1], -p[t][1][2], -p[t][1][3]));
    const f32x2 a = sw32(mx, nm);
    r[t] = mx2(a[0], a[1]);
  }
#pragma unroll
  for (int q = 0; q < 2; ++q) {
    const f32x2 a = sw16(r[2 * q], r[2 * q + 1]);
    bcast2(mx2(a[0], a[1]), c[2 * q], c[2 * q + 1]);
  }
#pragma unroll
  for (int t = 0; t < 4; ++t) c[t] *= -sc[t];
#pragma unroll
  for (int q = 0; q < 2; ++q) {
    f32x2 e[2][8], sm[2];
#pragma unroll
    for (int i = 0; i < 8; ++i)
#pragma unroll
      for (int j = 0; j < 2; ++j) {
        const float v = p[2 * q + j][i >> 2][i & 3];
        const f32x2 a = __builtin_elementwise_fma(f32x2{v, v}, f32x2{sc[2 * q + j], -sc[2 * q + j]}, c[2 * q + j]);
        e[j][i] = f32x2{__builtin_amdgcn_exp2f(a[0]), __builtin_amdgcn_exp2f(a[1])};
      }
    if constexpr (NRM) {
#pragma unroll
    for (int j = 0; j < 2; ++j) {
      sm[j] = ((e[j][0] + e[j][1]) + (e[j][2] + e[j][3])) + ((e[j][4] + e[j][5]) + (e[j][6] + e[j][7]));
      sm[j] = sw32(sm[j][0], sm[j][1]);
    }
    const f32x2 a = sw16(sm[0][0] + sm[0][1], sm[1][0] + sm[1][1]);
    float rc = __builtin_amdgcn_rcpf(a[0] + a[1]);
    rc *= nrm[q];
    bcast2(rc, sm[0], sm[1]);
    }
#pragma unroll
    for (int j = 0; j < 2; ++j) {
      unsigned pe[8];
#pragma unroll
      for (int i = 0; i < 8; ++i) if (NRM) e[j][i] *= sm[j];
#pragma unroll
      for (int i = 0; i < 4; ++i) { pe[i] = pk2(e[j][2 * i][0], e[j][2 * i + 1][0]); pe[4 + i] = pk2(e[j][2 * i][1], e[j][2 * i + 1][1]); }
      sink(2 * q + j, pe);
    }
  }
}

struct Sel { unsigned e[4], o[4]; };
DI Sel make_sel(int l) {
  Sel s;
#pragma unroll
  for (int q = 0; q < 4; ++q) {
    const bool on = ((l & 7) >> 1) == q;
    s.e[q] = on ? ((l & 1) ? 0x01000c0cu : 0x0c0c0100u) : 0x0c0c0c0cu;
    s.o[q] = on ? ((l & 1) ? 0x03020c0cu : 0x0c0c0302u) : 0x0c0c0c0cu;
  }
  return s;
}
DI f32x4 gemv2(unsigned xp, u32x4 w0, u32x4 w1, const Sel& s, f32x4 d) {
  const u32x4 a0 = {__builtin_amdgcn_perm(xp, xp, s.e[0]), __builtin_amdgcn_perm(xp, xp, s.e[1]), __builtin_amdgcn_perm(xp, xp, s.e[2]), __builtin_amdgcn_perm(xp, xp, s.e[3])};
  const u32x4 a1 = {__builtin_amdgcn_perm(xp, xp, s.o[0]), __builtin_amdgcn_perm(xp, xp, s.o[1]), __builtin_amdgcn_perm(xp, xp, s.o[2]), __builtin_amdgcn_perm(xp, xp, s.o[3])};
  return MFMA(as8(a1), as8(w1), MFMA(as8(a0), as8(w0), d));
}
DI u32x4 gemv_fin(f32x4 d, unsigned t12) {
  u32x4 v;
#pragma unroll
  for (int e = 0; e < 4; ++e) { const float t = ror8(d[e]); v[e] = tag12(d[e] + sw32(t, t)[1], t12); }
  return v;
}

DI void opair(u32x4 (&o)[2], unsigned v0, unsigned v1, const char* p) { GL4(o[0], v0, p, 0, P_SC1); GL4(o[1], v1, p, 0, P_SC1); }
template <int N, bool COLD = false> DI void ocheck(u32x4 (&o)[2], unsigned v0, unsigned v1, const char* p) {
  asm volatile("s_waitcnt vmcnt(%2)" : "+v"(o[0]), "+v"(o[1]) : "n"(N));
  if (COLD && __builtin_expect(__ballot(max(o[0][3], o[1][3]) == ~0u) == 0ull, 1)) return;
#pragma unroll 1
  for (unsigned spins = 0; spins < SPIN && __ballot(max(o[0][3], o[1][3]) == ~0u) != 0ull; ++spins)
    asm volatile("s_sleep 2\n\tglobal_load_dwordx4 %0, %2, %4 sc1\n\tglobal_load_dwordx4 %1, %3, %4 sc1\n\ts_waitcnt vmcnt(0)"
                 : "+v"(o[0]), "+v"(o[1]) : "v"(v0), "v"(v1), "s"(p) : "memory");
}
DI void sw16u(unsigned& a, unsigned& b) { auto r = __builtin_amdgcn_permlane16_swap(a, b, false, false); a = r[0]; b = r[1]; }
template <int N> DI void waitl2(s16x4& a, s16x4& c) { asm volatile("s_waitcnt lgkmcnt(%2)" : "+v"(a), "+v"(c) : "n"(N)); }
template <int N> DI void tie4(f32x4 (&a)[4]) { asm volatile("s_nop %4" : "+v"(a[0]), "+v"(a[1]), "+v"(a[2]), "+v"(a[3]) : "n"(N)); }
DI void rowst(f32x2 (&m)[4], unsigned a) {
  asm volatile("ds_read2_b32 %0, %4 offset1:128\n\tds_read2_b32 %1, %4 offset0:16 offset1:144\n\tds_read2_b32 %2, %4 offset0:32 offset1:160\n\t"
               "ds_read2_b32 %3, %4 offset0:48 offset1:176\n\ts_waitcnt lgkmcnt(0)" : "=&v"(m[0]), "=&v"(m[1]), "=&v"(m[2]), "=&v"(m[3]) : "v"(a) : "memory");
}
DI void reload4c(u32x4 (&p)[4], unsigned o, const char* sb, unsigned long long m0, unsigned long long m1, unsigned long long m2, unsigned long long m3) {
  asm volatile("s_mov_b64 exec, %6\n\tglobal_load_dwordx4 %0, %4, %5 sc1\n\ts_mov_b64 exec, %7\n\tglobal_load_dwordx4 %1, %4, %5 offset:1024 sc1\n\t"
               "s_mov_b64 exec, %8\n\tglobal_load_dwordx4 %2, %4, %5 offset:2048 sc1\n\ts_mov_b64 exec, %9\n\tglobal_load_dwordx4 %3, %4, %5 offset:3072 sc1\n\t"
               "s_mov_b64 exec, -1\n\ts_waitcnt vmcnt(0)"
               : "+v"(p[0]), "+v"(p[1]), "+v"(p[2]), "+v"(p[3]) : "v"(o), "s"(sb), "s"(m0), "s"(m1), "s"(m2), "s"(m3) : "memory");
}

#define PL(r, o) "global_load_dwordx4 v[" #r ":" #r "+3], %[vo], %[sb] offset:" #o " sc1\n\t"
#define PC(r, m) "v_cmp_eq_u32 " m ", -1, v[" #r "+3]\n\t"
#define PM(d, r) "v_mov_b32 v[" #d "], v[" #r "]\n\tv_mov_b32 v[" #d "+1], v[" #r "+1]\n\tv_mov_b32 v[" #d "+2], v[" #r "+2]\n\tv_mov_b32 v[" #d "+3], v[" #r "+3]\n\t"
template <int D> DI void poll2(u32x4 (&p)[2], u32x4 (&q)[4], unsigned vo, const char* sb) {
  unsigned long long m; unsigned n = SPIN;
  asm volatile(PL(128, 0) PL(132, 1024) "s_sleep %[d]\n\t" PL(136, 0) PL(140, 1024)
               "1:\n\ts_waitcnt vmcnt(2)\n\t" PC(128, "vcc") PC(132, "%[m]") "s_or_b64 exec, vcc, %[m]\n\ts_cbranch_scc0 2f\n\t" PL(128, 0) PL(132, 1024)
               "s_mov_b64 exec, -1\n\ts_waitcnt vmcnt(2)\n\t" PC(136, "vcc") PC(140, "%[m]") "s_or_b64 exec, vcc, %[m]\n\ts_cbranch_scc0 3f\n\t" PL(136, 0) PL(140, 1024)
               "s_mov_b64 exec, -1\n\ts_sub_u32 %[n], %[n], 1\n\ts_cbranch_scc0 1b\n\ts_waitcnt vmcnt(0)\n"
               "3:\n\ts_mov_b64 exec, -1\n\t" PM(120, 136) PM(124, 140) "s_branch 4f\n"
               "2:\n\ts_mov_b64 exec, -1\n\t" PM(120, 128) PM(124, 132) "4:\n\ts_nop 1"
               : "={v[120:123]}"(p[0]), "={v[124:127]}"(p[1]), "=&{v[128:131]}"(q[0]), "=&{v[132:135]}"(q[1]), "=&{v[136:139]}"(q[2]), "=&{v[140:143]}"(q[3]),
                 [m] "=&s"(m), [n] "+s"(n)
               : [vo] "v"(vo), [sb] "s"(sb), [d] "n"(D) : "vcc", "scc", "memory");
}
#define PL4(r) PL(r, 0) PL(r+4, 1024) PL(r+8, 2048) PL(r+12, 3072)
#define PM2(o, r) "v_mov_b64 v[128+" #o ":129+" #o "], v[" #r "+" #o ":" #r "+1+" #o "]\n\t"
#define PM16(r) PM2(0, r) PM2(2, r) PM2(8, r) PM2(10, r) PM2(4, r) PM2(6, r) PM2(12, r) PM2(14, r)
#define PQ8(c) c "{v[96:99]}"(q[0]), c "{v[100:103]}"(q[1]), c "{v[104:107]}"(q[2]), c "{v[108:111]}"(q[3]), c "{v[112:115]}"(q[4]), c "{v[116:119]}"(q[5]), c "{v[120:123]}"(q[6]), c "{v[124:127]}"(q[7])
#define PB(a, r) "v_bitop3_b32 " a ", " a ", v[" #r "], %[t] bitop3:0xf6\n\t"
#define PB7(a, r) "v_xor_b32 " a ", %[t], v[" #r "]\n\t" PB(a, r+1) PB(a, r+2) PB(a, r+3) PB(a, r+4) PB(a, r+5) PB(a, r+6) PB(a, r+7)
#define PCT(r) "s_waitcnt vmcnt(4)\n\t" PB7("v128", r) PB7("v129", r+8) "v_and_b32 v128, 0xfff, v128\n\tv_and_or_b32 v128, v129, %[tm], v128\n\tv_cmp_ne_u32 vcc, 0, v128\n\ts_or_b64 exec, vcc, vcc\n\t"
template <int D> DI void pollt(u32x4 (&p)[4], u32x4 (&q)[8], unsigned vo, const char* sb, unsigned t, unsigned tm) {
  unsigned n = SPIN;
  asm volatile(PL4(96) "s_sleep %[d]\n\t" PL4(112)
               "1:\n\t" PCT(96) "s_cbranch_scc0 2f\n\t" PL4(96) "s_mov_b64 exec, -1\n\t" PCT(112) "s_cbranch_scc0 3f\n\t" PL4(112)
               "s_mov_b64 exec, -1\n\ts_sub_u32 %[n], %[n], 1\n\ts_cbranch_scc0 1b\n\ts_waitcnt vmcnt(0)\n"
               "3:\n\ts_mov_b64 exec, -1\n\t" PM16(112) "s_branch 4f\n"
               "2:\n\ts_mov_b64 exec, -1\n\t" PM16(96) "4:\n\ts_nop 1"
               : "=&{v[128:131]}"(p[0]), "=&{v[132:135]}"(p[1]), "=&{v[136:139]}"(p[2]), "=&{v[140:143]}"(p[3]), PQ8("=&"), [n] "+s"(n)
               : [vo] "v"(vo), [sb] "s"(sb), [t] "s"(t), [tm] "s"(tm), [d] "n"(D) : "vcc", "scc", "memory");
}
#define PKEEP(pl) "v"(pl[0]), "v"(pl[1]), "v"(pl[2]), "v"(pl[3]), "v"(pl[4]), "v"(pl[5]), "v"(pl[6]), "v"(pl[7])
__global__ __launch_bounds__(768) void kf_main(const u16* __restrict__ x, const u16* __restrict__ wqkv, char* ws, const u16* __restrict__ w_r,
                                               const u16* __restrict__ wphi, const u16* __restrict__ wo, const u16* __restrict__ w_gate,
                                               const u16* __restrict__ w_up, const u16* __restrict__ w_down, u16* __restrict__ out) {
  extern __shared__ __attribute__((aligned(16))) char smem[];
  const int b = blockIdx.x;
  const int xcd = b & 7, jj = b >> 3;
  const int panel = (xcd >> 2) * 8 + (jj & 7), hp = (xcd & 3) * 4 + (jj >> 3);
  const int tid = threadIdx.x;
  const int w = __builtin_amdgcn_readfirstlane(tid >> 6), lw = w & 3;
  int l = tid & 63, r15 = l & 15, g = l >> 4, tq = r15 >> 2, tp = l & 3;
  unsigned xo;
  auto relane = [&]() {
    asm volatile("v_mbcnt_lo_u32_b32 %0, -1, 0\n\tv_mbcnt_hi_u32_b32 %0, -1, %0" : "=v"(l));
    r15 = l & 15; g = l >> 4; tq = r15 >> 2; tp = l & 3; xo = r15 * 2048u + g * 16u;
  };
  const unsigned lbase = LDS_OFF(smem);
  auto pf3 = [&](int half) {
    touch(l * 6144u, (const char*)wqkv + (size_t)(half * 512 + (jj & 7) * 64) * 6144 + (jj >> 3) * 2048 + (xcd & 3) * 512 + 384, lbase + L_DU);
  };
  const char* pc0; unsigned clen;
  asm volatile("s_getpc_b64 %0\n.Lkf_here:\n\ts_mov_b32 %1, .Lkf_last-.Lkf_here" : "=s"(pc0), "=s"(clen));
  if (__builtin_expect(w < 4, 1)) {
    if (__builtin_expect(w >= 2, 1)) {
      touch(min(((jj * 2 + w) & 15) * 4096u + l * 64u, clen), pc0, lbase + L_DU);
    }
    if (__builtin_expect(w == 0 && !(xcd & 1) && jj < 24, 1)) pf3(xcd >> 2);
  }
  unsigned* flg = (unsigned*)(smem + L_F);
  const bf16x8 ones = {0x3f80, 0x3f80, 0x3f80, 0x3f80, 0x3f80, 0x3f80, 0x3f80, 0x3f80};
  const f32x4 z4 = {0.f, 0.f, 0.f, 0.f};
  const int row0 = panel * 128 + hp * 8;
  auto ptile = [&](int p) { return p == 0 ? hp : p <= 12 ? ((hp | 3) + p) & 15 : hp ^ (p - 12); };
  auto kvo = [&]() { return (l >> 3) * 2048u + (l & 7) * 16u + ((panel ^ (l >> 3)) & 15) * 128u; };
  if (__builtin_expect((w >> 2) == 1, 1)) {
    const int srow = l >> 3, sch = l & 7, cg = sch, rg = srow;
    const int kt0 = 4 * (panel >> 2);
    unsigned G, Tw, t12;
    unsigned voA[2], voW[2];
    const char* sA = (const char*)(x + (size_t)panel * 128 * DM);
    const char* sQ = (const char*)(wqkv + hp * 64);
#pragma unroll
    for (int i = 0; i < 2; ++i) {
      const int row = lw * 32 + i * 8 + srow, wr = lw * 16 + i * 8 + srow;
      voA[i] = (unsigned)(row * 2048 + ((sch ^ ((row >> 1) & 7)) * 16));
      voW[i] = (unsigned)(wr * 6144 + ((sch ^ ((((wr >> 1) & 1) << 2) | (((wr >> 3) & 1) << 1))) * 16));
    }
    auto stage = [&](int S) {
      const int kt = (S + kt0) & 15;
      const char* a = sA + kt * 128; const char* wv = sQ + (size_t)kt * 64 * 6144;
      const unsigned d = lbase + (S % 3) * SLOT;
#pragma unroll
      for (int i = 0; i < 4; ++i) glds(voA[i & 1], a + (i >> 1) * 32768, d + lw * 4096 + i * 1024);
#pragma unroll
      for (int i = 0; i < 6; ++i) glds(voW[i & 1], wv + ((i >> 1) == 2 ? 0 : ((i >> 1) + 1) * 2048), d + ABYTES + (i >> 1) * WSLAB + lw * 2048 + (i & 1) * 1024);
    };
    auto last = [&](int i) {
      const int kt = (15 + kt0) & 15;
      const char* a = sA + kt * 128; const char* wv = sQ + (size_t)kt * 64 * 6144;
      if (i < 4) glds(voA[i & 1], a + (i >> 1) * 32768, lbase + L_EX + lw * 4096 + i * 1024);
      else if (i < 8) glds(voW[i & 1], wv + ((i >> 1) - 1) * 2048, lbase + L_EW + ((i >> 1) - 2) * WSLAB + lw * 2048 + (i & 1) * 1024);
      else glds(voW[i & 1], wv, lbase + ABYTES + 2 * WSLAB + lw * 2048 + (i & 1) * 1024);
    };
    __builtin_amdgcn_s_setprio(3);
    stage(0); stage(1);
    GL1(G, 0u, (const char*)ws, 0);
    const bool gu = (hp & 7) != 3;
    const int bb = gu ? (hp - (hp > 3) - (hp > 11)) * 16 + panel : 0;
    const int ke = bb & 7, cb = ((bb >> 3) * 2 + (lw & 1)) ^ ((lw >> 1) * 4);
    const int kc = (bb >> 4) * 4 + ((bb >> 1) & 3), cq = ((bb >> 3) & 1) | ((bb & 1) << 1);
    u32x4 gv[16], dv[8];
    const uintptr_t gb = (uintptr_t)(((lw >> 1) ? w_up : w_gate) + (size_t)(ke * 128) * FF + cb * 64);
    const u32x4 grs = {(unsigned)gb, (unsigned)(gb >> 32) & 0xffffu, gu ? 127u * FF * 2 + 128u : 0u, 0x00020000u};
    const unsigned go = rg * (16u * FF * 2) + cg * 16u;
    const uintptr_t db = (uintptr_t)(w_down + (size_t)(kc * 64) * DM + cq * 256 + lw * 64);
    const u32x4 drs = {(unsigned)db, (unsigned)(db >> 32) & 0xffffu, gu ? 63u * DM * 2 + 128u : 0u, 0x00020000u};
    const unsigned dof = rg * 16384u + cg * 16u;
    auto strm = [&](auto I0, auto I1, auto W) {
      static_for<(decltype(I1)::value > decltype(I0)::value ? decltype(I1)::value - decltype(I0)::value : 0)>([&](auto I) {
        constexpr int i = decltype(I)::value + decltype(I0)::value;
        if constexpr (decltype(W)::value) waitv<decltype(W)::value>();
        if constexpr (i < 16) bl4nt(gv[i], go, grs, i * (FF * 2u)); else bl4nt(dv[i - 16], dof, drs, (i - 16) * 2048u);
      });
    };
    const int wrow = 8 * g + tq, wsw = (((wrow >> 1) & 1) << 2) | (((wrow >> 3) & 1) << 1);
    const unsigned csa = lbase + ABYTES + (lw < 3 ? lw : 0) * WSLAB + wrow * 128 + (((2 * (panel & 3) + (tp >> 1)) ^ wsw) * 16) + 8 * (tp & 1);
    waitv<11>();
    RAW_BARRIER();
    float mean = 0.f, var = 1.f, c0 = 0.f, c1 = 0.f;
    f32x4 sm = z4;
    u32x4 ga[2], ca;
    const char* gab = ws + (lw < 3 ? WS_CS + (hp * 3 + lw) * 2048 : WS_ST + panel * 1024 - 1024);
    const unsigned go0 = l * 16u + (lw < 3 ? 0u : 1024u);
    static_for<13>([&](auto I) {
      constexpr int S = decltype(I)::value - 1;
      if constexpr (S >= 0) {
        waitv<10 + n_x(S - 2) + n_x(S - 1)>();
        RAW_BARRIER();
      }
      if constexpr (S < 3) {
        u32x2 c[4];
        cs_read(c, csa + ((S + 1) % 3) * SLOT);
        stage(S + 3);
        cs_sum(c, c0, c1);
      } else stage(S + 3);
      if constexpr (S == 0) {
        asm volatile("" : "+v"(G));
        Tw = __builtin_amdgcn_readfirstlane(G) + 1u;
        if ((Tw & 0xfffu) == 0u) ++Tw;
        t12 = Tw & 0xfffu;
        if (tid == 256) flg[F_T] = Tw;
      }
      if constexpr (S == SB0) {
        const f32x4* rp = (const f32x4*)(smem + L_REC) + (l & 7);
        sm = ((rp[0] + rp[8]) + (rp[16] + rp[24])) + ((rp[32] + rp[40]) + (rp[48] + rp[56]));
        pinf(sm);
      }
      if constexpr (S == SB0 + 1) {
        mean = sm[0] * (1.0f / 1024.0f);
        var = __builtin_fmaf(-mean, mean, sm[2] * (1.0f / 1024.0f));
        if (lw == 3 && __ballot(!(var * 64.0f >= mean * mean)) != 0ull) {
#pragma unroll 1
          for (int r = 0; r < 8; ++r) {
            const float m = __builtin_bit_cast(float, __builtin_amdgcn_readlane(__builtin_bit_cast(int, mean), r));
            const u32x4* p = (const u32x4*)(x + (size_t)(row0 + r) * DM) + l * 2;
            const u32x4 a = p[0], c = p[1];
            float q = 0.f;
#pragma unroll
            for (int i = 0; i < 8; ++i) {
              const unsigned u = i < 4 ? a[i & 3] : c[i & 3];
              const float e0 = bflo(u) - m, e1 = bfhi(u) - m;
              q = __builtin_fmaf(e0, e0, q); q = __builtin_fmaf(e1, e1, q);
            }
            q = wave_total(q) * (1.0f / 1024.0f);
            if ((l & 7) == r) var = q;
          }
        }
        mean = rbf(mean); var = rbf(rbf(var) + 1e-5f);
        pin1(mean); pin1(var);
      }
      if constexpr (S == SB0 + 2) { var = rbf(sqrtf(var)); pin1(var); }
      if constexpr (S == SB0 + 3) { sm[1] = rbf(__builtin_fmaf(-mean, sm[3], sm[1]) / var); pinf(sm); }
      if constexpr (S == PUB) {
        if (lw == 3) {
          const float r = sm[1];
          const unsigned u = __float_as_uint(r);
          const unsigned key = (r == r ? ((u & 0x80000000u) ? (~u & 0xffff0000u) : (u | 0x80000000u)) : 0u) | (unsigned)(2047 - row0 - (l & 7));
          const unsigned km = dpp_umax<0x141>(dpp_umax<0x4e>(dpp_umax<0xb1>(key)));
          const unsigned md = __float_as_uint(mean) | (__float_as_uint(var) >> 16);
          if (l < 8) {
            gs2(WS_ST + (row0 + l) * 8, ws, u32x2{md, Tw});
            if (key == km) gs4(WS_CA + (panel * 16 + hp) * 16, ws, u32x4{key, md, Tw, Tw});
          }
        } else {
          const float c = sum_g(c0 + c1);
          if (l < 16) gs2(WS_CS + (((hp * 3 + lw) * 4 + (panel >> 2)) * 64 + (panel & 3) * 16 + l) * 8, ws, u32x2{__float_as_uint(c), Tw});
        }
      }
      if constexpr (S == GAS) { GL4(ga[0], go0, gab, 0, P_SC1); GL4(ga[1], l * 16u, gab, 1024, P_SC1); }
      static_for<8>([&](auto J) { if constexpr (ES0 + decltype(J)::value * ESL / 8 == S) last(decltype(J)::value); });
      if constexpr (S == CTS) touch4(min(((jj * 4 + lw) * 4 + l) * 128u, clen), pc0, lbase + L_DU);
      if constexpr (S == 0) glds(l * 16u, (const char*)(wphi + hp * 2048 + lw * 512), lbase + L_WP + lw * 1024);
    });
    waitv<10 + n_x(10) + n_x(11)>(); RAW_BARRIER();
    last(8); last(9);
    GL4(ca, WS_CA + lw * 1024 + l * 16, ws, 0, P_SC1);
    strm(IC(0), IC(NF), IC(0));
    auto ga_bad = [&]() { return (ga[0][1] ^ Tw) | (ga[0][3] ^ Tw) | (ga[1][1] ^ Tw) | (ga[1][3] ^ Tw); };
    auto ga_put = [&]() {
      if (lw < 3) {
        const float u0 = __uint_as_float(ga[0][0]) + __uint_as_float(ga[1][0]), u1 = __uint_as_float(ga[0][2]) + __uint_as_float(ga[1][2]);
        const f32x2 a = sw32(u0, u1);
        ((float*)(smem + L_C))[lw * 64 + (l & 31) * 2 + (l >> 5)] = a[0] + a[1];
      } else {
        *(f32x2*)(smem + L_MN + l * 8) = f32x2{-bfhi(ga[0][0]), -bfhi(ga[0][2])};
        *(f32x2*)(smem + L_RS + l * 8) = f32x2{__builtin_amdgcn_rcpf(bflo(ga[0][0])), __builtin_amdgcn_rcpf(bflo(ga[0][2]))};
        *(unsigned*)(smem + L_DN + l * 4) = (ga[0][0] & 0xffffu) | (ga[0][2] << 16);
      }
      lds_set(flg + F_ST + lw, 1u, l);
    };
    asm volatile("s_waitcnt vmcnt(%2)" : "+v"(ga[0]), "+v"(ga[1]) : "n"(3 + NF) : "memory");
    RAW_BARRIER();
    strm(IC(NF), IC(NF + NG), IC(0));
#pragma unroll 1
    for (unsigned spins = 0; spins < SPIN; ++spins) {
      if (all0(ga_bad())) break;
      asm volatile("global_load_dwordx4 %0, %2, %4 sc1\n\tglobal_load_dwordx4 %1, %3, %4 offset:1024 sc1\n\ts_waitcnt vmcnt(0)"
                   : "+v"(ga[0]), "+v"(ga[1]) : "v"(go0), "v"(l * 16u), "s"(gab) : "memory");
    }
    ga_put();
    strm(IC(NF + NG), IC(NF + NG + NH), IC(0));
    asm volatile("s_waitcnt vmcnt(%1)" : "+v"(ca) : "n"(NF + NG + NH) : "memory");
    __builtin_amdgcn_s_setprio(FP0);
#pragma unroll 1
    for (unsigned spins = 0; spins < SPIN; ++spins) {
      if (all0((ca[2] ^ Tw) | (ca[3] ^ Tw))) break;
      asm volatile("global_load_dwordx4 %0, %1, %2 sc1\n\ts_waitcnt vmcnt(0)" : "+v"(ca) : "v"(WS_CA + lw * 1024 + l * 16), "s"(ws) : "memory");
    }
    {
      unsigned m = dpp_umax<0x140>(dpp_umax<0x141>(dpp_umax<0x4e>(dpp_umax<0xb1>(ca[0]))));
      m = dpp_umax_r<0x143, 0xc>(dpp_umax_r<0x142, 0xa>(m));
      m = __builtin_amdgcn_readlane(m, 63);
      if (ca[0] == m) *(u32x2*)(flg + F_CA + 2 * lw) = u32x2{m, ca[1]};
      lds_set(flg + F_SD + lw, 1u, l);
    }
    const Sel sl = make_sel(l);
    unsigned voO[2];
#pragma unroll
    for (int i = 0; i < 2; ++i) voO[i] = (unsigned)((lw * 16 + i * 8 + srow) * 2048 + (sch ^ (((srow >> 1) & 1) | ((((srow >> 2) ^ i) & 1) << 2))) * 16);
    auto wotile = [&](int p) {
      const char* wv = (const char*)(wo + (size_t)(ptile(p) * 64) * DM + hp * 64);
      if (WX) return;
      waitv<WNO>();
      glds(voO[0], wv, lbase + p * 8192 + lw * 2048); glds(voO[1], wv, lbase + p * 8192 + lw * 2048 + 1024);
    };
    lds_wait4(flg + F_SD);
    u32x2 sel;
    {
      const u32x4 e0 = ldsr1(LDS_OFF(flg + F_CA)), e1 = ldsr1(LDS_OFF(flg + F_CA + 4));
      const bool p = e0[2] > e0[0], q = e1[2] > e1[0];
      const unsigned k0 = p ? e0[2] : e0[0], m0 = p ? e0[3] : e0[1], k1 = q ? e1[2] : e1[0], m1 = q ? e1[3] : e1[1];
      sel = k1 > k0 ? u32x2{k1, m1} : u32x2{k0, m0};
    }
    const unsigned key = __builtin_amdgcn_readfirstlane(sel[0]);
    const int idx = 2047 - (int)(key & 2047u);
    if (tid == 256) flg[F_SEL] = key;
    unsigned xd;
    GL1P(xd, l * 4u, (const char*)(x + (size_t)idx * DM + ke * 128));
    strm(IC(NF + NG + NH), IC(24), IC(WIN));
    if (WE || GL) { lds_wait4(flg + F_LD); wotile(12); wotile(13); wotile(15); }
    if (GL) { lds_wait4(flg + F_IM); __builtin_amdgcn_s_setprio(FPR); wotile(0); wotile(1); wotile(2); wotile(3); wotile(7); wotile(8); }
    asm volatile("s_waitcnt vmcnt(%9)" : "+v"(xd), "+v"(gv[0]), "+v"(gv[1]), "+v"(gv[2]), "+v"(gv[3]), "+v"(gv[4]), "+v"(gv[5]), "+v"(gv[6]), "+v"(gv[7]) : "n"(GL ? 26 : WE ? 14 : 8) : "memory");
    asm volatile("" : "+v"(gv[8]), "+v"(gv[9]), "+v"(gv[10]), "+v"(gv[11]), "+v"(gv[12]), "+v"(gv[13]), "+v"(gv[14]), "+v"(gv[15]));
    char* hs = smem + L_H + lw * 256;
    {
      const float sm = bfhi(sel[1]), rden = 1.0f / bflo(sel[1]);
      const unsigned d = pk2(bflo(xd) - sm, bfhi(xd) - sm);
      *(unsigned*)(hs + l * 4) = pk2(bflo(d) * rden, bfhi(d) * rden);
      const u32x4 x0 = *(const u32x4*)(hs + rg * 32), x1 = *(const u32x4*)(hs + rg * 32 + 16);
      f32x4 d0 = gemv2(x0[0], gv[0], gv[1], sl, z4), d1 = gemv2(x0[1], gv[2], gv[3], sl, z4);
      d0 = gemv2(x0[2], gv[4], gv[5], sl, d0); d1 = gemv2(x0[3], gv[6], gv[7], sl, d1);
      d0 = gemv2(x1[0], gv[8], gv[9], sl, d0); d1 = gemv2(x1[1], gv[10], gv[11], sl, d1);
      d0 = gemv2(x1[2], gv[12], gv[13], sl, d0); d1 = gemv2(x1[3], gv[14], gv[15], sl, d1);
      const u32x4 v = gemv_fin(d0 + d1, t12);
      if (gu && (l & 40) == 0) gs4(WS_GU + (cb * 16 + (lw >> 1) * 8 + ke) * 256 + cg * 32 + g * 16, ws, v);
    }
    __builtin_amdgcn_s_setprio(FPR);
    if (!WE && !GL) { lds_wait4(flg + F_LD); wotile(12); wotile(13); wotile(15); }
    if (!GL) { lds_wait4(flg + F_IM); wotile(0); wotile(1); wotile(2); wotile(3); wotile(7); wotile(8); }
    lds_wait4(flg + F_BL);
    u32x4 p[4];
    const char* gp = ws + WS_GU + kc * 4096;
    static_for<8>([&](auto I) {
      constexpr int i = decltype(I)::value;
      if constexpr (i == PE) { GL4(p[0], l * 16u, gp, 0, P_SC1); GL4(p[1], l * 16u, gp, 1024, P_SC1); GL4(p[2], l * 16u, gp, 2048, P_SC1); GL4(p[3], l * 16u, gp, 3072, P_SC1); }
      if constexpr (i < 7) wotile((0x4eba965u >> (4 * i)) & 15);
    });
    waitv<0>(); lds_set(flg + F_WO + lw, 1u, l);
    if (FPR != FPE) __builtin_amdgcn_s_setprio(FPE);
    waitv4<0>(p);
    asm volatile("" : "+v"(dv[0]), "+v"(dv[1]), "+v"(dv[2]), "+v"(dv[3]), "+v"(dv[4]), "+v"(dv[5]), "+v"(dv[6]), "+v"(dv[7]) :: "memory");
    if (gu) {
#pragma unroll 1
      for (unsigned spins = 0; spins < SPIN; ++spins) {
        const unsigned long long m0 = __ballot(bad4(p[0], t12) & 0xfffu), m1 = __ballot(bad4(p[1], t12) & 0xfffu), m2 = __ballot(bad4(p[2], t12) & 0xfffu),
                                 m3 = __ballot(bad4(p[3], t12) & 0xfffu);
        if ((m0 | m1 | m2 | m3) == 0ull) break;
        reload4c(p, l * 16u, gp, m0, m1, m2, m3);
      }
      float hv[4];
#pragma unroll
      for (int e = 0; e < 4; ++e) {
        const float gg = rbf(sum_g(untag12(p[0][e]) + untag12(p[1][e]))), uu = rbf(sum_g(untag12(p[2][e]) + untag12(p[3][e])));
        hv[e] = rbf(silu_f(gg)) * uu;
      }
      if (l < 16) *(u32x2*)(hs + l * 8) = u32x2{pk2(hv[0], hv[1]), pk2(hv[2], hv[3])};
      const u32x4 hp4 = *(const u32x4*)(hs + rg * 16);
      f32x4 d0 = gemv2(hp4[0], dv[0], dv[1], sl, z4), d1 = gemv2(hp4[1], dv[2], dv[3], sl, z4);
      d0 = gemv2(hp4[2], dv[4], dv[5], sl, d0); d1 = gemv2(hp4[3], dv[6], dv[7], sl, d1);
      const u32x4 v = gemv_fin(d0 + d1, t12);
      if ((l & 40) == 0) gs4(WS_Y + ((cq * 4 + lw) * 56 + kc) * 256 + cg * 32 + g * 16, ws, v);
    }
    if (FPG != FPE) __builtin_amdgcn_s_setprio(FPG);
    if ((idx >> 7) == panel) {
      const char* yb = ws + WS_Y + hp * 56 * 256 + lw * 4096;
      u32x2 xr;
      GL2(xr, r15 * 8u, (const char*)(x + (size_t)idx * DM + hp * 64), 0);
      const unsigned tm = lw < 3 ? 0xfffu : 0u;
      u32x4 pl[8];
      lds_wait4(flg + F_OS);
      if (PYD) __builtin_amdgcn_s_sleep(PYD);
      pollt<PPY>(p, pl, l * 16u, yb, t12, tm);
      asm volatile("" : "+v"(xr));
      f32x4 y;
#pragma unroll
      for (int e = 0; e < 4; ++e) y[e] = sum_g((untag12(p[0][e]) + untag12(p[1][e])) + (lw < 3 ? untag12(p[2][e]) + untag12(p[3][e]) : 0.f));
      if (lw) {
        if (l < 16) *(f32x4*)(hs + l * 16) = y;
        lds_set(flg + F_AL + lw, 1u, l);
      } else {
      if (l == 0) flg[F_AL] = 1u;
      lds_wait4(flg + F_AL);
      y = (y + *(const f32x4*)(hs + 256 + r15 * 16)) + (*(const f32x4*)(hs + 512 + r15 * 16) + *(const f32x4*)(hs + 768 + r15 * 16));
      const int row = idx & 127;
      const float mn = ((const float*)(smem + L_MN))[row], rs = ((const float*)(smem + L_RS))[row];
      const float r = __uint_as_float((key & 0x80000000u) ? (key & 0x7fff0000u) : (~key & 0xffff0000u));
      const float sg = rbf(1.0f / (1.0f + __expf(-r)));
      float gi[4];
#pragma unroll
      for (int e = 0; e < 4; ++e) {
        const float xe = (e & 1) ? bfhi(xr[e >> 1]) : bflo(xr[e >> 1]);
        gi[e] = silu_r(rbf(rbf(rbf(xe + mn) * rs) + rbf(rbf(y[e]) * sg)));
      }
      if (l < 16) *(u32x2*)(smem + L_GI + l * 8) = u32x2{pk2(gi[0], gi[1]), pk2(gi[2], gi[3])};
      lds_set(flg + F_GT, 1u, l);
      }
      asm volatile("s_waitcnt vmcnt(0)" :: PKEEP(pl) : "memory");
    }
    if (b == 0 && tid == 256) gs1(0u, ws, Tw);
    return;
  }
  const int s = w >> 3;
  const int wt = lw & 1, wc = lw >> 1;
  const char* ob = ws + WS_O + (size_t)(panel * 4 + lw) * 65536;
  const char* xfb = (const char*)(x + (size_t)(panel * 128 + lw * 32 + s * 16) * DM + hp * 64);
  u32x4 xv[2], gq[2];
  unsigned T;
  auto gate = [&]() {
    const float mn = ((const float*)(smem + L_MN))[lw * 32 + s * 16 + r15], rs = ((const float*)(smem + L_RS))[lw * 32 + s * 16 + r15];
#pragma unroll
    for (int hf = 0; hf < 2; ++hf) {
      unsigned x0 = xv[hf][0], x1 = xv[hf][1], x2 = xv[hf][2], x3 = xv[hf][3];
      sw16u(x0, x2); sw16u(x1, x3);
      xv[hf] = u32x4{x0, x1, x2, x3};
#pragma unroll
      for (int e = 0; e < 4; ++e) {
        const unsigned d = pk2(bflo(xv[hf][e]) + mn, bfhi(xv[hf][e]) + mn);
        const unsigned y = pk2(bflo(d) * rs, bfhi(d) * rs);
        gq[hf][e] = pk2(silu_r(bflo(y)), silu_r(bfhi(y)));
      }
    }
    pin2(gq); pin2(xv);
  };
  const int wrow = 8 * g + tq, wsw = (((wrow >> 1) & 1) << 2) | (((wrow >> 3) & 1) << 1);
  unsigned asw[3][2], wa[3][2];
#pragma unroll
  for (int q = 0; q < 3; ++q) {
#pragma unroll
    for (int ks = 0; ks < 2; ++ks) asw[q][ks] = lbase + (wt * 64 + r15) * 128 + (((4 * ks + g) ^ ((r15 >> 1) & 7)) * 16) + q * SLOT;
#pragma unroll
    for (int c = 0; c < 2; ++c) wa[q][c] = lbase + ABYTES + wrow * 128 + (((wc * 4 + 2 * c + (tp >> 1)) ^ wsw) * 16) + 8 * (tp & 1) + q * SLOT;
  }
  if (s) {
    u32x4 xa[4], wr[4];
    auto st_load = [&]() {
      const char* xb = (const char*)(x + (size_t)row0 * DM + lw * 256);
      const unsigned o = (l & 7) * 2048u + ((l >> 3) & 1) * 64u + g * 16u;
      GL4(xa[0], o, xb, 0, P_DEF); GL4(xa[1], o, xb, 128, P_DEF); GL4(xa[2], o, xb, 256, P_DEF); GL4(xa[3], o, xb, 384, P_DEF);
      const char* rb = (const char*)(w_r + lw * 256);
      const unsigned ro = (l & 1) * 64u + g * 16u;
      GL4(wr[0], ro, rb, 0, P_DEF); GL4(wr[1], ro, rb, 128, P_DEF); GL4(wr[2], ro, rb, 256, P_DEF); GL4(wr[3], ro, rb, 384, P_DEF);
    };
    auto st_sum = [&](auto NV) {
      waitv4<decltype(NV)::value>(xa); waitv4<decltype(NV)::value>(wr);
      f32x4 d0 = z4, d1 = z4, d2 = z4, d3 = z4;
#pragma unroll
      for (int j = 0; j < 4; ++j) {
        d0 = MFMA(ones, as8(xa[j]), d0); d1 = MFMA(as8(wr[j]), as8(xa[j]), d1);
        d2 = MFMA(as8(xa[j]), as8(xa[j]), d2); d3 = MFMA(as8(wr[j]), ones, d3);
      }
      const bool hf = l & 8;
      const float q2 = (l & 2) ? ((l & 1) ? d2[3] : d2[2]) : ((l & 1) ? d2[1] : d2[0]);
      if (g == tq) ldsw1(lbase + L_REC + (lw * 16 + r15) * 16, __builtin_bit_cast(u32x4, f32x4{d0[0], hf ? d1[1] : d1[0], q2, hf ? d3[1] : d3[0]}));
      SB();
    };
    f32x4 acc[4][2];
    bf16x8 w0, w1;
    float sc[4];
    {
#pragma unroll
      for (int t = 0; t < 4; ++t) { acc[t][0] = z4; acc[t][1] = z4; }
      Frag<2> f[2];
      auto rd = [&](Frag<2>& y, auto I, auto OW, unsigned pa, const unsigned (&pw)[2]) {
        constexpr int i = decltype(I)::value, ow = decltype(OW)::value + 2 * WSLAB;
        if constexpr (i < 4) ds_128<i * 2048>(y.af[i], pa);
        else if constexpr (i & 1) ds_tr<ow + 512>(y.wh[(i - 4) >> 1], pw[(i - 4) >> 1]);
        else ds_tr<ow>(y.wl[(i - 4) >> 1], pw[(i - 4) >> 1]);
      };
      auto step = [&](Frag<2>& c, Frag<2>& y, auto OW, unsigned pa, const unsigned (&pw)[2], auto RD) {
        static_for<8>([&](auto I) {
          constexpr int i = decltype(I)::value;
          acc[i & 3][i >> 2] = MFMA(cat8(c.wl[i >> 2], c.wh[i >> 2]), c.af[i & 3], acc[i & 3][i >> 2]);
          SB();
          if constexpr (i < 4 && decltype(RD)::value) { rd(y, IC(2 * i), OW, pa, pw); rd(y, IC(2 * i + 1), OW, pa, pw); SB(); }
        });
      };
      auto tile = [&](auto J, auto BAR, auto RD) {
        constexpr int j = decltype(J)::value, n = (j + 1) % 3;
        f[0].wait();
        step(f[0], f[1], IC(4096), asw[j][1], wa[j], IC(1));
        f[1].wait();
        if constexpr (decltype(BAR)::value) { __builtin_amdgcn_s_barrier(); asm volatile("" ::: "memory"); }
        if constexpr (!decltype(RD)::value) { if (l == 0) __hip_atomic_store(flg + F_BL + lw, 1u, __ATOMIC_RELAXED, __HIP_MEMORY_SCOPE_WORKGROUP); SB(); }
        step(f[1], f[0], IC(0), asw[n][0], wa[n], RD);
      };
      auto sents = [&]() {
        unsigned ln;
        asm volatile("v_mbcnt_lo_u32_b32 %0, -1, 0\n\tv_mbcnt_hi_u32_b32 %0, -1, %0" : "=v"(ln));
        u32x4 ff = {~0u, ~0u, ~0u, ~0u};
        asm volatile("" : "+v"(ff));
        const unsigned ko = (ln >> 3) * 2048u + (ln & 7) * 16u + ((panel ^ (ln >> 3)) & 15) * 128u;
        static_for<4>([&](auto J) { gs4o<decltype(J)::value * 1024>(ln * 16u, ws + WS_O + (size_t)(panel * 4 + lw) * 65536 + hp * 4096, ff); });
        gs4(ko, ws + WS_KVP + (hp * 4 + lw) * 32768, ff);
        gs4(ko ^ 17408u, ws + WS_KVP + (hp * 4 + lw) * 32768, ff);
        gs4m(ln * 16u, ws + WS_KVR + hp * 8192 + lw * 2048 + panel * 128, ff);
        SB();
      };
      TICK();
      static_for<8>([&](auto I) { rd(f[0], I, IC(0), asw[0][0], wa[0]); });
      static_for<13>([&](auto J) {
        constexpr int t = decltype(J)::value, j = t % 3;
        if constexpr (t == 0) st_load();
        if constexpr (t == SBP) { waitv<0>(); SB(); }
        tile(IC(j), IC(1), IC(1));
        if constexpr (t == 1) {
          st_sum(IC(0));
          if (lw == 0 && !(xcd & 1) && jj < 24) pf3((xcd >> 2) ^ 1);
          if (lw & 1) touch((l >> 2) * 2048u + (l & 3) * 128u, (const char*)wo + jj * 65536 + (lw >> 1) * 32768 + (xcd & 3) * 512, lbase + L_DU);
        }
        if constexpr (t == SBI) sents();
        if constexpr (PRE && t == PRE) {
          unsigned ln;
          asm volatile("v_mbcnt_lo_u32_b32 %0, -1, 0\n\tv_mbcnt_hi_u32_b32 %0, -1, %0" : "=v"(ln));
          const unsigned sl = ln & 15;
          const unsigned o = ln < 16 ? WS_KVP + (hp * 4 + lw) * 32768 + sl * 2048 + ((panel ^ sl) & 15) * 128
                           : ln < 32 ? WS_KVR + hp * 8192 + lw * 2048 + panel * 128 : WS_O + (panel * 4 + lw) * 65536 + hp * 4096 + (ln & 31) * 128;
          touch(o, ws, lbase + L_DU);
          SB();
        }
      });
      __builtin_amdgcn_s_barrier(); asm volatile("" ::: "memory");
      relane();
      asw[0][0] += L_EX; asw[0][1] += L_EX;
      GL4(xv[0], xo, xfb, 0, P_DEF); GL4(xv[1], xo, xfb, 64, P_DEF);
      wphi_frag(w0, w1, lbase + L_WP + wc * 2048 + (4 * g + tq) * 64 + tp * 8);
      lds_wait4(flg + F_ST);
      {
        const f32x4 c0 = *(const f32x4*)(smem + L_C + 512 + (wc * 32 + 4 * g) * 4), c1 = *(const f32x4*)(smem + L_C + 512 + (wc * 32 + 16 + 4 * g) * 4);
        f32x2 mr[4];
        rowst(mr, lbase + L_MN + (wt * 64 + r15) * 4);
#pragma unroll
        for (int t = 0; t < 4; ++t) {
          sc[t] = mr[t][1] * L2E;
          acc[t][0] += mr[t][0] * c0; acc[t][1] += mr[t][0] * c1;
          asm volatile("" : "+v"(sc[t]), "+v"(acc[t][0]), "+v"(acc[t][1]));
        }
      }
      asm volatile("s_waitcnt vmcnt(0)" : "+v"(xv[0]), "+v"(xv[1]), "+v"(w0), "+v"(w1));
      if (GB) gate();
      SB();
      lds_wait4(flg + F_SD);
      lds_wait1(flg + (BQ ? F_EX : F_IM) + lw);
      __builtin_amdgcn_s_setprio(BP);
      tile(IC(1), IC(0), IC(1)); tile(IC(2), IC(0), IC(1)); tile(IC(0), IC(0), IC(0));
    }
    bf16x8 pq[4][2];
    {
      f32x4 p[4][2];
      const float one[2] = {1.f, 1.f};
#pragma unroll
      for (int t = 0; t < 4; ++t) {
        const f32x4 v0 = acc[t][0], v1 = acc[t][1];
        const bf16x8 qp = as8(u32x4{pk2(v0[0], v0[1]), pk2(v0[2], v0[3]), pk2(v1[0], v1[1]), pk2(v1[2], v1[3])});
        p[t][0] = MFMA(w0, qp, z4); p[t][1] = MFMA(w1, qp, z4);
      }
      phi_map<false>(p, sc, one, [&](int t, const unsigned (&pe)[8]) {
        pq[t][0] = as8(u32x4{pe[0], pe[1], pe[2], pe[3]}); pq[t][1] = as8(u32x4{pe[4], pe[5], pe[6], pe[7]});
      });
    }
    asm volatile("" : "+v"(pq[0][0]), "+v"(pq[0][1]), "+v"(pq[1][0]), "+v"(pq[1][1]), "+v"(pq[2][0]), "+v"(pq[2][1]), "+v"(pq[3][0]), "+v"(pq[3][1]));
    if (!GB) gate();
    {
      f32x2 rd[4];
      u32x4 kf[4];
      const char* kb = ws + WS_KVR + hp * 8192 + wc * 4096;
      unsigned l16 = l * 16u;
      lds_wait4(flg + F_BL);
      lds_wait2(flg + F_SL + 2 * wc);
      __builtin_amdgcn_s_sleep(PD2);
      GL4(kf[0], l16, kb, 0, P_SC1); GL4(kf[1], l16, kb, 1024, P_SC1); GL4(kf[2], l16, kb, 2048, P_SC1); GL4(kf[3], l16, kb, 3072, P_SC1);
      lds_wait2(flg + F_KV + 2 * wc);
      {
        bf16x8 z0 = *(const bf16x8*)(smem + L_Z + wc * 128 + g * 16), z1 = *(const bf16x8*)(smem + L_Z + wc * 128 + 64 + g * 16);
        if (l & 1) { z0 = ones; z1 = ones; }
#pragma unroll
        for (int t = 0; t < 4; ++t) {
          const f32x4 a = MFMA(z0, pq[t][0], z4), c = MFMA(z1, pq[t][1], z4);
          const f32x2 r = {__builtin_amdgcn_rcpf(a[1]), __builtin_amdgcn_rcpf(c[1])};
          rd[t] = r * __builtin_amdgcn_rcpf(__builtin_fmaf(a[0], r[0], __builtin_fmaf(c[0], r[1], 1e-6f)));
          asm volatile("" : "+v"(rd[t]));
        }
      }
      const char* pb = ws + WS_O + (size_t)(panel * 4 + wt * 2) * 65536 + hp * 4096 + wc * 2048;
      unsigned l2 = l16 + 65536u, oa = lbase + L_EX + wc * 8192 + wt * 4096 + l * 16, fo = LDS_OFF(flg + F_OS + lw);
      asm volatile("" : "+s"(pb));
      asm volatile("" : "+v"(l2), "+v"(oa), "+v"(fo));
      waitv4<0>(kf);
      if (__builtin_expect(__ballot(max(max(kf[0][3], kf[1][3]), max(kf[2][3], kf[3][3])) == ~0u) != 0ull, 0))
#pragma unroll 1
        for (unsigned spins = 0; spins < SPIN; ++spins) {
          const unsigned long long m0 = __ballot(kf[0][3] == ~0u), m1 = __ballot(kf[1][3] == ~0u), m2 = __ballot(kf[2][3] == ~0u), m3 = __ballot(kf[3][3] == ~0u);
          if ((m0 | m1 | m2 | m3) == 0ull) break;
          reload4c(kf, l16, kb, m0, m1, m2, m3);
        }
      u32x4 ov[4];
      f32x4 n[4][4];
      auto mm = [&](int t, int i) { n[t][i] = MFMA(as8(kf[i]), pq[t][i >> 1], z4); SB(); };
#pragma unroll
      for (int i = 0; i < 8; ++i) mm(i >> 2, i & 3);
      static_for<4>([&](auto TT) {
        constexpr int t = decltype(TT)::value;
        f32x4 a = n[t][0] * rd[t][0], c = n[t][1] * rd[t][0];
        SB(); if (t < 2) mm(t + 2, 0);
        a += n[t][2] * rd[t][1];
        SB(); if (t < 2) mm(t + 2, 1);
        c += n[t][3] * rd[t][1];
        SB(); if (t < 2) mm(t + 2, 2);
        ov[t] = u32x4{pk2(a[0], a[1]), pk2(a[2], a[3]), pk2(c[0], c[1]), nosent(pk2(c[2], c[3]))};
        SB(); if (t < 2) mm(t + 2, 3);
        asm volatile("global_store_dwordx4 %0, %1, %2 offset:%3 sc1" :: "v"(t < 2 ? l16 : l2), "v"(ov[t]), "s"(pb), "n"((t & 1) * 1024) : "memory");
        SB();
      });
      asm volatile("ds_write_b128 %0, %1\n\tds_write_b128 %0, %2 offset:1024\n\tds_write_b128 %0, %3 offset:2048\n\tds_write_b128 %0, %4 offset:3072\n\tds_write_b32 %5, %5\n\ts_nop 0"
                   :: "v"(oa), "v"(ov[0]), "v"(ov[1]), "v"(ov[2]), "v"(ov[3]), "v"(fo) : "memory");
    }
    __builtin_amdgcn_s_setprio(0);
  } else {
    if (tid < 20) ((u32x4*)flg)[tid] = u32x4{0, 0, 0, 0};
    f32x4 acc[4][4];
    Frag<4> f[2];
    u32x4 fs;
    f32x4 c0, c1;
    f32x2 mr[4];
    s16x4 wq[4];
    unsigned mra, cca;
    {
#pragma unroll
      for (int t = 0; t < 4; ++t)
#pragma unroll
        for (int c = 0; c < 4; ++c) acc[t][c] = z4;
      auto rd = [&](Frag<4>& y, auto I, auto OW, unsigned pa, const unsigned (&pw)[2]) {
        constexpr int i = decltype(I)::value, ow = decltype(OW)::value;
        if constexpr (i < 4) ds_128<i * 2048>(y.af[i], pa);
        else {
          constexpr int c = (i - 4) >> 1;
          if constexpr (i & 1) ds_tr<ow + (c >> 1) * WSLAB + 512>(y.wh[c], pw[c & 1]);
          else ds_tr<ow + (c >> 1) * WSLAB>(y.wl[c], pw[c & 1]);
        }
      };
      auto step = [&](Frag<4>& c, Frag<4>& y, auto OW, unsigned pa, const unsigned (&pw)[2], auto RD) {
        static_for<decltype(RD)::value ? 16 : 8>([&](auto I) {
          constexpr int i = decltype(I)::value, t = i & 3, cb = i >> 2;
          if constexpr (decltype(RD)::value == 2) {
            if constexpr (cb < 2) mfma_t(acc[t][cb], cat8(c.wl[cb], c.wh[cb]), __builtin_bit_cast(u32x4, c.af[t]));
            else mfma_t(acc[t][cb], c.af[t], __builtin_bit_cast(u32x4, cat8(c.wl[cb], c.wh[cb])));
          } else if constexpr (cb < 2) acc[t][cb] = MFMA(cat8(c.wl[cb], c.wh[cb]), c.af[t], acc[t][cb]);
          else acc[t][cb] = MFMA(c.af[t], cat8(c.wl[cb], c.wh[cb]), acc[t][cb]);
          SB();
          if constexpr (i < 12 && decltype(RD)::value) { rd(y, I, OW, pa, pw); SB(); }
        });
      };
      __builtin_amdgcn_s_setprio(2);
#pragma unroll
      for (int t = 0; t < 4; ++t) asm volatile("" : "+v"(acc[t][0]), "+v"(acc[t][1]), "+v"(acc[t][2]), "+v"(acc[t][3]));
      TICK();
      static_for<12>([&](auto I) { rd(f[0], I, IC(0), asw[0][0], wa[0]); });
#pragma unroll 1
      for (int it = 0; it < 5; ++it) {
        static_for<3>([&](auto J) {
          constexpr int j = decltype(J)::value, n = (j + 1) % 3;
          f[0].wait();
          step(f[0], f[1], IC(4096), asw[j][1], wa[j], IC(1));
          if constexpr (j == 1) {
            const unsigned dx = it == 4 ? L_EX : 0, dw = it == 4 ? L_EW - ABYTES : 0;
            asw[0][0] += dx; asw[0][1] += dx; wa[0][0] += dw; wa[0][1] += dw;
            SB();
          }
          f[1].wait();
          if (j < 2 || __builtin_expect(it < 4, 1)) __builtin_amdgcn_s_barrier();
          asm volatile("" ::: "memory");
          step(f[1], f[0], IC(0), asw[n][0], wa[n], IC(1));
        });
      }
      relane();
      mra = lbase + L_MN + (wt * 64 + r15) * 4; cca = lbase + L_C + (wc * 32 + 4 * g) * 4;
      unsigned wpa = lbase + L_WP + wc * 2048 + (4 * g + tq) * 64 + tp * 8, fa = lbase + L_F;
      asm volatile("" : "+v"(mra), "+v"(cca), "+v"(wpa), "+v"(fa));
      f[0].wait();
      step(f[0], f[1], IC(4096), asw[0][1], wa[0], IC(2));
      f[1].wait();
      asm volatile("ds_read_b128 %0, %12 offset:%16\n\tds_read_b128 %1, %13\n\tds_read_b128 %2, %13 offset:64\n\t"
                   "ds_read2_b32 %3, %14 offset1:128\n\tds_read2_b32 %4, %14 offset0:16 offset1:144\n\tds_read2_b32 %5, %14 offset0:32 offset1:160\n\t"
                   "ds_read2_b32 %6, %14 offset0:48 offset1:176\n\tds_read_b64_tr_b16 %7, %15\n\tds_read_b64_tr_b16 %8, %15 offset:1024\n\t"
                   "ds_read_b64_tr_b16 %9, %15 offset:32\n\tds_read_b64_tr_b16 %10, %15 offset:1056\n\tds_read_b32 %11, %12 offset:%17"
                   : "=&v"(fs), "=&v"(c0), "=&v"(c1), "=&v"(mr[0]), "=&v"(mr[1]), "=&v"(mr[2]), "=&v"(mr[3]), "=&v"(wq[0]), "=&v"(wq[1]), "=&v"(wq[2]), "=&v"(wq[3]), "=&v"(T)
                   : "v"(fa), "v"(cca), "v"(mra), "v"(wpa), "n"(F_ST * 4), "n"(F_T * 4) : "memory");
      asm volatile("v_mov_b32 %0, %1\n\tds_write_b32 %0, %0" : "=v"(fa) : "s"(LDS_OFF(flg + F_LD + lw)) : "memory");
      SB();
      step(f[1], f[0], IC(0), asw[0][0], wa[0], IC(0));
    }
    if (AP != 2) __builtin_amdgcn_s_setprio(AP);
    asm volatile("s_waitcnt lgkmcnt(0)" : "+v"(fs), "+v"(c0), "+v"(c1), "+v"(mr[0]), "+v"(mr[1]), "+v"(mr[2]), "+v"(mr[3]), "+v"(wq[0]), "+v"(wq[1]), "+v"(wq[2]), "+v"(wq[3]), "+v"(T));
    if (__builtin_expect(!__builtin_amdgcn_readfirstlane(fs[0] & fs[1] & fs[2] & fs[3]), 1)) {
      lds_wait4(flg + F_ST);
      asm volatile("ds_read_b128 %0, %2\n\tds_read_b128 %1, %2 offset:64\n\ts_waitcnt lgkmcnt(0)" : "=&v"(c0), "=&v"(c1) : "v"(cca) : "memory");
      rowst(mr, mra);
    }
    T = __builtin_amdgcn_readfirstlane(T);
    const unsigned t12 = T & 0xfffu;
    const bf16x8 w0 = cat8(wq[0], wq[1]), w1 = cat8(wq[2], wq[3]);
    char* img = smem + lw * 8192;
    u32x4 vp[2][4];
    unsigned va = lbase + SLOT + ABYTES + lw * 4096 + l * 16, fe = LDS_OFF(flg + F_EX + lw), ia;
    {
      const int row = 4 * g + tq, sx = (((row >> 1) & 3) << 2) | (row & 1) | (((row >> 3) & 1) << 1);
      ia = LDS_OFF(img) + row * 128 + (((8 * wt + tp) ^ sx) * 8);
      const int sw = (((r15 >> 1) & 3) << 2) | (r15 & 1) | (((r15 >> 3) & 1) << 1);
      f32x4 p[4][2], m4[4];
      f32x2 cv;
      float sc[4], nrm[2];
#pragma unroll
      for (int t = 0; t < 4; ++t) {
        sc[t] = mr[t][1] * L2E;
        if (t < 2) nrm[t] = (g & 1) ? mr[2 * t + 1][1] : mr[2 * t][1];
        const f32x4 v0 = mr[t][0] * c0 + acc[t][0], v1 = mr[t][0] * c1 + acc[t][1];
        const bf16x8 kc = as8(u32x4{pk2(v0[0], v0[1]), pk2(v0[2], v0[3]), pk2(v1[0], v1[1]), pk2(v1[2], v1[3])});
        p[t][0] = MFMA(w0, kc, z4); p[t][1] = MFMA(w1, kc, z4);
      }
      auto vl = [&](int i0, int i1) {
#pragma unroll
        for (int i = i0; i < i1; ++i)
          acc[i & 3][2 + (i >> 2)] = MFMA(f[1].af[i & 3], cat8(f[1].wl[2 + (i >> 2)], f[1].wh[2 + (i >> 2)]), acc[i & 3][2 + (i >> 2)]);
      };
      asm volatile("" : "+v"(va), "+v"(fe), "+v"(ia));
      SB();
      phi_map<true>(p, sc, nrm, [&](int t, const unsigned (&pe)[8]) {
        if (t < 2) { SB(); vl(4 * t, 4 * t + 4); SB(); }
        if (t == 2) asm volatile("s_waitcnt lgkmcnt(0)" : "+v"(m4[0]), "+v"(m4[1]), "+v"(m4[2]), "+v"(m4[3]), "+v"(cv) :: "memory");
#pragma unroll
        for (int fb = 0; fb < 4; ++fb) *(u32x2*)(img + (t * 16 + r15) * 128 + (((4 * fb + g) ^ sw) * 8)) = u32x2{pe[2 * fb], pe[2 * fb + 1]};
        if (t == 1) {
          asm volatile("ds_read_b128 %0, %5\n\tds_read_b128 %1, %5 offset:64\n\tds_read_b128 %2, %5 offset:128\n\tds_read_b128 %3, %5 offset:192\n\tds_read2_b32 %4, %6 offset1:16"
                       : "=&v"(m4[0]), "=&v"(m4[1]), "=&v"(m4[2]), "=&v"(m4[3]), "=&v"(cv)
                       : "v"(lbase + L_MN + (wt * 64 + 4 * g) * 4), "v"(lbase + L_C + (64 + wc * 32 + r15) * 4) : "memory");
          SB();
        }
        if (t == VPT) {
#pragma unroll
          for (int d = 0; d < 2; ++d)
#pragma unroll
            for (int q = 0; q < 2; ++q) {
              const f32x4 a = m4[2 * q] * cv[d] + acc[2 * q][2 + d], c = m4[2 * q + 1] * cv[d] + acc[2 * q + 1][2 + d];
              vp[0][2 * q + d] = u32x4{pk2(a[0], a[1]), pk2(a[2], a[3]), pk2(c[0], c[1]), pk2(c[2], c[3])};
            }
          asm volatile("ds_write_b128 %0, %1\n\tds_write_b128 %0, %2 offset:1024\n\tds_write_b128 %0, %3 offset:2048\n\tds_write_b128 %0, %4 offset:3072"
                       :: "v"(va), "v"(vp[0][0]), "v"(vp[0][1]), "v"(vp[0][2]), "v"(vp[0][3]) : "memory");
        }
      });
    }
    const char* kb = ws + WS_KVP + (hp * 4 + lw) * 32768;
    {
      const unsigned da = lbase + L_DN + wt * 128 + g * 8;
      s16x4 tl[2][2], th[2][2];
#define IMG8 "ds_read_b64_tr_b16 %0, %8\n\tds_read_b64_tr_b16 %1, %8 offset:2048\n\tds_read_b64_tr_b16 %2, %9\n\tds_read_b64_tr_b16 %3, %9 offset:2048\n\t" \
             "ds_read_b64_tr_b16 %4, %8 offset:4096\n\tds_read_b64_tr_b16 %5, %8 offset:6144\n\tds_read_b64_tr_b16 %6, %9 offset:4096\n\tds_read_b64_tr_b16 %7, %9 offset:6144\n\t"
#define IMGO(c) c(tl[0][0]), c(th[0][0]), c(tl[0][1]), c(th[0][1]), c(tl[1][0]), c(th[1][0]), c(tl[1][1]), c(th[1][1])
      asm volatile("ds_write_b32 %10, %10\n\t" IMG8 : IMGO("=&v") : "v"(ia), "v"(ia ^ 32u), "v"(fe) : "memory");
      {
        u32x4 d0, d1, d2, d3;
        asm volatile("ds_read2_b64 %0, %4 offset1:4\n\tds_read2_b64 %1, %4 offset0:8 offset1:12\n\tds_read2_b64 %2, %5 offset1:4\n\tds_read2_b64 %3, %5 offset0:8 offset1:12"
                     : "=&v"(d0), "=&v"(d1), "=&v"(d2), "=&v"(d3) : "v"(da), "v"(da ^ 128u) : "memory");
        unsigned o = kvo(), zo = WS_ZP + ((hp * 4 + lw) * 16 + panel) * 128 + (r15 * 4 + g) * 16, ib = ia ^ 8192u, ic = ia ^ 8224u, fx;
        va ^= 4096u; fe ^= 4u;
        asm volatile("s_waitcnt lgkmcnt(4)" : IMGO("+v"), "+v"(o), "+v"(zo), "+v"(ib), "+v"(ic), "+v"(va), "+v"(fe), "+s"(kb) :: "memory");
        f32x4 kva[2][2], za[2];
        bf16x8 a[2][2];
#pragma unroll
        for (int q = 0; q < 2; ++q)
#pragma unroll
          for (int fb = 0; fb < 2; ++fb) {
            a[q][fb] = cat8(tl[q][fb], th[q][fb]);
#pragma unroll
            for (int d = 0; d < 2; ++d) kva[fb][d] = MFMA(a[q][fb], as8(vp[0][2 * q + d]), q ? kva[fb][d] : z4);
          }
        SB();
        s16x4 ul[2][2], uh[2][2];
#define IMGP(c) c(ul[0][0]), c(uh[0][0]), c(ul[0][1]), c(uh[0][1]), c(ul[1][0]), c(uh[1][0]), c(ul[1][1]), c(uh[1][1])
#define OTH "ds_read_b32 %10, %15\n\t" IMG8 "ds_read_b128 %11, %16\n\tds_read_b128 %12, %16 offset:1024\n\tds_read_b128 %13, %16 offset:2048\n\tds_read_b128 %14, %16 offset:3072"
        asm volatile("s_waitcnt lgkmcnt(0)" : "+v"(d0), "+v"(d1), "+v"(d2), "+v"(d3) :: "memory");
        asm volatile(OTH : IMGP("=&v"), "+v"(ib), "+v"(ic), "=&v"(fx), "=&v"(vp[1][0]), "=&v"(vp[1][1]), "=&v"(vp[1][2]), "=&v"(vp[1][3]) : "v"(fe), "v"(va) : "memory");
        SB();
#pragma unroll
        for (int fb = 0; fb < 2; ++fb) za[fb] = MFMA(a[1][fb], as8(d1), MFMA(a[0][fb], as8(d0), z4));
        SB();
        asm volatile("s_waitcnt lgkmcnt(0)" : IMGP("+v"), "+v"(fx), "+v"(vp[1][0]), "+v"(vp[1][1]), "+v"(vp[1][2]), "+v"(vp[1][3]) :: "memory");
        if (__builtin_expect(__builtin_amdgcn_readfirstlane(fx) == 0u, 0))
#pragma unroll 1
          for (unsigned spins = 0; spins < SPIN && __builtin_amdgcn_readfirstlane(fx) == 0u; ++spins)
            asm volatile(OTH "\n\ts_waitcnt lgkmcnt(0)" : IMGP("+v"), "+v"(ib), "+v"(ic), "+v"(fx), "+v"(vp[1][0]), "+v"(vp[1][1]), "+v"(vp[1][2]), "+v"(vp[1][3]) : "v"(fe), "v"(va) : "memory");
#pragma unroll
        for (int d = 0; d < 2; ++d)
#pragma unroll
          for (int q = 0; q < 2; ++q)
#pragma unroll
            for (int fb = 0; fb < 2; ++fb) kva[fb][d] = MFMA(cat8(ul[q][fb], uh[q][fb]), as8(vp[1][2 * q + d]), kva[fb][d]);
        SB();
#pragma unroll
        for (int d = 0; d < 2; ++d) {
          za[d] = MFMA(cat8(ul[1][d], uh[1][d]), as8(d3), MFMA(cat8(ul[0][d], uh[0][d]), as8(d2), za[d]));
          const f32x4 u = kva[0][d], v = kva[1][d];
          gs4(o ^ (d * 17408u), kb, u32x4{pk2(u[0], u[1]), pk2(u[2], u[3]), pk2(v[0], v[1]), nosent(pk2(v[2], v[3]))});
        }
        asm volatile("ds_write_b32 %0, %0" :: "v"(LDS_OFF(flg + F_IM + lw)) : "memory");
        const f32x4 z = (l & 1) ? za[1] : za[0];
        if (r15 < 2) gs4(zo, ws, u32x4{tag12(z[0], t12), tag12(z[1], t12), tag12(z[2], t12), tag12(z[3], t12)});
      }
    }
    GL4(xv[0], xo, xfb, 0, P_DEF); GL4(xv[1], xo, xfb, 64, P_DEF);
    const char* zb = ws + WS_ZP + (hp * 4 + lw) * 2048;
    u32x4 zp[2], pl[4];
    {
      u32x4 p[2];
      const unsigned one = (l & 1) ? 0x3f800000u : 0x3f80u;
      u32x4 s0 = {(l & 2) ? 0u : one, (l & 2) ? one : 0u, 0u, 0u}, s1 = {0u, 0u, s0[0], s0[1]};
      asm volatile("" : "+v"(s0), "+v"(s1));
      if (PD1) __builtin_amdgcn_s_sleep(PD1);
      poll2<PP1>(p, pl, l * 16u + panel * 2048, kb);
      asm volatile("" : "+v"(xv[0]), "+v"(xv[1]));
      GL4(zp[0], l * 16u, zb, 0, P_SC1); GL4(zp[1], l * 16u, zb, 1024, P_SC1);
      f32x4 u = MFMA(as8(s0), as8(p[1]), MFMA(as8(s0), as8(p[0]), z4)), v = MFMA(as8(s1), as8(p[1]), MFMA(as8(s1), as8(p[0]), z4));
#pragma unroll
      for (int e = 0; e < 4; ++e) { u[e] += ror8(u[e]); v[e] += ror8(v[e]); }
      gs4m(l * 16u, ws + WS_KVR + hp * 8192 + lw * 2048 + panel * 128, u32x4{pk2(u[0], u[1]), pk2(u[2], u[3]), pk2(v[0], v[1]), nosent(pk2(v[2], v[3]))});
      if (l == 0) __hip_atomic_store(flg + F_SL + lw, 1u, __ATOMIC_RELAXED, __HIP_MEMORY_SCOPE_WORKGROUP);
    }
    {
      asm volatile("s_waitcnt vmcnt(1)" : "+v"(zp[0]), "+v"(zp[1]) :: "memory");
      asm volatile("" :: "v"(pl[0]), "v"(pl[1]), "v"(pl[2]), "v"(pl[3]));
#pragma unroll 1
      for (unsigned spins = 0; spins < SPIN; ++spins) {
        const unsigned long long m0 = __ballot(bad4(zp[0], t12) & 0xfffu), m1 = __ballot(bad4(zp[1], t12) & 0xfffu);
        if ((m0 | m1) == 0ull) break;
        reload2(zp, l * 16u, zb, m0, m1);
      }
      float z[4];
#pragma unroll
      for (int e = 0; e < 4; ++e) { const float q = sum_g(untag12(zp[0][e]) + untag12(zp[1][e])); z[e] = q + ror8(q); }
      if (l < 8) *(u32x2*)(smem + L_Z + lw * 64 + (l & 3) * 16 + (l >> 2) * 8) = u32x2{pk2(z[0], z[1]), pk2(z[2], z[3])};
      lds_set(flg + F_KV + lw, 1u, l);
    }
    __builtin_amdgcn_s_setprio(0);
    gate();
  }
  {
    relane();
    u32x4 of[NS][2], o0[2];
    f32x4 acc[2][4];
#pragma unroll
    for (int t = 0; t < 2; ++t)
#pragma unroll
      for (int c = 0; c < 4; ++c) acc[t][c] = z4;
    tie8<1>(acc);
    unsigned wb[4], wx[4];
    {
      const int row = 4 * g + tq, f = ((row >> 1) & 1) | ((((row >> 2) ^ (row >> 3)) & 1) << 2);
#pragma unroll
      for (int nb = 0; nb < 4; ++nb) wb[nb] = lbase + s * 4096 + row * 128 + ((((tp & 2) + (nb & 1) + 4 * (nb >> 1)) ^ f) * 16) + (tp & 1) * 8;
    }
    const unsigned v0 = l * 16u + s * 3072u, v1 = l * 16u + s * 1024u + 1024u, v3 = l * 16u + 2048u - s * 1024u;
    auto oadr = [&](int h) { return ob + ptile(h) * 4096; };
    s16x4 wl[4], wh[4], el[2][4], eh[2][4];
    auto wrd = [&](auto H, auto NB) {
      constexpr int h = decltype(H)::value, nb = decltype(NB)::value, o = (h & 7) * 8192;
      if constexpr (h == 8) { wb[nb] += 65536u; asm volatile("" : "+v"(wb[nb])); }
      ds_tr<o>(wl[nb], wb[nb]); ds_tr<o + 2048>(wh[nb], wb[nb]);
    };
    unsigned xa = lbase + lw * 8192 + s * 4096 + l * 16, fa = LDS_OFF(flg + F_X + s * 4 + lw), fg = LDS_OFF(flg + F_PG + s * 4), one = 1u;
    if (s) lds_wait1(flg + F_GO + lw);
    else {
      lds_wait4(flg + F_OS);
      if (PD3) __builtin_amdgcn_s_sleep(PD3);
      if (l == 0) __hip_atomic_store(flg + F_GO + lw, 1u, __ATOMIC_RELAXED, __HIP_MEMORY_SCOPE_WORKGROUP);
    }
    static_for<NS>([&](auto H) { opair(of[decltype(H)::value], v0, v1, oadr(decltype(H)::value + 1)); });
    lds_wait4(flg + F_WO);
    const int idx = 2047 - (int)(lds_get(flg + F_SEL) & 2047u);
    {
      const unsigned a = lbase + L_EX + s * 8192 + lw * 2048 + l * 16;
      asm volatile("ds_read_b128 %0, %2\n\tds_read_b128 %1, %3" : "=&v"(o0[0]), "=&v"(o0[1]) : "v"(a + s * 1024u), "v"(a + 1024u - s * 1024u) : "memory");
    }
    static_for<4>([&](auto NB) { wrd(IC(0), NB); });
    f32x2 xf[8], gf[8];
#pragma unroll
    for (int i = 0; i < 8; ++i) {
      xf[i] = f32x2{bflo(xv[i >> 2][i & 3]), bfhi(xv[i >> 2][i & 3])}; gf[i] = f32x2{bflo(gq[i >> 2][i & 3]), bfhi(gq[i >> 2][i & 3])};
      asm volatile("" : "+v"(xf[i]), "+v"(gf[i]));
    }
    char* op = (char*)(out + (size_t)(panel * 128 + lw * 32 + s * 16) * DM + hp * 64);
    unsigned oo = (l & 7) * 2048u + (l & 8) * 8u + g * 16u;
    char* op2 = op + 16384;
    asm volatile("" : "+v"(oo), "+s"(op), "+s"(op2), "+v"(xa), "+v"(fa), "+v"(fg), "+v"(one), "+v"(o0[0]), "+v"(o0[1]));
    u32x4 pg;
    static_for<14>([&](auto H) {
      constexpr int h = decltype(H)::value, x0 = h == 10 || h == 11, x1 = h == 11 || h == 12;
      u32x4 (&o)[2] = h ? of[(h + NS - 1) % NS] : o0;
      if constexpr (h == 1) { if (s) __builtin_amdgcn_s_setprio(1); }
      static_for<4>([&](auto NB) {
        constexpr int nb = decltype(NB)::value;
        waitl2<2 * x1 + (3 - nb) * (2 + 2 * x1) + nb * (2 + 2 * x0) + (h == 5 || h == 13)>(wl[nb], wh[nb]);
        mfma_t(acc[1][nb], cat8(wl[nb], wh[nb]), o[1]); mfma_t(acc[0][nb], cat8(wl[nb], wh[nb]), o[0]);
        wrd(IC(h + 1), NB);
        if constexpr (x0) { ds_tr<(h - 4) * 8192>(el[h - 10][nb], wx[nb]); ds_tr<(h - 4) * 8192 + 2048>(eh[h - 10][nb], wx[nb]); }
      });
      if constexpr (h && h + NS < 16) opair(o, v0, h + NS < 14 ? v1 : v3, oadr(h + NS));
      if constexpr (h == 4) asm volatile("ds_write_b32 %0, %1" :: "v"(fg + lw * 4u), "v"(one) : "memory");
      if constexpr (h == 8) {
#pragma unroll
        for (int nb = 0; nb < 4; ++nb) { wx[nb] = wb[nb] ^ 4096u; asm volatile("" : "+v"(wx[nb])); }
      }
      if constexpr (h == 12) asm volatile("ds_read_b128 %0, %1" : "=v"(pg) : "v"(fg) : "memory");
      if constexpr (h < 13) ocheck<2 * ((h + NS < 15 ? h + NS : 15) - h - 1), true>(of[h % NS], v0, v1, oadr(h + 1));
    });
    asm volatile("s_waitcnt lgkmcnt(8)" : "+v"(pg));
    if (__builtin_expect(!__builtin_amdgcn_readfirstlane(pg[0] & pg[1] & pg[2] & pg[3]), 0)) lds_wait4(flg + F_PG + s * 4);
    asm volatile("s_nop 1\n\tds_write_b128 %0, %1\n\tds_write_b128 %0, %2 offset:1024\n\tds_write_b128 %0, %3 offset:2048\n\tds_write_b128 %0, %4 offset:3072\n\tds_write_b32 %5, %6"
                 :: "v"(xa), "v"(acc[1][0]), "v"(acc[1][1]), "v"(acc[1][2]), "v"(acc[1][3]), "v"(fa), "v"(one) : "memory");
    __builtin_amdgcn_s_setprio(0);
    ocheck<2, true>(of[13 % NS], v0, v3, oadr(14));
    waitl8<5>(wl, wh); waitl8<5>(el[0], eh[0]); waitl8<5>(el[1], eh[1]);
    static_for<4>([&](auto NB) { constexpr int nb = decltype(NB)::value; mfma_t(acc[0][nb], cat8(wl[nb], wh[nb]), of[13 % NS][0]); wrd(IC(15), NB); });
#pragma unroll
    for (int nb = 0; nb < 4; ++nb) mfma_t(acc[0][nb], cat8(el[0][nb], eh[0][nb]), of[13 % NS][1]);
    unsigned fx;
    fa ^= 16u; xa ^= 4096u;
    asm volatile("ds_read_b32 %0, %5\n\tds_read_b128 %1, %6\n\tds_read_b128 %2, %6 offset:1024\n\tds_read_b128 %3, %6 offset:2048\n\tds_read_b128 %4, %6 offset:3072"
                 : "=&v"(fx), "=&v"(acc[1][0]), "=&v"(acc[1][1]), "=&v"(acc[1][2]), "=&v"(acc[1][3]) : "v"(fa), "v"(xa) : "memory");
    ocheck<0, true>(of[14 % NS], v0, v3, oadr(15));
#pragma unroll
    for (int nb = 0; nb < 4; ++nb) mfma_t(acc[0][nb], cat8(el[1][nb], eh[1][nb]), of[14 % NS][1]);
    waitl8<5>(wl, wh);
#pragma unroll
    for (int nb = 0; nb < 4; ++nb) mfma_t(acc[0][nb], cat8(wl[nb], wh[nb]), of[14 % NS][0]);
    if (__builtin_expect((idx >> 4) == panel * 8 + lw * 2 + s, 0)) {
      lds_wait1(flg + F_GT);
      relane();
      if (r15 == (idx & 15)) {
        const u32x2* gi = (const u32x2*)(smem + L_GI + (g >> 1) * 32 + (g & 1) * 8);
#pragma unroll
        for (int i = 0; i < 4; ++i) {
          const u32x2 a = gi[(i & 1) * 2 + (i >> 1) * 8];
          gf[2 * i] = f32x2{bflo(a[0]), bfhi(a[0])}; gf[2 * i + 1] = f32x2{bflo(a[1]), bfhi(a[1])};
        }
      }
    }
    asm volatile("s_waitcnt lgkmcnt(0)" : "+v"(fx), "+v"(acc[1][0]), "+v"(acc[1][1]), "+v"(acc[1][2]), "+v"(acc[1][3]) :: "memory");
    if (__builtin_expect(__builtin_amdgcn_readfirstlane(fx) == 0u, 0))
#pragma unroll 1
    for (unsigned spins = 0; spins < SPIN && __builtin_amdgcn_readfirstlane(fx) == 0u; ++spins)
      asm volatile("s_sleep 1\n\tds_read_b32 %0, %5\n\tds_read_b128 %1, %6\n\tds_read_b128 %2, %6 offset:1024\n\tds_read_b128 %3, %6 offset:2048\n\t"
                   "ds_read_b128 %4, %6 offset:3072\n\ts_waitcnt lgkmcnt(0)"
                   : "+v"(fx), "+v"(acc[1][0]), "+v"(acc[1][1]), "+v"(acc[1][2]), "+v"(acc[1][3]) : "v"(fa), "v"(xa) : "memory");
#pragma unroll
    for (int i = 0; i < 8; ++i) xf[i] = __builtin_elementwise_fma(f32x2{acc[1][i >> 1][2 * (i & 1)], acc[1][i >> 1][2 * (i & 1) + 1]}, gf[i], xf[i]);
    asm volatile("" : "+v"(xf[0]), "+v"(xf[1]), "+v"(xf[2]), "+v"(xf[3]), "+v"(xf[4]), "+v"(xf[5]), "+v"(xf[6]), "+v"(xf[7]),
                 "+v"(acc[0][0]), "+v"(acc[0][1]), "+v"(acc[0][2]), "+v"(acc[0][3]));
    unsigned rr[8];
#pragma unroll
    for (int i = 0; i < 8; ++i) {
      const f32x2 r = __builtin_elementwise_fma(f32x2{acc[0][i >> 1][2 * (i & 1)], acc[0][i >> 1][2 * (i & 1) + 1]}, gf[i], xf[i]);
      rr[i] = pk2(r[0], r[1]);
    }
    {
      sw16u(rr[0], rr[2]); sw16u(rr[1], rr[3]); sw16u(rr[4], rr[6]); sw16u(rr[5], rr[7]);
      unsigned a0, a1, a2, a3, c0, c1, c2, c3;
#define CD(d, x, y) "v_cndmask_b32_dpp " d ", " x ", " y ", vcc row_ror:8 row_mask:0xf bank_mask:0xf\n\t"
      asm volatile("s_mov_b32 vcc_lo, 0x00ff00ff\n\ts_mov_b32 vcc_hi, 0x00ff00ff\n\t" CD("%0", "%12", "%8") CD("%2", "%14", "%10") CD("%1", "%13", "%9") CD("%3", "%15", "%11")
                   "s_not_b64 vcc, vcc\n\t" CD("%4", "%8", "%12") CD("%5", "%9", "%13") CD("%6", "%10", "%14") CD("%7", "%11", "%15")
                   : "=&v"(a0), "=&v"(a1), "=&v"(a2), "=&v"(a3), "=&v"(c0), "=&v"(c1), "=&v"(c2), "=&v"(c3)
                   : "v"(rr[0]), "v"(rr[1]), "v"(rr[2]), "v"(rr[3]), "v"(rr[4]), "v"(rr[5]), "v"(rr[6]), "v"(rr[7]) : "vcc", "scc");
      const u32x4 a = {a0, a1, a2, a3}, c = {c0, c1, c2, c3};
      asm volatile("global_store_dwordx4 %0, %1, %3 sc1\n.Lkf_last:\n\tglobal_store_dwordx4 %0, %2, %4 sc1" :: "v"(oo), "v"(a), "v"(c), "s"(op), "s"(op2) : "memory");
    }
    if (s) {
    } else {
    }
  }
}

extern "C" void kernel_launch(void* const* d_in, const int* in_sizes, int n_in,
                              void* d_out, int out_size, void* d_ws, size_t ws_size,
                              hipStream_t stream) {
  (void)hipFuncSetAttribute((const void*)kf_main, hipFuncAttributeMaxDynamicSharedMemorySize, LDSM);
  kf_main<<<256, 768, LDSM, stream>>>((const u16*)d_in[0], (const u16*)d_in[1], (char*)d_ws, (const u16*)d_in[5], (const u16*)d_in[2],
                                      (const u16*)d_in[3], (const u16*)d_in[6], (const u16*)d_in[7], (const u16*)d_in[8], (u16*)d_out);
}
```

```cpp
#include <hip/hip_runtime.h>
#include <stdint.h>
#include <utility>

typedef __attribute__((ext_vector_type(8))) short bf16x8;
typedef __attribute__((ext_vector_type(4))) short s16x4;
typedef __attribute__((ext_vector_type(4))) float f32x4;
typedef __attribute__((ext_vector_type(2))) float f32x2;
typedef __attribute__((ext_vector_type(4))) unsigned u32x4;
typedef __attribute__((ext_vector_type(2))) unsigned u32x2;
typedef __attribute__((ext_vector_type(2))) __bf16 bfx2;
typedef unsigned short u16;

#define DI __device__ __forceinline__
#define MFMA(a, b, c) __builtin_amdgcn_mfma_f32_16x16x32_bf16((a), (b), (c), 0, 0, 0)
#define SB() __builtin_amdgcn_sched_barrier(0)
#define IC(x) std::integral_constant<int, (x)>{}

DI unsigned pk2(float a, float b) { f32x2 v = {a, b}; return __builtin_bit_cast(unsigned, __builtin_convertvector(v, bfx2)); }
DI float bflo(unsigned u) { return __uint_as_float(u << 16); }
DI float bfhi(unsigned u) { return __uint_as_float(u & 0xffff0000u); }
DI float rbf(float x) { return (float)(__bf16)x; }
DI f32x2 rb2(f32x2 v) { return f32x2{__uint_as_float(pk2(0.f, v[0])), __uint_as_float(pk2(0.f, v[1]))}; }
DI bf16x8 cat8(s16x4 lo, s16x4 hi) { return __builtin_shufflevector(lo, hi, 0, 1, 2, 3, 4, 5, 6, 7); }
DI bf16x8 as8(u32x4 v) { return __builtin_bit_cast(bf16x8, v); }
template <int... I, class F> DI void static_for_impl(std::integer_sequence<int, I...>, F&& f) { (f(std::integral_constant<int, I>{}), ...); }
template <int N, class F> DI void static_for(F&& f) { static_for_impl(std::make_integer_sequence<int, N>{}, f); }
template <int N> DI void waitv() { asm volatile("s_waitcnt vmcnt(%0)" ::"n"(N) : "memory"); }
#define RAW_BARRIER() do { asm volatile("s_waitcnt lgkmcnt(0)" ::: "memory"); __builtin_amdgcn_s_barrier(); asm volatile("" ::: "memory"); } while (0)
#define TICK() do { SB(); RAW_BARRIER(); SB(); } while (0)
#define LAY 1
#define GAP(n) asm volatile("s_branch .Lkg" #n "\n.Lkh" #n ":\n\t.fill ((.Lkg" #n " - .Lkt" #n " - (.Lkh" #n " - kf_main)) & 4095) / 4, 4, 0xbf800000\n.Lkg" #n ":" ::: "memory")
#define MARK(n) asm volatile(".Lkt" #n ":" ::: "memory")
#define PAD(n) do { unsigned d_; asm volatile(".Lkt" #n ":\n\ts_mov_b32 %0, 0xbe801d6a" : "=s"(d_)); } while (0)
#define VISIT(n) do { const char* p_; int o_; asm volatile("s_getpc_b64 %0\n.Lkv" #n ":\n\ts_mov_b32 %1, .Lkt" #n " - .Lkv" #n " + 4" : "=s"(p_), "=s"(o_)); \
                      asm volatile("s_swappc_b64 vcc, %0" :: "s"(p_ + o_) : "vcc"); } while (0)
#define DS_B128(dst, addr, off) asm volatile("ds_read_b128 %0, %1 offset:%2" : "=v"(dst) : "v"(addr), "n"(off))
#define DS_TR(dst, addr, off) asm volatile("ds_read_b64_tr_b16 %0, %1 offset:%2" : "=v"(dst) : "v"(addr), "n"(off))
#define LDS_OFF(p) ((unsigned)(uintptr_t)(__attribute__((address_space(3))) const char*)(p))
enum { P_DEF = 0, P_SC1 = 1, P_NT = 2 };
template <int OFF, int POL> DI void gl4(u32x4& d, unsigned voff, const char* sb) {
  if constexpr (POL == P_SC1) asm volatile("global_load_dwordx4 %0, %1, %2 offset:%3 sc1" : "=v"(d) : "v"(voff), "s"(sb), "n"(OFF));
  else if constexpr (POL == P_NT) asm volatile("global_load_dwordx4 %0, %1, %2 offset:%3 nt" : "=v"(d) : "v"(voff), "s"(sb), "n"(OFF));
  else asm volatile("global_load_dwordx4 %0, %1, %2 offset:%3" : "=v"(d) : "v"(voff), "s"(sb), "n"(OFF));
}
#define GL4(dst, voff, sb, off, pol) gl4<off, pol>(dst, voff, sb)
#define GL2(dst, voff, sb, off) asm volatile("global_load_dwordx2 %0, %1, %2 offset:%3" : "=v"(dst) : "v"(voff), "s"(sb), "n"(off))
#define GL1(dst, voff, sb, off) asm volatile("global_load_dword %0, %1, %2 offset:%3 sc1" : "=v"(dst) : "v"(voff), "s"(sb), "n"(off))
DI void bl4nt(u32x4& d, unsigned voff, u32x4 rs, unsigned soff) {
  asm volatile("buffer_load_dwordx4 %0, %1, %2, %3 offen nt" : "=v"(d) : "v"(voff), "s"(rs), "s"(soff));
}
DI void mfma_t(f32x4& c, bf16x8 a, u32x4 bb) { asm volatile("v_mfma_f32_16x16x32_bf16 %0, %1, %2, %0" : "+v"(c) : "v"(a), "v"(bb)); }
template <int N> DI void mfma_w(f32x4& c, bf16x8 a, u32x4 bb) {
  asm volatile("s_waitcnt lgkmcnt(%3)\n\tv_mfma_f32_16x16x32_bf16 %0, %1, %2, %0" : "+v"(c) : "v"(a), "v"(bb), "n"(N));
}
template <int N> DI void tie8(f32x4 (&a)[2][4]) {
  asm volatile("s_nop %8" : "+v"(a[0][0]), "+v"(a[0][1]), "+v"(a[0][2]), "+v"(a[0][3]), "+v"(a[1][0]), "+v"(a[1][1]), "+v"(a[1][2]), "+v"(a[1][3]) : "n"(N));
}
template <int OFF> DI void ds_tr(s16x4& d, unsigned a) { DS_TR(d, a, OFF); }
template <int N> DI void waitv4(u32x4 (&v)[4]) { asm volatile("s_waitcnt vmcnt(%4)" : "+v"(v[0]), "+v"(v[1]), "+v"(v[2]), "+v"(v[3]) : "n"(N)); }
template <int N> DI void waitl8(s16x4 (&a)[4], s16x4 (&c)[4]) {
  asm volatile("s_waitcnt lgkmcnt(%8)" : "+v"(a[0]), "+v"(c[0]), "+v"(a[1]), "+v"(c[1]), "+v"(a[2]), "+v"(c[2]), "+v"(a[3]), "+v"(c[3]) : "n"(N));
}
DI void gs4(unsigned voff, const char* sb, u32x4 v) { asm volatile("global_store_dwordx4 %0, %1, %2 sc1\n\ts_nop 1" :: "v"(voff), "v"(v), "s"(sb) : "memory"); }
DI void gs1(unsigned voff, const char* sb, unsigned v) { asm volatile("global_store_dword %0, %1, %2 sc1" :: "v"(voff), "v"(v), "s"(sb) : "memory"); }
DI void glds(unsigned voff, const char* sbase, unsigned lds) {
  asm volatile("s_mov_b32 m0, %2\n\ts_nop 0\n\tglobal_load_lds_dwordx4 %0, %1" :: "v"(voff), "s"(sbase), "s"(lds) : "memory");
}
DI void touch(unsigned voff, const char* sbase, unsigned lds) {
  asm volatile("s_mov_b32 m0, %2\n\ts_nop 0\n\tglobal_load_lds_dword %0, %1" :: "v"(voff), "s"(sbase), "s"(lds) : "memory");
}
DI void touchn(unsigned voff, const char* sbase, unsigned lds) {
  asm volatile("s_mov_b32 m0, %2\n\ts_nop 0\n\tglobal_load_lds_dword %0, %1 nt" :: "v"(voff), "s"(sbase), "s"(lds) : "memory");
}
DI void touch4(unsigned voff, const char* sbase, unsigned lds) {
  asm volatile("s_mov_b64 exec, 15\n\ts_mov_b32 m0, %2\n\ts_nop 0\n\tglobal_load_lds_dword %0, %1\n\ts_mov_b64 exec, -1" :: "v"(voff), "s"(sbase), "s"(lds) : "memory");
}
DI float silu_f(float x) { return x / (1.0f + __expf(-x)); }
DI float silu_r(float x) { return x * __builtin_amdgcn_rcpf(1.0f + __expf(-x)); }
DI float dot2(unsigned a, unsigned b, float c) { return __builtin_amdgcn_fdot2_f32_bf16(__builtin_bit_cast(bfx2, a), __builtin_bit_cast(bfx2, b), c, false); }

template <int OFF> DI void gs4o(unsigned voff, const char* sb, u32x4 v) {
  asm volatile("global_store_dwordx4 %0, %1, %2 offset:%3 sc1\n\ts_nop 1" :: "v"(voff), "v"(v), "s"(sb), "n"(OFF) : "memory");
}
DI void gs4m(unsigned voff, const char* sb, u32x4 v) {
  asm volatile("s_mov_b64 exec, 0xff\n\tglobal_store_dwordx4 %0, %1, %2 sc1\n\ts_mov_b64 exec, -1\n\ts_nop 0" :: "v"(voff), "v"(v), "s"(sb) : "memory");
}
DI void gs2(unsigned voff, const char* sb, u32x2 v) { asm volatile("global_store_dwordx2 %0, %1, %2 sc1" :: "v"(voff), "v"(v), "s"(sb) : "memory"); }
#define GL1P(dst, voff, sb) asm volatile("global_load_dword %0, %1, %2" : "=v"(dst) : "v"(voff), "s"(sb))
DI u32x4 ldsr1(unsigned a) { u32x4 v; asm volatile("ds_read_b128 %0, %1\n\ts_waitcnt lgkmcnt(0)" : "=v"(v) : "v"(a) : "memory"); return v; }
DI void ldsw1(unsigned a, u32x4 v) { asm volatile("ds_write_b128 %0, %1" :: "v"(a), "v"(v) : "memory"); }
DI unsigned nosent(unsigned v) { return min(v, 0xfffffffeu); }

constexpr int DM = 1024, FF = 3584;
constexpr unsigned WS_ST = 4096;
constexpr unsigned WS_CA = WS_ST + 2048 * 8;
constexpr unsigned WS_CS = WS_CA + 256 * 16;
constexpr unsigned WS_KVP = WS_CS + 256 * 1024;
constexpr unsigned WS_ZP = WS_KVP + 2048 * 1024;
constexpr unsigned WS_KVR = WS_ZP + 1024 * 128;
constexpr unsigned WS_O = WS_KVR + 16 * 8192;
constexpr unsigned WS_GU = WS_O + 4096 * 1024;
constexpr unsigned WS_Y = WS_GU + 56 * 4096;

constexpr int ABYTES = 16384, WSLAB = 8192, SLOT = 40960;
constexpr int L_EW = 3 * SLOT;
constexpr int L_EX = L_EW + 2 * WSLAB;
constexpr int L_REC = L_EW;
constexpr int L_Z = L_EX + ABYTES;
constexpr int L_WP = L_Z + 256;
constexpr int L_MN = L_WP + 4096;
constexpr int L_RS = L_MN + 512;
constexpr int L_DN = L_RS + 512;
constexpr int L_C = L_DN + 256;
constexpr int L_H = L_C + 768;
constexpr int L_GI = L_H + 1024;
constexpr int L_F = L_GI + 128;
constexpr int L_DU = L_F + 320;
constexpr int LDSM = L_DU + 256;
constexpr int F_KV = 0, F_WO = 4, F_PG = 8, F_X = 16, F_T = 24, F_GT = 25, F_SEL = 28, F_BL = 32, F_IM = 36, F_EX = 40, F_ST = 44, F_SD = 48, F_CA = 52, F_OS = 60, F_GO = 64, F_AL = 68, F_SL = 72, F_LD = 76;
#define SPINV (1u << 20)
constexpr unsigned SPIN = SPINV;
DI void pin1(float& v) { asm volatile("" : "+v"(v)); }
DI void pinf(f32x4& v) { asm volatile("" : "+v"(v)); }
DI void pin2(u32x4 (&v)[2]) { asm volatile("" : "+v"(v[0]), "+v"(v[1])); }
constexpr float L2E = 1.4426950408889634f;

template <int CTRL, int RMASK> DI float dpp_add(float v) {
  return v + __builtin_bit_cast(float, __builtin_amdgcn_update_dpp(0, __builtin_bit_cast(int, v), CTRL, RMASK, 0xf, true));
}
DI float wave_total(float v) {
  v = dpp_add<0x111, 0xf>(v); v = dpp_add<0x112, 0xf>(v); v = dpp_add<0x114, 0xf>(v); v = dpp_add<0x118, 0xf>(v);
  v = dpp_add<0x142, 0xa>(v); v = dpp_add<0x143, 0xc>(v);
  return __builtin_bit_cast(float, __builtin_amdgcn_readlane(__builtin_bit_cast(int, v), 63));
}
DI f32x2 sw32(float a, float b) {
  auto r = __builtin_amdgcn_permlane32_swap(__float_as_uint(a), __float_as_uint(b), false, false);
  return f32x2{__uint_as_float(r[0]), __uint_as_float(r[1])};
}
DI f32x2 sw16(float a, float b) {
  auto r = __builtin_amdgcn_permlane16_swap(__float_as_uint(a), __float_as_uint(b), false, false);
  return f32x2{__uint_as_float(r[0]), __uint_as_float(r[1])};
}
DI float ror8(float v) { return __builtin_bit_cast(float, __builtin_amdgcn_update_dpp(0, __builtin_bit_cast(int, v), 0x128, 0xf, 0xf, true)); }
DI float sum_g(float v) {
  f32x2 a = sw32(v, v); v = a[0] + a[1];
  a = sw16(v, v); return a[0] + a[1];
}
DI unsigned tag12(float v, unsigned t) { return ((__float_as_uint(v) + 0x800u) & 0xfffff000u) | t; }
DI float untag12(unsigned u) { return __uint_as_float(u & 0xfffff000u); }
DI unsigned bad4(u32x4 v, unsigned t) { return (v[0] ^ t) | (v[1] ^ t) | (v[2] ^ t) | (v[3] ^ t); }
DI bool all0(unsigned v) { return __ballot(v != 0u) == 0ull; }
DI void lds_set(unsigned* f, unsigned v, int l) {
  asm volatile("s_waitcnt lgkmcnt(0)" ::: "memory");
  if (l == 0) __hip_atomic_store(f, v, __ATOMIC_RELAXED, __HIP_MEMORY_SCOPE_WORKGROUP);
}
DI unsigned lds_get(unsigned* f) { return __builtin_amdgcn_readfirstlane(__hip_atomic_load(f, __ATOMIC_RELAXED, __HIP_MEMORY_SCOPE_WORKGROUP)); }
DI void lds_wait4(unsigned* f) {
#pragma unroll 1
  for (unsigned spins = 0; spins < SPIN; ++spins) {
    u32x4 v; asm volatile("ds_read_b128 %0, %1\n\ts_waitcnt lgkmcnt(0)" : "=v"(v) : "v"(LDS_OFF(f)) : "memory");
    if (__builtin_amdgcn_readfirstlane(v[0] & v[1] & v[2] & v[3])) break;
    __builtin_amdgcn_s_sleep(1);
  }
}

template <int NC> struct Frag {
  bf16x8 af[4]; s16x4 wl[NC], wh[NC];
  DI void wait() {
    if constexpr (NC == 4)
      asm volatile("s_waitcnt lgkmcnt(0)" : "+v"(af[0]), "+v"(af[1]), "+v"(af[2]), "+v"(af[3]), "+v"(wl[0]), "+v"(wh[0]), "+v"(wl[1]), "+v"(wh[1]),
                   "+v"(wl[2]), "+v"(wh[2]), "+v"(wl[3]), "+v"(wh[3]));
    else
      asm volatile("s_waitcnt lgkmcnt(0)" : "+v"(af[0]), "+v"(af[1]), "+v"(af[2]), "+v"(af[3]), "+v"(wl[0]), "+v"(wh[0]), "+v"(wl[1]), "+v"(wh[1]));
  }
};
template <int OFF> DI void ds_32(unsigned& d, unsigned a) { asm volatile("ds_read_b32 %0, %1 offset:%2" : "=v"(d) : "v"(a), "n"(OFF) : "memory"); }
template <int OFF, class V> DI void ds_128v(V& d, unsigned a) { DS_B128(d, a, OFF); }
template <int O> DI void ds_2x32(f32x2& d, unsigned a) { asm volatile("ds_read2_b32 %0, %1 offset0:%2 offset1:%3" : "=v"(d) : "v"(a), "n"(O), "n"(O + 128) : "memory"); }
template <int F> DI void raise(unsigned a) { asm volatile("ds_write_b32 %0, %0 offset:%1" :: "v"(a), "n"(F * 4) : "memory"); }
template <int OFF> DI void ds_w64(unsigned a, u32x2 v) { asm volatile("ds_write_b64 %0, %1 offset:%2" :: "v"(a), "v"(v), "n"(OFF) : "memory"); }
template <int T> DI void ds_if(u32x4 (&v)[2], unsigned a, int t) {
  asm volatile("s_cmp_eq_u32 %4, %5\n\ts_cselect_b64 exec, -1, 0\n\tds_read_b128 %0, %2\n\tds_read_b128 %1, %3\n\ts_mov_b64 exec, -1"
               : "+v"(v[0]), "+v"(v[1]) : "v"(a), "v"(a ^ 64u), "s"(t), "n"(T) : "scc", "memory");
}
template <int OFF> DI void ds_128(bf16x8& d, unsigned a) { DS_B128(d, a, OFF); }
DI void lds_wait2(unsigned* f) {
#pragma unroll 1
  for (unsigned spins = 0; spins < SPIN; ++spins) {
    u32x2 v; asm volatile("ds_read_b64 %0, %1\n\ts_waitcnt lgkmcnt(0)" : "=v"(v) : "v"(LDS_OFF(f)) : "memory");
    if (__builtin_amdgcn_readfirstlane(v[0] & v[1])) break;
    __builtin_amdgcn_s_sleep(1);
  }
}
DI void reload2(u32x4 (&p)[2], unsigned o, const char* sb, unsigned long long m0, unsigned long long m1) {
  asm volatile("s_mov_b64 exec, %4\n\tglobal_load_dwordx4 %0, %2, %3 sc1\n\ts_mov_b64 exec, %5\n\tglobal_load_dwordx4 %1, %2, %3 offset:1024 sc1\n\t"
               "s_mov_b64 exec, -1\n\ts_waitcnt vmcnt(0)"
               : "+v"(p[0]), "+v"(p[1]) : "v"(o), "s"(sb), "s"(m0), "s"(m1) : "memory");
}

template <int CTRL> DI unsigned dpp_umax(unsigned v) { return max(v, (unsigned)__builtin_amdgcn_update_dpp((int)v, (int)v, CTRL, 0xf, 0xf, false)); }
template <int CTRL, int RMASK> DI unsigned dpp_umax_r(unsigned v) { return max(v, (unsigned)__builtin_amdgcn_update_dpp((int)v, (int)v, CTRL, RMASK, 0xf, false)); }
DI unsigned now() { return (unsigned)__builtin_amdgcn_s_memrealtime(); }
DI void lds_wait1(unsigned* f) {
#pragma unroll 1
  for (unsigned spins = 0; spins < SPIN && lds_get(f) == 0u; ++spins) __builtin_amdgcn_s_sleep(1);
  asm volatile("" ::: "memory");
}

DI void cs_read(u32x2 (&c)[4], unsigned a) {
  asm volatile("ds_read_b64_tr_b16 %0, %4\n\tds_read_b64_tr_b16 %1, %4 offset:512\n\tds_read_b64_tr_b16 %2, %4 offset:4096\n\tds_read_b64_tr_b16 %3, %4 offset:4608"
               : "=&v"(c[0]), "=&v"(c[1]), "=&v"(c[2]), "=&v"(c[3]) : "v"(a) : "memory");
}
DI void cs_sum(u32x2 (&c)[4], float& s0, float& s1) {
  asm volatile("s_waitcnt lgkmcnt(0)" : "+v"(c[0]), "+v"(c[1]), "+v"(c[2]), "+v"(c[3]) :: "memory");
#pragma unroll
  for (int i = 0; i < 4; ++i) { s0 = dot2(c[i][0], 0x3f803f80u, s0); s1 = dot2(c[i][1], 0x3f803f80u, s1); }
  asm volatile("" : "+v"(s0), "+v"(s1));
}
#define WIN 0
#define NG 4
#define NH 12
#define PE 7
#define PP1 12
#define PD1 12
#define PD3 6
#define PPY 12
#define FP0 1
#define FPR 2
#define FPE 0
#define FPG 3
#define GAS 10
#define SB0 2
#define SBI 3
#define SBP 5
#define CTS 6
#define ES0 7
#define ESL 5
#define NF 4
#define VPT 2
#define WX 0
#define WE 0
#define AP 2
#define BP 1
#define BQ 0
#define WNO 16
#define D2 40
#define GB 1
#define PYD 16
#define PRE 0
#define GL 0
#define CT2 0
#define GIN 0
#define TP3 1
#define TPW 1
#define LM7 2
#define RM 0
#define XH 11
#define PW 1
#define BGO 1
#define BP3 1
#define FIN 0
#define FI 0
#define DL 0
#define CAE 0
#define TD 0
#define TDP 0
#define QW 2
#define NS 3
constexpr int PUB = SBP > SB0 + 3 ? SBP : SB0 + 3;
constexpr int n_e(int S) { int n = 0; for (int i = 0; i < 8; ++i) n += ES0 + i * ESL / 8 == S; return n; }
constexpr int n_x(int S) { return (S == PUB) + (S == 0) + (S == GAS ? 2 : 0) + (S == CTS) + n_e(S) + (S == 11 ? FI + CAE : 0); }
static_assert(SBP > SBI && PUB >= 3 && PUB < GAS && GAS <= 10 && (CTS <= 10 || CTS == 12) && ES0 > SB0 && ES0 + ESL <= 12 && FI + NF + NG + NH <= 24 && (!DL || FI + NF + NG + NH <= 16), "");
DI void wphi_frag(bf16x8& w0, bf16x8& w1, unsigned a) {
  s16x4 t0, t1, t2, t3;
  asm volatile("ds_read_b64_tr_b16 %0, %4\n\tds_read_b64_tr_b16 %1, %4 offset:1024\n\tds_read_b64_tr_b16 %2, %4 offset:32\n\t"
               "ds_read_b64_tr_b16 %3, %4 offset:1056\n\ts_waitcnt lgkmcnt(0)" : "=&v"(t0), "=&v"(t1), "=&v"(t2), "=&v"(t3) : "v"(a) : "memory");
  w0 = cat8(t0, t1); w1 = cat8(t2, t3);
}
DI float mx2(float a, float b) { return __builtin_elementwise_maximum(a, b); }
DI float mx3(float a, float b, float c) { return __builtin_elementwise_maximum(__builtin_elementwise_maximum(a, b), c); }
DI void bcast2(float m, f32x2& c0, f32x2& c1) {
  const f32x2 a = sw16(m, m);
  c0 = sw32(a[0], a[0]); c1 = sw32(a[1], a[1]);
}
template <bool NRM, class F> DI void phi_map(const f32x4 (&p)[4][2], const float (&sc)[4], const float (&nrm)[2], F&& sink) {
  float r[4];
  f32x2 c[4];
#pragma unroll
  for (int t = 0; t < 4; ++t) {
    const float mx = mx2(mx3(mx3(p[t][0][0], p[t][0][1], p[t][0][2]), p[t][0][3], p[t][1][0]), mx3(p[t][1][1], p[t][1][2], p[t][1][3]));
    const float nm = mx2(mx3(mx3(-p[t][0][0], -p[t][0][1], -p[t][0][2]), -p[t][0][3], -p[t][1][0]), mx3(-p[t][1][1], -p[t][1][2], -p[t][1][3]));
    const f32x2 a = sw32(mx, nm);
    r[t] = mx2(a[0], a[1]);
  }
#pragma unroll
  for (int q = 0; q < 2; ++q) {
    const f32x2 a = sw16(r[2 * q], r[2 * q + 1]);
    bcast2(mx2(a[0], a[1]), c[2 * q], c[2 * q + 1]);
  }
#pragma unroll
  for (int t = 0; t < 4; ++t) c[t] *= -sc[t];
#pragma unroll
  for (int q = 0; q < 2; ++q) {
    f32x2 e[2][8], sm[2];
#pragma unroll
    for (int i = 0; i < 8; ++i)
#pragma unroll
      for (int j = 0; j < 2; ++j) {
        const float v = p[2 * q + j][i >> 2][i & 3];
        const f32x2 a = __builtin_elementwise_fma(f32x2{v, v}, f32x2{sc[2 * q + j], -sc[2 * q + j]}, c[2 * q + j]);
        e[j][i] = f32x2{__builtin_amdgcn_exp2f(a[0]), __builtin_amdgcn_exp2f(a[1])};
      }
    if constexpr (NRM) {
#pragma unroll
    for (int j = 0; j < 2; ++j) {
      sm[j] = ((e[j][0] + e[j][1]) + (e[j][2] + e[j][3])) + ((e[j][4] + e[j][5]) + (e[j][6] + e[j][7]));
      sm[j] = sw32(sm[j][0], sm[j][1]);
    }
    const f32x2 a = sw16(sm[0][0] + sm[0][1], sm[1][0] + sm[1][1]);
    float rc = __builtin_amdgcn_rcpf(a[0] + a[1]);
    rc *= nrm[q];
    bcast2(rc, sm[0], sm[1]);
    }
#pragma unroll
    for (int j = 0; j < 2; ++j) {
      unsigned pe[8];
#pragma unroll
      for (int i = 0; i < 8; ++i) if (NRM) e[j][i] *= sm[j];
#pragma unroll
      for (int i = 0; i < 4; ++i) { pe[i] = pk2(e[j][2 * i][0], e[j][2 * i + 1][0]); pe[4 + i] = pk2(e[j][2 * i][1], e[j][2 * i + 1][1]); }
      sink(2 * q + j, pe);
    }
  }
}

struct Sel { unsigned e[4], o[4]; };
DI Sel make_sel(int l) {
  Sel s;
#pragma unroll
  for (int q = 0; q < 4; ++q) {
    const bool on = ((l & 7) >> 1) == q;
    s.e[q] = on ? ((l & 1) ? 0x01000c0cu : 0x0c0c0100u) : 0x0c0c0c0cu;
    s.o[q] = on ? ((l & 1) ? 0x03020c0cu : 0x0c0c0302u) : 0x0c0c0c0cu;
  }
  return s;
}
DI f32x4 gemv2(unsigned xp, u32x4 w0, u32x4 w1, const Sel& s, f32x4 d) {
  const u32x4 a0 = {__builtin_amdgcn_perm(xp, xp, s.e[0]), __builtin_amdgcn_perm(xp, xp, s.e[1]), __builtin_amdgcn_perm(xp, xp, s.e[2]), __builtin_amdgcn_perm(xp, xp, s.e[3])};
  const u32x4 a1 = {__builtin_amdgcn_perm(xp, xp, s.o[0]), __builtin_amdgcn_perm(xp, xp, s.o[1]), __builtin_amdgcn_perm(xp, xp, s.o[2]), __builtin_amdgcn_perm(xp, xp, s.o[3])};
  return MFMA(as8(a1), as8(w1), MFMA(as8(a0), as8(w0), d));
}
DI u32x4 gemv_fin(f32x4 d, unsigned t12) {
  u32x4 v;
#pragma unroll
  for (int e = 0; e < 4; ++e) { const float t = ror8(d[e]); v[e] = tag12(d[e] + sw32(t, t)[1], t12); }
  return v;
}

DI void opair(u32x4 (&o)[2], unsigned v0, unsigned v1, const char* p) { GL4(o[0], v0, p, 0, P_SC1); GL4(o[1], v1, p, 0, P_SC1); }
template <int N, bool COLD = false> DI void ocheck(u32x4 (&o)[2], unsigned v0, unsigned v1, const char* p) {
  asm volatile("s_waitcnt vmcnt(%2)" : "+v"(o[0]), "+v"(o[1]) : "n"(N));
  if (COLD && __builtin_expect(__ballot(max(o[0][3], o[1][3]) == ~0u) == 0ull, 1)) return;
#pragma unroll 1
  for (unsigned spins = 0; spins < SPIN && __ballot(max(o[0][3], o[1][3]) == ~0u) != 0ull; ++spins)
    asm volatile("s_sleep 2\n\tglobal_load_dwordx4 %0, %2, %4 sc1\n\tglobal_load_dwordx4 %1, %3, %4 sc1\n\ts_waitcnt vmcnt(0)"
                 : "+v"(o[0]), "+v"(o[1]) : "v"(v0), "v"(v1), "s"(p) : "memory");
}
DI void sw16u(unsigned& a, unsigned& b) { auto r = __builtin_amdgcn_permlane16_swap(a, b, false, false); a = r[0]; b = r[1]; }
template <int N> DI void waitl2(s16x4& a, s16x4& c) { asm volatile("s_waitcnt lgkmcnt(%2)" : "+v"(a), "+v"(c) : "n"(N)); }
template <int N> DI void tie4(f32x4 (&a)[4]) { asm volatile("s_nop %4" : "+v"(a[0]), "+v"(a[1]), "+v"(a[2]), "+v"(a[3]) : "n"(N)); }
DI void rowst(f32x2 (&m)[4], unsigned a) {
  asm volatile("ds_read2_b32 %0, %4 offset1:128\n\tds_read2_b32 %1, %4 offset0:16 offset1:144\n\tds_read2_b32 %2, %4 offset0:32 offset1:160\n\t"
               "ds_read2_b32 %3, %4 offset0:48 offset1:176\n\ts_waitcnt lgkmcnt(0)" : "=&v"(m[0]), "=&v"(m[1]), "=&v"(m[2]), "=&v"(m[3]) : "v"(a) : "memory");
}
DI void reload4c(u32x4 (&p)[4], unsigned o, const char* sb, unsigned long long m0, unsigned long long m1, unsigned long long m2, unsigned long long m3) {
  asm volatile("s_mov_b64 exec, %6\n\tglobal_load_dwordx4 %0, %4, %5 sc1\n\ts_mov_b64 exec, %7\n\tglobal_load_dwordx4 %1, %4, %5 offset:1024 sc1\n\t"
               "s_mov_b64 exec, %8\n\tglobal_load_dwordx4 %2, %4, %5 offset:2048 sc1\n\ts_mov_b64 exec, %9\n\tglobal_load_dwordx4 %3, %4, %5 offset:3072 sc1\n\t"
               "s_mov_b64 exec, -1\n\ts_waitcnt vmcnt(0)"
               : "+v"(p[0]), "+v"(p[1]), "+v"(p[2]), "+v"(p[3]) : "v"(o), "s"(sb), "s"(m0), "s"(m1), "s"(m2), "s"(m3) : "memory");
}

#define PL(r, o) "global_load_dwordx4 v[" #r ":" #r "+3], %[vo], %[sb] offset:" #o " sc1\n\t"
#define PC(r, m) "v_cmp_eq_u32 " m ", -1, v[" #r "+3]\n\t"
#define PM(d, r) "v_mov_b32 v[" #d "], v[" #r "]\n\tv_mov_b32 v[" #d "+1], v[" #r "+1]\n\tv_mov_b32 v[" #d "+2], v[" #r "+2]\n\tv_mov_b32 v[" #d "+3], v[" #r "+3]\n\t"
template <int D> DI void poll2(u32x4 (&p)[2], u32x4 (&q)[4], unsigned vo, const char* sb) {
  unsigned long long m; unsigned n = SPIN;
  asm volatile(PL(128, 0) PL(132, 1024) "s_sleep %[d]\n\t" PL(136, 0) PL(140, 1024)
               "1:\n\ts_waitcnt vmcnt(2)\n\t" PC(128, "vcc") PC(132, "%[m]") "s_or_b64 exec, vcc, %[m]\n\ts_cbranch_scc0 2f\n\t" PL(128, 0) PL(132, 1024)
               "s_mov_b64 exec, -1\n\ts_waitcnt vmcnt(2)\n\t" PC(136, "vcc") PC(140, "%[m]") "s_or_b64 exec, vcc, %[m]\n\ts_cbranch_scc0 3f\n\t" PL(136, 0) PL(140, 1024)
               "s_mov_b64 exec, -1\n\ts_sub_u32 %[n], %[n], 1\n\ts_cbranch_scc0 1b\n\ts_waitcnt vmcnt(0)\n"
               "3:\n\ts_mov_b64 exec, -1\n\t" PM(120, 136) PM(124, 140) "s_branch 4f\n"
               "2:\n\ts_mov_b64 exec, -1\n\t" PM(120, 128) PM(124, 132) "4:\n\ts_nop 1"
               : "={v[120:123]}"(p[0]), "={v[124:127]}"(p[1]), "=&{v[128:131]}"(q[0]), "=&{v[132:135]}"(q[1]), "=&{v[136:139]}"(q[2]), "=&{v[140:143]}"(q[3]),
                 [m] "=&s"(m), [n] "+s"(n)
               : [vo] "v"(vo), [sb] "s"(sb), [d] "n"(D) : "vcc", "scc", "memory");
}
#define PL4(r) PL(r, 0) PL(r+4, 1024) PL(r+8, 2048) PL(r+12, 3072)
#define PM2(o, r) "v_mov_b64 v[128+" #o ":129+" #o "], v[" #r "+" #o ":" #r "+1+" #o "]\n\t"
#define PM16(r) PM2(0, r) PM2(2, r) PM2(8, r) PM2(10, r) PM2(4, r) PM2(6, r) PM2(12, r) PM2(14, r)
#define PQ8(c) c "{v[96:99]}"(q[0]), c "{v[100:103]}"(q[1]), c "{v[104:107]}"(q[2]), c "{v[108:111]}"(q[3]), c "{v[112:115]}"(q[4]), c "{v[116:119]}"(q[5]), c "{v[120:123]}"(q[6]), c "{v[124:127]}"(q[7])
#define PB(a, r) "v_bitop3_b32 " a ", " a ", v[" #r "], %[t] bitop3:0xf6\n\t"
#define PB7(a, r) "v_xor_b32 " a ", %[t], v[" #r "]\n\t" PB(a, r+1) PB(a, r+2) PB(a, r+3) PB(a, r+4) PB(a, r+5) PB(a, r+6) PB(a, r+7)
#define PCT(r) "s_waitcnt vmcnt(4)\n\t" PB7("v128", r) PB7("v129", r+8) "v_and_b32 v128, 0xfff, v128\n\tv_and_or_b32 v128, v129, %[tm], v128\n\tv_cmp_ne_u32 vcc, 0, v128\n\ts_or_b64 exec, vcc, vcc\n\t"
template <int D> DI void pollt(u32x4 (&p)[4], u32x4 (&q)[8], unsigned vo, const char* sb, unsigned t, unsigned tm) {
  unsigned n = SPIN;
  asm volatile(PL4(96) "s_sleep %[d]\n\t" PL4(112)
               "1:\n\t" PCT(96) "s_cbranch_scc0 2f\n\t" PL4(96) "s_mov_b64 exec, -1\n\t" PCT(112) "s_cbranch_scc0 3f\n\t" PL4(112)
               "s_mov_b64 exec, -1\n\ts_sub_u32 %[n], %[n], 1\n\ts_cbranch_scc0 1b\n\ts_waitcnt vmcnt(0)\n"
               "3:\n\ts_mov_b64 exec, -1\n\t" PM16(112) "s_branch 4f\n"
               "2:\n\ts_mov_b64 exec, -1\n\t" PM16(96) "4:\n\ts_nop 1"
               : "=&{v[128:131]}"(p[0]), "=&{v[132:135]}"(p[1]), "=&{v[136:139]}"(p[2]), "=&{v[140:143]}"(p[3]), PQ8("=&"), [n] "+s"(n)
               : [vo] "v"(vo), [sb] "s"(sb), [t] "s"(t), [tm] "s"(tm), [d] "n"(D) : "vcc", "scc", "memory");
}
#define PKEEP(pl) "v"(pl[0]), "v"(pl[1]), "v"(pl[2]), "v"(pl[3]), "v"(pl[4]), "v"(pl[5]), "v"(pl[6]), "v"(pl[7])
extern "C" __global__ __launch_bounds__(768) __attribute__((aligned(4096))) void kf_main(const u16* __restrict__ x, const u16* __restrict__ wqkv, char* ws, const u16* __restrict__ w_r,
                                               const u16* __restrict__ wphi, const u16* __restrict__ wo, const u16* __restrict__ w_gate,
                                               const u16* __restrict__ w_up, const u16* __restrict__ w_down, u16* __restrict__ out) {
  extern __shared__ __attribute__((aligned(16))) char smem[];
  const int b = blockIdx.x;
  const int xcd = b & 7, jj = b >> 3;
  const int panel = (xcd >> 2) * 8 + (jj & 7), hp = (xcd & 3) * 4 + (jj >> 3);
  const int tid = threadIdx.x;
  const int w = __builtin_amdgcn_readfirstlane(tid >> 6), lw = w & 3;
  int l = tid & 63, r15 = l & 15, g = l >> 4, tq = r15 >> 2, tp = l & 3;
  unsigned xo;
  auto relane = [&]() {
    asm volatile("v_mbcnt_lo_u32_b32 %0, -1, 0\n\tv_mbcnt_hi_u32_b32 %0, -1, %0" : "=v"(l));
    r15 = l & 15; g = l >> 4; tq = r15 >> 2; tp = l & 3; xo = r15 * 2048u + g * 16u;
  };
  const unsigned lbase = LDS_OFF(smem);
  auto pf3 = [&](int half) {
    touch(l * 6144u, (const char*)wqkv + (size_t)(half * 512 + (jj & 7) * 64) * 6144 + (jj >> 3) * 2048 + (xcd & 3) * 512 + 384, lbase + L_DU);
  };
  const char* pc0; unsigned clen;
  asm volatile("s_getpc_b64 %0\n.Lkf_here:\n\ts_mov_b32 %1, .Lkf_last-.Lkf_here" : "=s"(pc0), "=s"(clen));
  if (__builtin_expect(w < 4, 1)) {
    if (__builtin_expect(w >= 2, 1)) {
      touch(min(((jj * 2 + w) & 15) * 4096u + l * 64u, clen), pc0, lbase + L_DU);
    }
    if (__builtin_expect(w == 0 && !(xcd & 1) && jj < 24, 1)) pf3(xcd >> 2);
  }
  unsigned* flg = (unsigned*)(smem + L_F);
  const bf16x8 ones = {0x3f80, 0x3f80, 0x3f80, 0x3f80, 0x3f80, 0x3f80, 0x3f80, 0x3f80};
  const f32x4 z4 = {0.f, 0.f, 0.f, 0.f};
  const int row0 = panel * 128 + hp * 8;
  auto ptile = [&](int p) { return p == 0 ? hp : p <= 12 ? ((hp | 3) + p) & 15 : hp ^ (p - 12); };
  auto kvo = [&]() { return (l >> 3) * 2048u + (l & 7) * 16u + ((panel ^ (l >> 3)) & 15) * 128u; };
  if (__builtin_expect((w >> 2) == 1, 1)) {
    const int srow = l >> 3, sch = l & 7, cg = sch, rg = srow;
    const int kt0 = 4 * (panel >> 2);
    unsigned G, Tw, t12;
    unsigned voA[2], voW[2];
    const char* sA = (const char*)(x + (size_t)panel * 128 * DM);
    const char* sQ = (const char*)(wqkv + hp * 64);
#pragma unroll
    for (int i = 0; i < 2; ++i) {
      const int row = lw * 32 + i * 8 + srow, wr = lw * 16 + i * 8 + srow;
      voA[i] = (unsigned)(row * 2048 + ((sch ^ ((row >> 1) & 7)) * 16));
      voW[i] = (unsigned)(wr * 6144 + ((sch ^ ((((wr >> 1) & 1) << 2) | (((wr >> 3) & 1) << 1))) * 16));
    }
    auto stage = [&](int S) {
      const int kt = (S + kt0) & 15;
      const char* a = sA + kt * 128; const char* wv = sQ + (size_t)kt * 64 * 6144;
      const unsigned d = lbase + (S % 3) * SLOT;
#pragma unroll
      for (int i = 0; i < 4; ++i) glds(voA[i & 1], a + (i >> 1) * 32768, d + lw * 4096 + i * 1024);
#pragma unroll
      for (int i = 0; i < 6; ++i) glds(voW[i & 1], wv + ((i >> 1) == 2 ? 0 : ((i >> 1) + 1) * 2048), d + ABYTES + (i >> 1) * WSLAB + lw * 2048 + (i & 1) * 1024);
    };
    auto last = [&](int i) {
      const int kt = (15 + kt0) & 15;
      const char* a = sA + kt * 128; const char* wv = sQ + (size_t)kt * 64 * 6144;
      if (i < 4) glds(voA[i & 1], a + (i >> 1) * 32768, lbase + L_EX + lw * 4096 + i * 1024);
      else if (i < 8) glds(voW[i & 1], wv + ((i >> 1) - 1) * 2048, lbase + L_EW + ((i >> 1) - 2) * WSLAB + lw * 2048 + (i & 1) * 1024);
      else glds(voW[i & 1], wv, lbase + ABYTES + 2 * WSLAB + lw * 2048 + (i & 1) * 1024);
    };
    __builtin_amdgcn_s_setprio(3);
    stage(0); stage(1);
    GL1(G, 0u, (const char*)ws, 0);
    const bool gu = (hp & 7) != 3;
    const int bb = gu ? (hp - (hp > 3) - (hp > 11)) * 16 + panel : 0;
    const int ke = bb & 7, cb = ((bb >> 3) * 2 + (lw & 1)) ^ ((lw >> 1) * 4);
    const int kc = (bb >> 4) * 4 + ((bb >> 1) & 3), cq = ((bb >> 3) & 1) | ((bb & 1) << 1);
    u32x4 gv[16], dv[8];
    const uintptr_t gb = (uintptr_t)(((lw >> 1) ? w_up : w_gate) + (size_t)(ke * 128) * FF + cb * 64);
    const u32x4 grs = {(unsigned)gb, (unsigned)(gb >> 32) & 0xffffu, gu ? 127u * FF * 2 + 128u : 0u, 0x00020000u};
    const unsigned go = rg * (16u * FF * 2) + cg * 16u;
    const uintptr_t db = (uintptr_t)(w_down + (size_t)(kc * 64) * DM + cq * 256 + lw * 64);
    const u32x4 drs = {(unsigned)db, (unsigned)(db >> 32) & 0xffffu, gu ? 63u * DM * 2 + 128u : 0u, 0x00020000u};
    const unsigned dof = rg * 16384u + cg * 16u;
    auto strm = [&](auto I0, auto I1, auto W) {
      static_for<(decltype(I1)::value > decltype(I0)::value ? decltype(I1)::value - decltype(I0)::value : 0)>([&](auto I) {
        constexpr int i = decltype(I)::value + decltype(I0)::value;
        if constexpr (decltype(W)::value) waitv<decltype(W)::value>();
        if constexpr (i < 16) bl4nt(gv[i], go, grs, i * (FF * 2u)); else bl4nt(dv[i - 16], dof, drs, (i - 16) * 2048u);
      });
    };
    const int wrow = 8 * g + tq, wsw = (((wrow >> 1) & 1) << 2) | (((wrow >> 3) & 1) << 1);
    const unsigned csa = lbase + ABYTES + (lw < 3 ? lw : 0) * WSLAB + wrow * 128 + (((2 * (panel & 3) + (tp >> 1)) ^ wsw) * 16) + 8 * (tp & 1);
    waitv<11>();
    RAW_BARRIER();
    float mean = 0.f, var = 1.f, c0 = 0.f, c1 = 0.f;
    f32x4 sm = z4;
    u32x4 ga[2], ca;
    const char* gab = ws + (lw < 3 ? WS_CS + (hp * 3 + lw) * 2048 : WS_ST + panel * 1024 - 1024);
    const unsigned go0 = l * 16u + (lw < 3 ? 0u : 1024u);
    static_for<13>([&](auto I) {
      constexpr int S = decltype(I)::value - 1;
      if constexpr (S >= 0) {
        waitv<10 + n_x(S - 2) + n_x(S - 1)>();
        RAW_BARRIER();
      }
      if constexpr (S < 3) {
        u32x2 c[4];
        cs_read(c, csa + ((S + 1) % 3) * SLOT);
        stage(S + 3);
        cs_sum(c, c0, c1);
      } else stage(S + 3);
      if constexpr (S == 0) {
        asm volatile("" : "+v"(G));
        Tw = __builtin_amdgcn_readfirstlane(G) + 1u;
        if ((Tw & 0xfffu) == 0u) ++Tw;
        t12 = Tw & 0xfffu;
        if (tid == 256) flg[F_T] = Tw;
      }
      if constexpr (S == SB0) {
        const f32x4* rp = (const f32x4*)(smem + L_REC) + (l & 7);
        sm = ((rp[0] + rp[8]) + (rp[16] + rp[24])) + ((rp[32] + rp[40]) + (rp[48] + rp[56]));
        pinf(sm);
      }
      if constexpr (S == SB0 + 1) {
        mean = sm[0] * (1.0f / 1024.0f);
        var = __builtin_fmaf(-mean, mean, sm[2] * (1.0f / 1024.0f));
        if (lw == 3 && __ballot(!(var * 64.0f >= mean * mean)) != 0ull) {
#pragma unroll 1
          for (int r = 0; r < 8; ++r) {
            const float m = __builtin_bit_cast(float, __builtin_amdgcn_readlane(__builtin_bit_cast(int, mean), r));
            const u32x4* p = (const u32x4*)(x + (size_t)(row0 + r) * DM) + l * 2;
            const u32x4 a = p[0], c = p[1];
            float q = 0.f;
#pragma unroll
            for (int i = 0; i < 8; ++i) {
              const unsigned u = i < 4 ? a[i & 3] : c[i & 3];
              const float e0 = bflo(u) - m, e1 = bfhi(u) - m;
              q = __builtin_fmaf(e0, e0, q); q = __builtin_fmaf(e1, e1, q);
            }
            q = wave_total(q) * (1.0f / 1024.0f);
            if ((l & 7) == r) var = q;
          }
        }
        mean = rbf(mean); var = rbf(rbf(var) + 1e-5f);
        pin1(mean); pin1(var);
      }
      if constexpr (S == SB0 + 2) { var = rbf(sqrtf(var)); pin1(var); }
      if constexpr (S == SB0 + 3) { sm[1] = rbf(__builtin_fmaf(-mean, sm[3], sm[1]) / var); pinf(sm); }
      if constexpr (S == PUB) {
        if (lw == 3) {
          const float r = sm[1];
          const unsigned u = __float_as_uint(r);
          const unsigned key = (r == r ? ((u & 0x80000000u) ? (~u & 0xffff0000u) : (u | 0x80000000u)) : 0u) | (unsigned)(2047 - row0 - (l & 7));
          const unsigned km = dpp_umax<0x141>(dpp_umax<0x4e>(dpp_umax<0xb1>(key)));
          const unsigned md = __float_as_uint(mean) | (__float_as_uint(var) >> 16);
          if (l < 8) {
            gs2(WS_ST + (row0 + l) * 8, ws, u32x2{md, Tw});
            if (key == km) gs4(WS_CA + (panel * 16 + hp) * 16, ws, u32x4{key, md, Tw, Tw});
          }
        } else {
          const float c = sum_g(c0 + c1);
          if (l < 16) gs2(WS_CS + (((hp * 3 + lw) * 4 + (panel >> 2)) * 64 + (panel & 3) * 16 + l) * 8, ws, u32x2{__float_as_uint(c), Tw});
        }
      }
      if constexpr (S == GAS) { GL4(ga[0], go0, gab, 0, P_SC1); GL4(ga[1], l * 16u, gab, 1024, P_SC1); }
      static_for<8>([&](auto J) { if constexpr (ES0 + decltype(J)::value * ESL / 8 == S) last(decltype(J)::value); });
      if constexpr (S == 11) { if (CAE) GL4(ca, WS_CA + lw * 1024 + l * 16, ws, 0, P_SC1); strm(IC(0), IC(FI), IC(0)); }
      if constexpr (S == CTS) touch4(min(((jj * 4 + lw) * 4 + l) * 128u, clen), pc0, lbase + L_DU);
      if constexpr (S == 0) glds(l * 16u, (const char*)(wphi + hp * 2048 + lw * 512), lbase + L_WP + lw * 1024);
    });
    waitv<10 + n_x(10) + n_x(11)>(); RAW_BARRIER();
    last(8); last(9);
    if (CTS == 12) touch4(min(((jj * 4 + lw) * 4 + l) * 128u, clen), pc0, lbase + L_DU);
    if (!CAE) GL4(ca, WS_CA + lw * 1024 + l * 16, ws, 0, P_SC1);
    auto tdv = [&]() { if (TD == 1) touchn(gu ? l * 2048u : 0u, (const char*)db, lbase + L_DU); if (TD == 2) touch(gu ? l * 2048u : 0u, (const char*)db, lbase + L_DU); };
    strm(IC(FI), IC(FI + NF), IC(0));
    if (TDP == 0) tdv();
    auto ga_bad = [&]() { return (ga[0][1] ^ Tw) | (ga[0][3] ^ Tw) | (ga[1][1] ^ Tw) | (ga[1][3] ^ Tw); };
    auto ga_put = [&]() {
      if (lw < 3) {
        const float u0 = __uint_as_float(ga[0][0]) + __uint_as_float(ga[1][0]), u1 = __uint_as_float(ga[0][2]) + __uint_as_float(ga[1][2]);
        const f32x2 a = sw32(u0, u1);
        ((float*)(smem + L_C))[lw * 64 + (l & 31) * 2 + (l >> 5)] = a[0] + a[1];
      } else {
        *(f32x2*)(smem + L_MN + l * 8) = f32x2{-bfhi(ga[0][0]), -bfhi(ga[0][2])};
        *(f32x2*)(smem + L_RS + l * 8) = f32x2{__builtin_amdgcn_rcpf(bflo(ga[0][0])), __builtin_amdgcn_rcpf(bflo(ga[0][2]))};
        *(unsigned*)(smem + L_DN + l * 4) = (ga[0][0] & 0xffffu) | (ga[0][2] << 16);
      }
      lds_set(flg + F_ST + lw, 1u, l);
    };
    asm volatile("s_waitcnt vmcnt(%2)" : "+v"(ga[0]), "+v"(ga[1]) : "n"(3 + NF + (CTS == 12) + FI + (TD && TDP == 0)) : "memory");
    RAW_BARRIER();
    strm(IC(FI + NF), IC(FI + NF + NG), IC(0));
    if (TDP == 1) tdv();
#pragma unroll 1
    for (unsigned spins = 0; spins < SPIN; ++spins) {
      if (all0(ga_bad())) break;
      asm volatile("global_load_dwordx4 %0, %2, %4 sc1\n\tglobal_load_dwordx4 %1, %3, %4 offset:1024 sc1\n\ts_waitcnt vmcnt(0)"
                   : "+v"(ga[0]), "+v"(ga[1]) : "v"(go0), "v"(l * 16u), "s"(gab) : "memory");
    }
    if (QW == 1) waitv<1 + NF + NG>();
    ga_put();
    strm(IC(FI + NF + NG), IC(FI + NF + NG + NH), IC(0));
    if (TDP == 2) tdv();
    asm volatile("s_waitcnt vmcnt(%1)" : "+v"(ca) : "n"(NF + NG + NH + (TD != 0)) : "memory");
    if (QW == 2) raise<F_GO>(lbase + L_F + lw * 4);
    __builtin_amdgcn_s_setprio(FP0);
#pragma unroll 1
    for (unsigned spins = 0; spins < SPIN; ++spins) {
      if (all0((ca[2] ^ Tw) | (ca[3] ^ Tw))) break;
      asm volatile("global_load_dwordx4 %0, %1, %2 sc1\n\ts_waitcnt vmcnt(0)" : "+v"(ca) : "v"(WS_CA + lw * 1024 + l * 16), "s"(ws) : "memory");
    }
    {
      unsigned m = dpp_umax<0x140>(dpp_umax<0x141>(dpp_umax<0x4e>(dpp_umax<0xb1>(ca[0]))));
      m = dpp_umax_r<0x143, 0xc>(dpp_umax_r<0x142, 0xa>(m));
      m = __builtin_amdgcn_readlane(m, 63);
      if (ca[0] == m) *(u32x2*)(flg + F_CA + 2 * lw) = u32x2{m, ca[1]};
      lds_set(flg + F_SD + lw, 1u, l);
    }
    const Sel sl = make_sel(l);
    unsigned voO[2];
#pragma unroll
    for (int i = 0; i < 2; ++i) voO[i] = (unsigned)((lw * 16 + i * 8 + srow) * 2048 + (sch ^ (((srow >> 1) & 1) | ((((srow >> 2) ^ i) & 1) << 2))) * 16);
    auto wotile = [&](int p) {
      const char* wv = (const char*)(wo + (size_t)(ptile(p) * 64) * DM + hp * 64);
      if (WX) return;
      waitv<WNO>();
      glds(voO[0], wv, lbase + p * 8192 + lw * 2048); glds(voO[1], wv, lbase + p * 8192 + lw * 2048 + 1024);
    };
    lds_wait4(flg + F_SD);
    u32x2 sel;
    {
      const u32x4 e0 = ldsr1(LDS_OFF(flg + F_CA)), e1 = ldsr1(LDS_OFF(flg + F_CA + 4));
      const bool p = e0[2] > e0[0], q = e1[2] > e1[0];
      const unsigned k0 = p ? e0[2] : e0[0], m0 = p ? e0[3] : e0[1], k1 = q ? e1[2] : e1[0], m1 = q ? e1[3] : e1[1];
      sel = k1 > k0 ? u32x2{k1, m1} : u32x2{k0, m0};
    }
    const unsigned key = __builtin_amdgcn_readfirstlane(sel[0]);
    const int idx = 2047 - (int)(key & 2047u);
    if (tid == 256) flg[F_SEL] = key;
    unsigned xd;
    GL1P(xd, l * 4u, (const char*)(x + (size_t)idx * DM + ke * 128));
    strm(IC(FI + NF + NG + NH), IC(DL ? 16 : 24), IC(WIN));
    if (WE || GL) { lds_wait4(flg + F_LD); wotile(12); wotile(13); wotile(15); }
    if (GL) { lds_wait4(flg + F_IM); __builtin_amdgcn_s_setprio(FPR); wotile(0); wotile(1); wotile(2); wotile(3); wotile(7); wotile(8); }
    asm volatile("s_waitcnt vmcnt(%9)" : "+v"(xd), "+v"(gv[0]), "+v"(gv[1]), "+v"(gv[2]), "+v"(gv[3]), "+v"(gv[4]), "+v"(gv[5]), "+v"(gv[6]), "+v"(gv[7]) : "n"((DL ? 0 : FI + NF + NG + NH > 16 ? 24 - (FI + NF + NG + NH) : 8) + (GL ? 18 : WE ? 6 : 0)) : "memory");
    asm volatile("" : "+v"(gv[8]), "+v"(gv[9]), "+v"(gv[10]), "+v"(gv[11]), "+v"(gv[12]), "+v"(gv[13]), "+v"(gv[14]), "+v"(gv[15]));
    char* hs = smem + L_H + lw * 256;
    {
      const float sm = bfhi(sel[1]), rden = 1.0f / bflo(sel[1]);
      const unsigned d = pk2(bflo(xd) - sm, bfhi(xd) - sm);
      *(unsigned*)(hs + l * 4) = pk2(bflo(d) * rden, bfhi(d) * rden);
      const u32x4 x0 = *(const u32x4*)(hs + rg * 32), x1 = *(const u32x4*)(hs + rg * 32 + 16);
      f32x4 d0 = gemv2(x0[0], gv[0], gv[1], sl, z4), d1 = gemv2(x0[1], gv[2], gv[3], sl, z4);
      d0 = gemv2(x0[2], gv[4], gv[5], sl, d0); d1 = gemv2(x0[3], gv[6], gv[7], sl, d1);
      d0 = gemv2(x1[0], gv[8], gv[9], sl, d0); d1 = gemv2(x1[1], gv[10], gv[11], sl, d1);
      d0 = gemv2(x1[2], gv[12], gv[13], sl, d0); d1 = gemv2(x1[3], gv[14], gv[15], sl, d1);
      const u32x4 v = gemv_fin(d0 + d1, t12);
      if (gu && (l & 40) == 0) gs4(WS_GU + (cb * 16 + (lw >> 1) * 8 + ke) * 256 + cg * 32 + g * 16, ws, v);
    }
    if (CT2) touch4(min(((jj * 4 + lw) * 4 + l) * 128u, clen), pc0, lbase + L_DU);
    __builtin_amdgcn_s_setprio(FPR);
    if (!WE && !GL) { lds_wait4(flg + F_LD); wotile(12); wotile(13); wotile(15); }
    if (!GL) { lds_wait4(flg + F_IM); wotile(0); wotile(1); wotile(2); wotile(3); wotile(7); wotile(8); }
    lds_wait4(flg + F_BL);
    if (DL == 1) strm(IC(16), IC(24), IC(WIN));
    u32x4 p[4];
    const char* gp = ws + WS_GU + kc * 4096;
    static_for<8>([&](auto I) {
      constexpr int i = decltype(I)::value;
      if constexpr (i == PE) { GL4(p[0], l * 16u, gp, 0, P_SC1); GL4(p[1], l * 16u, gp, 1024, P_SC1); GL4(p[2], l * 16u, gp, 2048, P_SC1); GL4(p[3], l * 16u, gp, 3072, P_SC1); }
      if constexpr (i < 7) wotile((0x4eba965u >> (4 * i)) & 15);
    });
    if (DL == 2) { lds_wait4(flg + F_SL); strm(IC(16), IC(24), IC(0)); waitv<8>(); } else waitv<0>();
    lds_set(flg + F_WO + lw, 1u, l);
    if (FPR != FPE) __builtin_amdgcn_s_setprio(FPE);
    waitv4<0>(p);
    asm volatile("" : "+v"(dv[0]), "+v"(dv[1]), "+v"(dv[2]), "+v"(dv[3]), "+v"(dv[4]), "+v"(dv[5]), "+v"(dv[6]), "+v"(dv[7]) :: "memory");
    if (gu) {
#pragma unroll 1
      for (unsigned spins = 0; spins < SPIN; ++spins) {
        const unsigned long long m0 = __ballot(bad4(p[0], t12) & 0xfffu), m1 = __ballot(bad4(p[1], t12) & 0xfffu), m2 = __ballot(bad4(p[2], t12) & 0xfffu),
                                 m3 = __ballot(bad4(p[3], t12) & 0xfffu);
        if ((m0 | m1 | m2 | m3) == 0ull) break;
        reload4c(p, l * 16u, gp, m0, m1, m2, m3);
      }
      float hv[4];
#pragma unroll
      for (int e = 0; e < 4; ++e) {
        const float gg = rbf(sum_g(untag12(p[0][e]) + untag12(p[1][e]))), uu = rbf(sum_g(untag12(p[2][e]) + untag12(p[3][e])));
        hv[e] = rbf(silu_f(gg)) * uu;
      }
      if (l < 16) *(u32x2*)(hs + l * 8) = u32x2{pk2(hv[0], hv[1]), pk2(hv[2], hv[3])};
      const u32x4 hp4 = *(const u32x4*)(hs + rg * 16);
      f32x4 d0 = gemv2(hp4[0], dv[0], dv[1], sl, z4), d1 = gemv2(hp4[1], dv[2], dv[3], sl, z4);
      d0 = gemv2(hp4[2], dv[4], dv[5], sl, d0); d1 = gemv2(hp4[3], dv[6], dv[7], sl, d1);
      const u32x4 v = gemv_fin(d0 + d1, t12);
      if ((l & 40) == 0) gs4(WS_Y + ((cq * 4 + lw) * 56 + kc) * 256 + cg * 32 + g * 16, ws, v);
    }
    if (FPG != FPE) __builtin_amdgcn_s_setprio(FPG);
    if ((idx >> 7) == panel) {
      const char* yb = ws + WS_Y + hp * 56 * 256 + lw * 4096;
      u32x2 xr;
      GL2(xr, r15 * 8u, (const char*)(x + (size_t)idx * DM + hp * 64), 0);
      const unsigned tm = lw < 3 ? 0xfffu : 0u;
      u32x4 pl[8];
      lds_wait4(flg + F_OS);
      if (PYD) __builtin_amdgcn_s_sleep(PYD);
      pollt<PPY>(p, pl, l * 16u, yb, t12, tm);
      asm volatile("" : "+v"(xr));
      f32x4 y;
#pragma unroll
      for (int e = 0; e < 4; ++e) y[e] = sum_g((untag12(p[0][e]) + untag12(p[1][e])) + (lw < 3 ? untag12(p[2][e]) + untag12(p[3][e]) : 0.f));
      if (lw) {
        if (l < 16) *(f32x4*)(hs + l * 16) = y;
        lds_set(flg + F_AL + lw, 1u, l);
      } else {
      if (l == 0) flg[F_AL] = 1u;
      lds_wait4(flg + F_AL);
      y = (y + *(const f32x4*)(hs + 256 + r15 * 16)) + (*(const f32x4*)(hs + 512 + r15 * 16) + *(const f32x4*)(hs + 768 + r15 * 16));
      const int row = idx & 127;
      const float mn = ((const float*)(smem + L_MN))[row], rs = ((const float*)(smem + L_RS))[row];
      const float r = __uint_as_float((key & 0x80000000u) ? (key & 0x7fff0000u) : (~key & 0xffff0000u));
      const float sg = rbf(1.0f / (1.0f + __expf(-r)));
      float gi[4];
#pragma unroll
      for (int e = 0; e < 4; ++e) {
        const float xe = (e & 1) ? bfhi(xr[e >> 1]) : bflo(xr[e >> 1]);
        gi[e] = silu_r(rbf(rbf(rbf(xe + mn) * rs) + rbf(rbf(y[e]) * sg)));
      }
      if (l < 16) *(u32x2*)(smem + L_GI + l * 8) = u32x2{pk2(gi[0], gi[1]), pk2(gi[2], gi[3])};
      lds_set(flg + F_GT, 1u, l);
      }
      asm volatile("s_waitcnt vmcnt(0)" :: PKEEP(pl) : "memory");
    }
    if (b == 0 && tid == 256) gs1(0u, ws, Tw);
    GAP(2);
    return;
  }
  const int s = w >> 3;
  const int wt = lw & 1, wc = lw >> 1;
  const char* ob = ws + WS_O + (size_t)((RM == 1 ? xcd * 2 + (jj & 1) : RM == 2 ? (xcd >> 1) * 4 + (jj & 3) : RM == 3 ? 0 : panel) * 4 + lw) * 65536;
  const char* xfb = (const char*)(x + (size_t)(panel * 128 + lw * 32 + s * 16) * DM + hp * 64);
  u32x4 xv[2], gq[2];
  unsigned T;
  auto gate = [&]() {
    const float mn = ((const float*)(smem + L_MN))[lw * 32 + s * 16 + r15], rs = ((const float*)(smem + L_RS))[lw * 32 + s * 16 + r15];
#pragma unroll
    for (int hf = 0; hf < 2; ++hf) {
      unsigned x0 = xv[hf][0], x1 = xv[hf][1], x2 = xv[hf][2], x3 = xv[hf][3];
      sw16u(x0, x2); sw16u(x1, x3);
      xv[hf] = u32x4{x0, x1, x2, x3};
#pragma unroll
      for (int e = 0; e < 4; ++e) {
        const unsigned d = pk2(bflo(xv[hf][e]) + mn, bfhi(xv[hf][e]) + mn);
        const unsigned y = pk2(bflo(d) * rs, bfhi(d) * rs);
        gq[hf][e] = pk2(silu_r(bflo(y)), silu_r(bfhi(y)));
      }
    }
    pin2(gq); pin2(xv);
  };
  const int wrow = 8 * g + tq, wsw = (((wrow >> 1) & 1) << 2) | (((wrow >> 3) & 1) << 1);
  unsigned asw[3][2], wa[3][2];
#pragma unroll
  for (int q = 0; q < 3; ++q) {
#pragma unroll
    for (int ks = 0; ks < 2; ++ks) asw[q][ks] = lbase + (wt * 64 + r15) * 128 + (((4 * ks + g) ^ ((r15 >> 1) & 7)) * 16) + q * SLOT;
#pragma unroll
    for (int c = 0; c < 2; ++c) wa[q][c] = lbase + ABYTES + wrow * 128 + (((wc * 4 + 2 * c + (tp >> 1)) ^ wsw) * 16) + 8 * (tp & 1) + q * SLOT;
  }
  if (s) {
    u32x4 xa[4], wr[4];
    auto st_load = [&]() {
      const char* xb = (const char*)(x + (size_t)row0 * DM + lw * 256);
      const unsigned o = (l & 7) * 2048u + ((l >> 3) & 1) * 64u + g * 16u;
      GL4(xa[0], o, xb, 0, P_DEF); GL4(xa[1], o, xb, 128, P_DEF); GL4(xa[2], o, xb, 256, P_DEF); GL4(xa[3], o, xb, 384, P_DEF);
      const char* rb = (const char*)(w_r + lw * 256);
      const unsigned ro = (l & 1) * 64u + g * 16u;
      GL4(wr[0], ro, rb, 0, P_DEF); GL4(wr[1], ro, rb, 128, P_DEF); GL4(wr[2], ro, rb, 256, P_DEF); GL4(wr[3], ro, rb, 384, P_DEF);
    };
    auto st_sum = [&](auto NV) {
      waitv4<decltype(NV)::value>(xa); waitv4<decltype(NV)::value>(wr);
      f32x4 d0 = z4, d1 = z4, d2 = z4, d3 = z4;
#pragma unroll
      for (int j = 0; j < 4; ++j) {
        d0 = MFMA(ones, as8(xa[j]), d0); d1 = MFMA(as8(wr[j]), as8(xa[j]), d1);
        d2 = MFMA(as8(xa[j]), as8(xa[j]), d2); d3 = MFMA(as8(wr[j]), ones, d3);
      }
      const bool hf = l & 8;
      const float q2 = (l & 2) ? ((l & 1) ? d2[3] : d2[2]) : ((l & 1) ? d2[1] : d2[0]);
      if (g == tq) ldsw1(lbase + L_REC + (lw * 16 + r15) * 16, __builtin_bit_cast(u32x4, f32x4{d0[0], hf ? d1[1] : d1[0], q2, hf ? d3[1] : d3[0]}));
      SB();
    };
    f32x4 acc[4][2];
    bf16x8 w0, w1;
    float sc[4];
    const char* kb = ws + WS_KVR + hp * 8192 + wc * 4096;
    unsigned l16, fsl = LDS_OFF(flg + F_SL + 2 * wc);
    {
#pragma unroll
      for (int t = 0; t < 4; ++t) { acc[t][0] = z4; acc[t][1] = z4; }
      int ts = (hp - 4 * (panel >> 2)) & 15;
      const unsigned xr = lbase + (ts == 15 ? L_EX : (ts % 3) * SLOT) + (lw * 32 + 16 + r15) * 128 + ((g ^ ((r15 >> 1) & 7)) * 16);
      ts = min(ts, 13);
      Frag<2> f[2];
      auto rd = [&](Frag<2>& y, auto I, auto OW, unsigned pa, const unsigned (&pw)[2]) {
        constexpr int i = decltype(I)::value, ow = decltype(OW)::value + 2 * WSLAB;
        if constexpr (i < 4) ds_128<i * 2048>(y.af[i], pa);
        else if constexpr (i & 1) ds_tr<ow + 512>(y.wh[(i - 4) >> 1], pw[(i - 4) >> 1]);
        else ds_tr<ow>(y.wl[(i - 4) >> 1], pw[(i - 4) >> 1]);
      };
      auto step = [&](Frag<2>& c, Frag<2>& y, auto OW, unsigned pa, const unsigned (&pw)[2], auto RD, auto XT) {
        static_for<8>([&](auto I) {
          constexpr int i = decltype(I)::value;
          acc[i & 3][i >> 2] = MFMA(cat8(c.wl[i >> 2], c.wh[i >> 2]), c.af[i & 3], acc[i & 3][i >> 2]);
          SB();
          if constexpr (i < 4 && decltype(RD)::value) { rd(y, IC(2 * i), OW, pa, pw); rd(y, IC(2 * i + 1), OW, pa, pw); SB(); }
          if constexpr (i == 4 && decltype(XT)::value >= 0) { ds_if<decltype(XT)::value>(xv, xr, ts); SB(); }
        });
      };
      auto tile = [&](auto J, auto BAR, auto RD) {
        constexpr int j = decltype(J)::value % 3, n = (j + 1) % 3;
        f[0].wait();
        step(f[0], f[1], IC(4096), asw[j][1], wa[j], IC(1), IC(decltype(BAR)::value ? decltype(J)::value : -1));
        f[1].wait();
        if constexpr (decltype(BAR)::value) { __builtin_amdgcn_s_barrier(); asm volatile("" ::: "memory"); }
        if constexpr (!decltype(RD)::value) { if (l == 0) __hip_atomic_store(flg + F_BL + lw, 1u, __ATOMIC_RELAXED, __HIP_MEMORY_SCOPE_WORKGROUP); SB(); }
        step(f[1], f[0], IC(0), asw[n][0], wa[n], RD, IC(-1));
      };
      auto sents = [&]() {
        unsigned ln;
        asm volatile("v_mbcnt_lo_u32_b32 %0, -1, 0\n\tv_mbcnt_hi_u32_b32 %0, -1, %0" : "=v"(ln));
        u32x4 ff = {~0u, ~0u, ~0u, ~0u};
        asm volatile("" : "+v"(ff));
        const unsigned ko = (ln >> 3) * 2048u + (ln & 7) * 16u + ((panel ^ (ln >> 3)) & 15) * 128u;
        static_for<4>([&](auto J) { gs4o<decltype(J)::value * 1024>(ln * 16u, ws + WS_O + (size_t)(panel * 4 + lw) * 65536 + hp * 4096, ff); });
        gs4(ko, ws + WS_KVP + (hp * 4 + lw) * 32768, ff);
        gs4(ko ^ 17408u, ws + WS_KVP + (hp * 4 + lw) * 32768, ff);
        gs4m(ln * 16u, ws + WS_KVR + hp * 8192 + lw * 2048 + panel * 128, ff);
        SB();
      };
      asm volatile("" : "=v"(xv[0]), "=v"(xv[1]));
      TICK();
      static_for<8>([&](auto I) { rd(f[0], I, IC(0), asw[0][0], wa[0]); });
      static_for<13>([&](auto J) {
        constexpr int t = decltype(J)::value;
        if constexpr (t == 0) st_load();
        if constexpr (t == SBP) { waitv<0>(); SB(); }
        tile(J, IC(1), IC(1));
        if constexpr (t == 1) {
          st_sum(IC(0));
        }
        if constexpr (t == TP3) { if (lw == 0 && !(xcd & 1) && jj < 24) pf3((xcd >> 2) ^ 1); }
        if constexpr (t == TPW) {
          if (lw & 1) touch((l >> 2) * 2048u + (l & 3) * 128u, (const char*)wo + jj * 65536 + (lw >> 1) * 32768 + (xcd & 3) * 512, lbase + L_DU);
        }
        if constexpr (t == 2) MARK(2);
        if constexpr (t == SBI) sents();
        if constexpr (PRE && t == PRE) {
          unsigned ln;
          asm volatile("v_mbcnt_lo_u32_b32 %0, -1, 0\n\tv_mbcnt_hi_u32_b32 %0, -1, %0" : "=v"(ln));
          const unsigned sl = ln & 15;
          const unsigned o = ln < 16 ? WS_KVP + (hp * 4 + lw) * 32768 + sl * 2048 + ((panel ^ sl) & 15) * 128
                           : ln < 32 ? WS_KVR + hp * 8192 + lw * 2048 + panel * 128 : WS_O + (panel * 4 + lw) * 65536 + hp * 4096 + (ln & 31) * 128;
          touch(o, ws, lbase + L_DU);
          SB();
        }
      });
      __builtin_amdgcn_s_barrier(); asm volatile("" ::: "memory");
      VISIT(3);
      relane();
      asw[0][0] += L_EX; asw[0][1] += L_EX;
      ds_if<13>(xv, xr, ts);
      wphi_frag(w0, w1, lbase + L_WP + wc * 2048 + (4 * g + tq) * 64 + tp * 8);
      lds_wait4(flg + F_ST);
      {
        const f32x4 c0 = *(const f32x4*)(smem + L_C + 512 + (wc * 32 + 4 * g) * 4), c1 = *(const f32x4*)(smem + L_C + 512 + (wc * 32 + 16 + 4 * g) * 4);
        f32x2 mr[4];
        rowst(mr, lbase + L_MN + (wt * 64 + r15) * 4);
#pragma unroll
        for (int t = 0; t < 4; ++t) {
          sc[t] = mr[t][1] * L2E;
          acc[t][0] += mr[t][0] * c0; acc[t][1] += mr[t][0] * c1;
          asm volatile("" : "+v"(sc[t]), "+v"(acc[t][0]), "+v"(acc[t][1]));
        }
      }
      asm volatile("" : "+v"(xv[0]), "+v"(xv[1]), "+v"(w0), "+v"(w1));
      if (GB) gate();
      SB();
      asm volatile("" : "+s"(kb));
      l16 = l * 16u;
      asm volatile("" : "+v"(l16), "+v"(fsl));
      GAP(6);
      VISIT(6);
      lds_wait1(flg + (BQ ? F_EX : F_IM) + lw);
      __builtin_amdgcn_s_setprio(BP);
      tile(IC(1), IC(0), IC(1));
      if (QW != 1) lds_wait4(flg + (QW == 2 ? F_GO : F_SD));
      tile(IC(2), IC(0), IC(1)); tile(IC(0), IC(0), IC(0));
    }
    bf16x8 pq[4][2];
    {
      f32x4 p[4][2];
      const float one[2] = {1.f, 1.f};
#pragma unroll
      for (int t = 0; t < 4; ++t) {
        const f32x4 v0 = acc[t][0], v1 = acc[t][1];
        const bf16x8 qp = as8(u32x4{pk2(v0[0], v0[1]), pk2(v0[2], v0[3]), pk2(v1[0], v1[1]), pk2(v1[2], v1[3])});
        p[t][0] = MFMA(w0, qp, z4); p[t][1] = MFMA(w1, qp, z4);
      }
      phi_map<false>(p, sc, one, [&](int t, const unsigned (&pe)[8]) {
        pq[t][0] = as8(u32x4{pe[0], pe[1], pe[2], pe[3]}); pq[t][1] = as8(u32x4{pe[4], pe[5], pe[6], pe[7]});
      });
    }
    asm volatile("" : "+v"(pq[0][0]), "+v"(pq[0][1]), "+v"(pq[1][0]), "+v"(pq[1][1]), "+v"(pq[2][0]), "+v"(pq[2][1]), "+v"(pq[3][0]), "+v"(pq[3][1]));
    if (!GB) gate();
    {
      f32x2 rd[4];
      u32x4 kf[4];
      PAD(6);
#pragma unroll 1
      for (unsigned spins = 0; spins < SPIN; ++spins) {
        u32x2 v; unsigned long long ck;
        asm volatile("ds_read_b64 %0, %2\n\ts_memrealtime %1\n\ts_waitcnt lgkmcnt(0)" : "=v"(v), "=s"(ck) : "v"(fsl) : "memory");
        const unsigned t0 = __builtin_amdgcn_readfirstlane(v[0]), t1 = __builtin_amdgcn_readfirstlane(v[1]), t = __builtin_amdgcn_readfirstlane((unsigned)ck);
        if ((t0 & t1) && min(t - t0, t - t1) >= D2) break;
      }
      GL4(kf[0], l16, kb, 0, P_SC1); GL4(kf[1], l16, kb, 1024, P_SC1); GL4(kf[2], l16, kb, 2048, P_SC1); GL4(kf[3], l16, kb, 3072, P_SC1);
      lds_wait4(flg + F_BL);
      lds_wait2(flg + F_KV + 2 * wc);
      {
        bf16x8 z0 = *(const bf16x8*)(smem + L_Z + wc * 128 + g * 16), z1 = *(const bf16x8*)(smem + L_Z + wc * 128 + 64 + g * 16);
        if (l & 1) { z0 = ones; z1 = ones; }
#pragma unroll
        for (int t = 0; t < 4; ++t) {
          const f32x4 a = MFMA(z0, pq[t][0], z4), c = MFMA(z1, pq[t][1], z4);
          const f32x2 r = {__builtin_amdgcn_rcpf(a[1]), __builtin_amdgcn_rcpf(c[1])};
          rd[t] = r * __builtin_amdgcn_rcpf(__builtin_fmaf(a[0], r[0], __builtin_fmaf(c[0], r[1], 1e-6f)));
          asm volatile("" : "+v"(rd[t]));
        }
      }
      const char* pb = ws + WS_O + (size_t)(panel * 4 + wt * 2) * 65536 + hp * 4096 + wc * 2048;
      unsigned l2 = l16 + 65536u, oa = lbase + L_EX + wc * 8192 + wt * 4096 + l * 16, fo = LDS_OFF(flg + F_OS + lw);
      asm volatile("" : "+s"(pb));
      asm volatile("" : "+v"(l2), "+v"(oa), "+v"(fo));
      waitv4<0>(kf);
      if (__builtin_expect(__ballot(max(max(kf[0][3], kf[1][3]), max(kf[2][3], kf[3][3])) == ~0u) != 0ull, 0))
#pragma unroll 1
        for (unsigned spins = 0; spins < SPIN; ++spins) {
          const unsigned long long m0 = __ballot(kf[0][3] == ~0u), m1 = __ballot(kf[1][3] == ~0u), m2 = __ballot(kf[2][3] == ~0u), m3 = __ballot(kf[3][3] == ~0u);
          if ((m0 | m1 | m2 | m3) == 0ull) break;
          reload4c(kf, l16, kb, m0, m1, m2, m3);
        }
      u32x4 ov[4];
      f32x4 n[4][4];
      auto mm = [&](int t, int i) { n[t][i] = MFMA(as8(kf[i]), pq[t][i >> 1], z4); SB(); };
#pragma unroll
      for (int i = 0; i < 8; ++i) mm(i >> 2, i & 3);
      static_for<4>([&](auto TT) {
        constexpr int t = decltype(TT)::value;
        f32x4 a = n[t][0] * rd[t][0], c = n[t][1] * rd[t][0];
        SB(); if (t < 2) mm(t + 2, 0);
        a += n[t][2] * rd[t][1];
        SB(); if (t < 2) mm(t + 2, 1);
        c += n[t][3] * rd[t][1];
        SB(); if (t < 2) mm(t + 2, 2);
        ov[t] = u32x4{pk2(a[0], a[1]), pk2(a[2], a[3]), pk2(c[0], c[1]), nosent(pk2(c[2], c[3]))};
        SB(); if (t < 2) mm(t + 2, 3);
        asm volatile("global_store_dwordx4 %0, %1, %2 offset:%3 sc1" :: "v"(t < 2 ? l16 : l2), "v"(ov[t]), "s"(pb), "n"((t & 1) * 1024) : "memory");
        SB();
      });
      asm volatile("ds_write_b128 %0, %1\n\tds_write_b128 %0, %2 offset:1024\n\tds_write_b128 %0, %3 offset:2048\n\tds_write_b128 %0, %4 offset:3072\n\tds_write_b32 %5, %5\n\ts_nop 0"
                   :: "v"(oa), "v"(ov[0]), "v"(ov[1]), "v"(ov[2]), "v"(ov[3]), "v"(fo) : "memory");
    }
    __builtin_amdgcn_s_setprio(0);
    GAP(3);
  } else {
    if (tid < 20) ((u32x4*)flg)[tid] = u32x4{0, 0, 0, 0};
    f32x4 acc[4][4];
    Frag<4> f[2];
    u32x4 fs;
    f32x4 c0, c1;
    f32x2 mr[4];
    s16x4 wq[4];
    unsigned mra = lbase + L_MN + (wt * 64 + r15) * 4, cca = lbase + L_C + (wc * 32 + 4 * g) * 4, wpa = lbase + L_WP + wc * 2048 + (4 * g + tq) * 64 + tp * 8, fa = lbase + L_F;
    char* img = smem + lw * 8192;
    unsigned va = lbase + SLOT + ABYTES + lw * 4096 + l * 16, fb = lbase + L_F + lw * 4, ia, wi[4], ko = kvo(), da = lbase + L_DN + wt * 128 + g * 8,
             zo = WS_ZP + ((hp * 4 + lw) * 16 + panel) * 128 + (r15 * 4 + g) * 16, m4a = lbase + L_MN + (wt * 64 + 4 * g) * 4, cva = lbase + L_C + (64 + wc * 32 + r15) * 4;
    {
      const int row = 4 * g + tq, sx = (((row >> 1) & 3) << 2) | (row & 1) | (((row >> 3) & 1) << 1);
      ia = LDS_OFF(img) + row * 128 + (((8 * wt + tp) ^ sx) * 8);
      const int sw = (((r15 >> 1) & 3) << 2) | (r15 & 1) | (((r15 >> 3) & 1) << 1);
#pragma unroll
      for (int i = 0; i < 4; ++i) wi[i] = LDS_OFF(img) + r15 * 128 + (((4 * i + g) ^ sw) * 8);
    }
    {
#pragma unroll
      for (int t = 0; t < 4; ++t)
#pragma unroll
        for (int c = 0; c < 4; ++c) acc[t][c] = z4;
      auto rd = [&](Frag<4>& y, auto I, auto OW, unsigned pa, const unsigned (&pw)[2]) {
        constexpr int i = decltype(I)::value, ow = decltype(OW)::value;
        if constexpr (i < 4) ds_128<i * 2048>(y.af[i], pa);
        else {
          constexpr int c = (i - 4) >> 1;
          if constexpr (i & 1) ds_tr<ow + (c >> 1) * WSLAB + 512>(y.wh[c], pw[c & 1]);
          else ds_tr<ow + (c >> 1) * WSLAB>(y.wl[c], pw[c & 1]);
        }
      };
      auto step = [&](Frag<4>& c, Frag<4>& y, auto OW, unsigned pa, const unsigned (&pw)[2], auto&& more) {
        static_for<16>([&](auto I) {
          constexpr int i = decltype(I)::value, t = i & 3, cb = i >> 2;
          if constexpr (cb < 2) mfma_t(acc[t][cb], cat8(c.wl[cb], c.wh[cb]), __builtin_bit_cast(u32x4, c.af[t]));
          else mfma_t(acc[t][cb], c.af[t], __builtin_bit_cast(u32x4, cat8(c.wl[cb], c.wh[cb])));
          if constexpr (i < 12) rd(y, I, OW, pa, pw); else more(I);
        });
      };
      auto none = [](auto) {};
      __builtin_amdgcn_s_setprio(2);
#pragma unroll
      for (int t = 0; t < 4; ++t) asm volatile("" : "+v"(acc[t][0]), "+v"(acc[t][1]), "+v"(acc[t][2]), "+v"(acc[t][3]));
      asm volatile("" : "+v"(mra), "+v"(cca), "+v"(wpa), "+v"(fa), "+v"(va), "+v"(fb), "+v"(ia), "+v"(wi[0]), "+v"(wi[1]), "+v"(wi[2]), "+v"(wi[3]),
                   "+v"(ko), "+v"(da), "+v"(zo), "+v"(m4a), "+v"(cva));
      TICK();
      static_for<12>([&](auto I) { rd(f[0], I, IC(0), asw[0][0], wa[0]); });
#pragma unroll 1
      for (int it = 0; it < 5; ++it) {
        static_for<3>([&](auto J) {
          constexpr int j = decltype(J)::value, n = (j + 1) % 3;
          f[0].wait();
          step(f[0], f[1], IC(4096), asw[j][1], wa[j], none);
          if constexpr (j == 1) {
            const unsigned dx = it == 4 ? L_EX : 0, dw = it == 4 ? L_EW - ABYTES : 0;
            asw[0][0] += dx; asw[0][1] += dx; wa[0][0] += dw; wa[0][1] += dw;
            asm volatile("" : "+v"(asw[0][0]), "+v"(asw[0][1]), "+v"(wa[0][0]), "+v"(wa[0][1]));
          }
          f[1].wait();
          if (j < 2 || __builtin_expect(it < 4, 1)) __builtin_amdgcn_s_barrier();
          asm volatile("" ::: "memory");
          step(f[1], f[0], IC(0), asw[n][0], wa[n], none);
        });
      }
      f[0].wait();
      step(f[0], f[1], IC(4096), asw[0][1], wa[0], [&](auto I) {
        constexpr int i = decltype(I)::value;
        if constexpr (i == 12) { ds_tr<0>(wq[0], wpa); ds_tr<1024>(wq[1], wpa); ds_tr<32>(wq[2], wpa); }
        if constexpr (i == 13) { ds_tr<1056>(wq[3], wpa); ds_32<F_T * 4>(T, fa); }
      });
      asm volatile("s_waitcnt lgkmcnt(5)" : "+v"(f[1].af[0]), "+v"(f[1].af[1]), "+v"(f[1].af[2]), "+v"(f[1].af[3]), "+v"(f[1].wl[0]), "+v"(f[1].wh[0]), "+v"(f[1].wl[1]), "+v"(f[1].wh[1]),
                   "+v"(f[1].wl[2]), "+v"(f[1].wh[2]), "+v"(f[1].wl[3]), "+v"(f[1].wh[3]));
      static_for<8>([&](auto I) {
        constexpr int i = decltype(I)::value, t = i >> 1, cb = i & 1;
        acc[t][cb] = MFMA(cat8(f[1].wl[cb], f[1].wh[cb]), f[1].af[t], acc[t][cb]);
        SB();
        if constexpr (i == 0) { ds_128v<F_ST * 4>(fs, fa); ds_128v<0>(c0, cca); ds_128v<64>(c1, cca); }
        if constexpr (i == 1) { ds_2x32<0>(mr[0], mra); ds_2x32<16>(mr[1], mra); }
        if constexpr (i == 2) { ds_2x32<32>(mr[2], mra); ds_2x32<48>(mr[3], mra); }
        if constexpr (i == 3) raise<F_LD>(fb);
        SB();
      });
    }
    if (AP != 2) __builtin_amdgcn_s_setprio(AP);
    asm volatile("s_waitcnt lgkmcnt(0)" : "+v"(fs), "+v"(c0), "+v"(c1), "+v"(mr[0]), "+v"(mr[1]), "+v"(mr[2]), "+v"(mr[3]), "+v"(wq[0]), "+v"(wq[1]), "+v"(wq[2]), "+v"(wq[3]), "+v"(T));
    if (__builtin_expect(!__builtin_amdgcn_readfirstlane(fs[0] & fs[1] & fs[2] & fs[3]), FIN)) {
      lds_wait4(flg + F_ST);
      asm volatile("ds_read_b128 %0, %2\n\tds_read_b128 %1, %2 offset:64\n\ts_waitcnt lgkmcnt(0)" : "=&v"(c0), "=&v"(c1) : "v"(cca) : "memory");
      rowst(mr, mra);
    }
    T = __builtin_amdgcn_readfirstlane(T);
    const unsigned t12 = T & 0xfffu;
    const bf16x8 w0 = cat8(wq[0], wq[1]), w1 = cat8(wq[2], wq[3]);
    u32x4 vp[2][4];
    auto selm = [](unsigned long long m, float a, float c) { float r; asm("v_cndmask_b32 %0, %1, %2, %3" : "=v"(r) : "v"(c), "v"(a), "s"(m)); return r; };
    {
      f32x4 p[4][2], m4[4];
      f32x2 cv;
      float sc[4], nrm[2];
#pragma unroll
      for (int t = 0; t < 4; ++t) {
        sc[t] = mr[t][1] * L2E;
        if (t < 2) nrm[t] = selm(0xffff0000ffff0000ull, mr[2 * t + 1][1], mr[2 * t][1]);
        const f32x4 v0 = mr[t][0] * c0 + acc[t][0], v1 = mr[t][0] * c1 + acc[t][1];
        const bf16x8 kc = as8(u32x4{pk2(v0[0], v0[1]), pk2(v0[2], v0[3]), pk2(v1[0], v1[1]), pk2(v1[2], v1[3])});
        p[t][0] = MFMA(w0, kc, z4); p[t][1] = MFMA(w1, kc, z4);
      }
      auto vl = [&](int i0, int i1) {
#pragma unroll
        for (int i = i0; i < i1; ++i)
          acc[i & 3][2 + (i >> 2)] = MFMA(f[1].af[i & 3], cat8(f[1].wl[2 + (i >> 2)], f[1].wh[2 + (i >> 2)]), acc[i & 3][2 + (i >> 2)]);
      };
      SB();
      PAD(3);
      SB();
      phi_map<true>(p, sc, nrm, [&](int t, const unsigned (&pe)[8]) {
        if (t < 2) { SB(); vl(4 * t, 4 * t + 4); SB(); }
        if (t == 2) asm volatile("s_waitcnt lgkmcnt(0)" : "+v"(m4[0]), "+v"(m4[1]), "+v"(m4[2]), "+v"(m4[3]), "+v"(cv) :: "memory");
#pragma unroll
        for (int i = 0; i < 4; ++i) *(u32x2*)(smem + (wi[i] - lbase) + t * 2048) = u32x2{pe[2 * i], pe[2 * i + 1]};
        if (t == 1) {
          asm volatile("ds_read_b128 %0, %5\n\tds_read_b128 %1, %5 offset:64\n\tds_read_b128 %2, %5 offset:128\n\tds_read_b128 %3, %5 offset:192\n\tds_read2_b32 %4, %6 offset1:16"
                       : "=&v"(m4[0]), "=&v"(m4[1]), "=&v"(m4[2]), "=&v"(m4[3]), "=&v"(cv)
                       : "v"(m4a), "v"(cva) : "memory");
          SB();
        }
        if (t == VPT) {
#pragma unroll
          for (int d = 0; d < 2; ++d)
#pragma unroll
            for (int q = 0; q < 2; ++q) {
              const f32x4 a = m4[2 * q] * cv[d] + acc[2 * q][2 + d], c = m4[2 * q + 1] * cv[d] + acc[2 * q + 1][2 + d];
              vp[0][2 * q + d] = u32x4{pk2(a[0], a[1]), pk2(a[2], a[3]), pk2(c[0], c[1]), pk2(c[2], c[3])};
            }
          asm volatile("ds_write_b128 %0, %1\n\tds_write_b128 %0, %2 offset:1024\n\tds_write_b128 %0, %3 offset:2048\n\tds_write_b128 %0, %4 offset:3072"
                       :: "v"(va), "v"(vp[0][0]), "v"(vp[0][1]), "v"(vp[0][2]), "v"(vp[0][3]) : "memory");
        }
      });
    }
    const char* kb = ws + WS_KVP + (hp * 4 + lw) * 32768;
    {
      s16x4 tl[2][2], th[2][2];
#define IMG8 "ds_read_b64_tr_b16 %0, %8\n\tds_read_b64_tr_b16 %1, %8 offset:2048\n\tds_read_b64_tr_b16 %2, %9\n\tds_read_b64_tr_b16 %3, %9 offset:2048\n\t" \
             "ds_read_b64_tr_b16 %4, %8 offset:4096\n\tds_read_b64_tr_b16 %5, %8 offset:6144\n\tds_read_b64_tr_b16 %6, %9 offset:4096\n\tds_read_b64_tr_b16 %7, %9 offset:6144\n\t"
#define IMGO(c) c(tl[0][0]), c(th[0][0]), c(tl[0][1]), c(th[0][1]), c(tl[1][0]), c(th[1][0]), c(tl[1][1]), c(th[1][1])
      raise<F_EX>(fb);
      asm volatile(IMG8 : IMGO("=&v") : "v"(ia), "v"(ia ^ 32u) : "memory");
      {
        u32x4 d0, d1, d2, d3;
        asm volatile("ds_read2_b64 %0, %4 offset1:4\n\tds_read2_b64 %1, %4 offset0:8 offset1:12\n\tds_read2_b64 %2, %5 offset1:4\n\tds_read2_b64 %3, %5 offset0:8 offset1:12"
                     : "=&v"(d0), "=&v"(d1), "=&v"(d2), "=&v"(d3) : "v"(da), "v"(da ^ 128u) : "memory");
        unsigned o1 = ko ^ 17408u, ib = ia ^ 8192u, ic = ia ^ 8224u, fx, fe = (fb ^ 4u) + F_EX * 4;
        va ^= 4096u;
        asm volatile("s_waitcnt lgkmcnt(4)" : IMGO("+v"), "+v"(o1), "+v"(ib), "+v"(ic), "+v"(va), "+v"(fe), "+s"(kb) :: "memory");
        f32x4 kva[2][2], za[2];
        bf16x8 a[2][2];
#pragma unroll
        for (int q = 0; q < 2; ++q)
#pragma unroll
          for (int fb = 0; fb < 2; ++fb) {
            a[q][fb] = cat8(tl[q][fb], th[q][fb]);
#pragma unroll
            for (int d = 0; d < 2; ++d) kva[fb][d] = MFMA(a[q][fb], as8(vp[0][2 * q + d]), q ? kva[fb][d] : z4);
          }
        SB();
        s16x4 ul[2][2], uh[2][2];
#define IMGP(c) c(ul[0][0]), c(uh[0][0]), c(ul[0][1]), c(uh[0][1]), c(ul[1][0]), c(uh[1][0]), c(ul[1][1]), c(uh[1][1])
#define OTH "ds_read_b32 %10, %15\n\t" IMG8 "ds_read_b128 %11, %16\n\tds_read_b128 %12, %16 offset:1024\n\tds_read_b128 %13, %16 offset:2048\n\tds_read_b128 %14, %16 offset:3072"
        asm volatile("s_waitcnt lgkmcnt(0)" : "+v"(d0), "+v"(d1), "+v"(d2), "+v"(d3) :: "memory");
        asm volatile(OTH : IMGP("=&v"), "+v"(ib), "+v"(ic), "=&v"(fx), "=&v"(vp[1][0]), "=&v"(vp[1][1]), "=&v"(vp[1][2]), "=&v"(vp[1][3]) : "v"(fe), "v"(va) : "memory");
        SB();
#pragma unroll
        for (int fb = 0; fb < 2; ++fb) za[fb] = MFMA(a[1][fb], as8(d1), MFMA(a[0][fb], as8(d0), z4));
        SB();
        asm volatile("s_waitcnt lgkmcnt(0)" : IMGP("+v"), "+v"(fx), "+v"(vp[1][0]), "+v"(vp[1][1]), "+v"(vp[1][2]), "+v"(vp[1][3]) :: "memory");
        if (__builtin_expect(__builtin_amdgcn_readfirstlane(fx) == 0u, 0))
#pragma unroll 1
          for (unsigned spins = 0; spins < SPIN && __builtin_amdgcn_readfirstlane(fx) == 0u; ++spins)
            asm volatile(OTH "\n\ts_waitcnt lgkmcnt(0)" : IMGP("+v"), "+v"(ib), "+v"(ic), "+v"(fx), "+v"(vp[1][0]), "+v"(vp[1][1]), "+v"(vp[1][2]), "+v"(vp[1][3]) : "v"(fe), "v"(va) : "memory");
#pragma unroll
        for (int d = 0; d < 2; ++d)
#pragma unroll
          for (int q = 0; q < 2; ++q)
#pragma unroll
            for (int fb = 0; fb < 2; ++fb) kva[fb][d] = MFMA(cat8(ul[q][fb], uh[q][fb]), as8(vp[1][2 * q + d]), kva[fb][d]);
        SB();
#pragma unroll
        for (int d = 0; d < 2; ++d) {
          za[d] = MFMA(cat8(ul[1][d], uh[1][d]), as8(d3), MFMA(cat8(ul[0][d], uh[0][d]), as8(d2), za[d]));
          const f32x4 u = kva[0][d], v = kva[1][d];
          gs4(d ? o1 : ko, kb, u32x4{pk2(u[0], u[1]), pk2(u[2], u[3]), pk2(v[0], v[1]), nosent(pk2(v[2], v[3]))});
        }
        raise<F_IM>(fb);
        relane();
        const f32x4 z = (l & 1) ? za[1] : za[0];
        if (r15 < 2) gs4(zo, ws, u32x4{tag12(z[0], t12), tag12(z[1], t12), tag12(z[2], t12), tag12(z[3], t12)});
      }
    }
    GL4(xv[0], xo, xfb, 0, P_DEF); GL4(xv[1], xo, xfb, 64, P_DEF);
    const char* zb = ws + WS_ZP + (hp * 4 + lw) * 2048;
    u32x4 zp[2], pl[4];
    {
      u32x4 p[2];
      const unsigned one = (l & 1) ? 0x3f800000u : 0x3f80u;
      u32x4 s0 = {(l & 2) ? 0u : one, (l & 2) ? one : 0u, 0u, 0u}, s1 = {0u, 0u, s0[0], s0[1]};
      asm volatile("" : "+v"(s0), "+v"(s1));
      GAP(7);
      VISIT(7);
      if (PD1) __builtin_amdgcn_s_sleep(PD1);
      poll2<PP1>(p, pl, l * 16u + panel * 2048, kb);
      asm volatile("" : "+v"(xv[0]), "+v"(xv[1]));
      GL4(zp[0], l * 16u, zb, 0, P_SC1); GL4(zp[1], l * 16u, zb, 1024, P_SC1);
      f32x4 u = MFMA(as8(s0), as8(p[1]), MFMA(as8(s0), as8(p[0]), z4)), v = MFMA(as8(s1), as8(p[1]), MFMA(as8(s1), as8(p[0]), z4));
#pragma unroll
      for (int e = 0; e < 4; ++e) { u[e] += ror8(u[e]); v[e] += ror8(v[e]); }
      gs4m(l * 16u, ws + WS_KVR + hp * 8192 + lw * 2048 + panel * 128, u32x4{pk2(u[0], u[1]), pk2(u[2], u[3]), pk2(v[0], v[1]), nosent(pk2(v[2], v[3]))});
      asm volatile("ds_write_b32 %0, %1 offset:%2" :: "v"(fb), "v"(now() | 1u), "n"(F_SL * 4) : "memory");
    }
    {
      asm volatile("s_waitcnt vmcnt(1)" : "+v"(zp[0]), "+v"(zp[1]) :: "memory");
      asm volatile("" :: "v"(pl[0]), "v"(pl[1]), "v"(pl[2]), "v"(pl[3]));
#pragma unroll 1
      for (unsigned spins = 0; spins < SPIN; ++spins) {
        const unsigned long long m0 = __ballot(bad4(zp[0], t12) & 0xfffu), m1 = __ballot(bad4(zp[1], t12) & 0xfffu);
        if ((m0 | m1) == 0ull) break;
        reload2(zp, l * 16u, zb, m0, m1);
      }
      float z[4];
#pragma unroll
      for (int e = 0; e < 4; ++e) { const float q = sum_g(untag12(zp[0][e]) + untag12(zp[1][e])); z[e] = q + ror8(q); }
      if (l < 8) *(u32x2*)(smem + L_Z + lw * 64 + (l & 3) * 16 + (l >> 2) * 8) = u32x2{pk2(z[0], z[1]), pk2(z[2], z[3])};
      lds_set(flg + F_KV + lw, 1u, l);
    }
    __builtin_amdgcn_s_setprio(0);
    gate();
  }
  {
    relane();
    u32x4 of[NS][2], o0[2];
    f32x4 acc[2][4];
#pragma unroll
    for (int t = 0; t < 2; ++t)
#pragma unroll
      for (int c = 0; c < 4; ++c) acc[t][c] = z4;
    tie8<1>(acc);
    unsigned wb[4], wx[4];
    {
      const int row = 4 * g + tq, f = ((row >> 1) & 1) | ((((row >> 2) ^ (row >> 3)) & 1) << 2);
#pragma unroll
      for (int nb = 0; nb < 4; ++nb) wb[nb] = lbase + s * 4096 + row * 128 + ((((tp & 2) + (nb & 1) + 4 * (nb >> 1)) ^ f) * 16) + (tp & 1) * 8;
    }
    const unsigned v0 = l * 16u + s * 3072u, v1 = l * 16u + s * 1024u + 1024u, v3 = l * 16u + 2048u - s * 1024u;
    auto oadr = [&](int h) { return ob + ptile(h) * 4096; };
    s16x4 wl[5], wh[5], el[2][4], eh[2][4];
    auto wrd = [&](auto K, auto HI) {
      constexpr int k = decltype(K)::value, h = k >> 2, nb = k & 3, o = (h & 7) * 8192;
      if constexpr (decltype(HI)::value) ds_tr<o + 2048>(wh[k % 5], wb[nb]);
      else {
        if constexpr (h == 8) { wb[nb] += 65536u; asm volatile("" : "+v"(wb[nb])); }
        ds_tr<o>(wl[k % 5], wb[nb]);
      }
    };
    unsigned xa = lbase + lw * 8192 + s * 4096 + l * 16, fa = LDS_OFF(flg + F_X + s * 4 + lw), fg = LDS_OFF(flg + F_PG + s * 4);
    f32x2 xf[8], gf[8];
    char* op = (char*)(out + (size_t)(panel * 128 + lw * 32 + s * 16) * DM + hp * 64);
    unsigned oo = (l & 7) * 2048u + (l & 8) * 8u + g * 16u, oa = lbase + L_EX + s * 9216 + lw * 2048 + l * 16;
    char* op2 = op + 16384;
    int idx;
    auto pre = [&]() {
#pragma unroll
      for (int i = 0; i < 8; ++i) {
        xf[i] = f32x2{bflo(xv[i >> 2][i & 3]), bfhi(xv[i >> 2][i & 3])}; gf[i] = f32x2{bflo(gq[i >> 2][i & 3]), bfhi(gq[i >> 2][i & 3])};
        asm volatile("" : "+v"(xf[i]), "+v"(gf[i]));
      }
      asm volatile("" : "+v"(oo), "+s"(op), "+s"(op2), "+v"(xa), "+v"(fa), "+v"(fg), "+v"(oa), "+v"(wb[0]), "+v"(wb[1]), "+v"(wb[2]), "+v"(wb[3]));
    };
    if (PW) pre();
    if (s && !BGO) lds_wait1(flg + F_GO + lw);
    else {
      lds_wait4(flg + F_OS);
      if (PD3) __builtin_amdgcn_s_sleep(PD3);
      if (!BGO && l == 0) __hip_atomic_store(flg + F_GO + lw, 1u, __ATOMIC_RELAXED, __HIP_MEMORY_SCOPE_WORKGROUP);
    }
    static_for<NS>([&](auto H) { opair(of[decltype(H)::value], v0, v1, oadr(decltype(H)::value + 1)); });
    lds_wait4(flg + F_WO);
    idx = 2047 - (int)(lds_get(flg + F_SEL) & 2047u);
    asm volatile("ds_read_b128 %0, %2\n\tds_read_b128 %1, %3" : "=&v"(o0[0]), "=&v"(o0[1]) : "v"(oa), "v"(oa ^ 1024u) : "memory");
    static_for<4>([&](auto NB) { wrd(NB, IC(0)); wrd(NB, IC(1)); });
    if (!PW) pre();
    u32x4 pg;
    static_for<14>([&](auto H) {
      constexpr int h = decltype(H)::value, x0 = h == XH || h == XH + 1, x1 = h == XH + 1 || h == XH + 2;
      u32x4 (&o)[2] = h ? of[(h + NS - 1) % NS] : o0;
      if constexpr (h == 1 && BP3) { if (s) __builtin_amdgcn_s_setprio(1); }
      static_for<4>([&](auto NB) {
        constexpr int nb = decltype(NB)::value, k = 4 * h + nb;
        constexpr bool ld = nb == 3 && h && h + NS < 16;
        const bf16x8 wf = cat8(wl[k % 5], wh[k % 5]);
        mfma_w<x1 + (3 - nb) * (2 + 2 * x1) + nb * (2 + 2 * x0) + (h == 5 || h == 13)>(acc[1][nb], wf, o[1]);
        wrd(IC(k + 4), IC(0));
        if constexpr (x0) ds_tr<(h - XH + 6) * 8192>(el[h - XH][nb], wx[nb]);
        if constexpr (ld) GL4(o[1], h + NS < 14 ? v1 : v3, oadr(h + NS), 0, P_SC1);
        mfma_t(acc[0][nb], wf, o[0]);
        wrd(IC(k + 4), IC(1));
        if constexpr (x0) ds_tr<(h - XH + 6) * 8192 + 2048>(eh[h - XH][nb], wx[nb]);
        if constexpr (ld) GL4(o[0], v0, oadr(h + NS), 0, P_SC1);
      });
      if constexpr (h == 4) asm volatile("ds_write_b32 %0, %0" :: "v"(fg + lw * 4u) : "memory");
      if constexpr (h == LM7) PAD(7);
      if constexpr (h == XH - 1) {
#pragma unroll
        for (int nb = 0; nb < 4; ++nb) { wx[nb] = wb[nb] ^ 4096u; asm volatile("" : "+v"(wx[nb])); }
      }
      if constexpr (h == 12) asm volatile("ds_read_b128 %0, %1" : "=v"(pg) : "v"(fg) : "memory");
      if constexpr (h < 13) ocheck<2 * ((h + NS < 15 ? h + NS : 15) - h - 1), true>(of[h % NS], v0, v1, oadr(h + 1));
    });
    asm volatile("s_waitcnt lgkmcnt(%1)" : "+v"(pg) : "n"(XH == 12 ? 15 : 8));
    if (__builtin_expect(!__builtin_amdgcn_readfirstlane(pg[0] & pg[1] & pg[2] & pg[3]), 0)) lds_wait4(flg + F_PG + s * 4);
    asm volatile("s_nop 1\n\tds_write_b128 %0, %1\n\tds_write_b128 %0, %2 offset:1024\n\tds_write_b128 %0, %3 offset:2048\n\tds_write_b128 %0, %4 offset:3072\n\tds_write_b32 %5, %5"
                 :: "v"(xa), "v"(acc[1][0]), "v"(acc[1][1]), "v"(acc[1][2]), "v"(acc[1][3]), "v"(fa) : "memory");
    __builtin_amdgcn_s_setprio(0);
    ocheck<2, true>(of[13 % NS], v0, v3, oadr(14));
    static_for<4>([&](auto NB) {
      constexpr int nb = decltype(NB)::value, k = 56 + nb;
      mfma_w<nb ? 15 : 5>(acc[0][nb], cat8(wl[k % 5], wh[k % 5]), of[13 % NS][0]);
      wrd(IC(k + 4), IC(0)); wrd(IC(k + 4), IC(1));
    });
#pragma unroll
    for (int nb = 0; nb < 4; ++nb) mfma_t(acc[0][nb], cat8(el[0][nb], eh[0][nb]), of[13 % NS][1]);
    unsigned fx;
    fa ^= 16u; xa ^= 4096u;
    asm volatile("ds_read_b32 %0, %5\n\tds_read_b128 %1, %6\n\tds_read_b128 %2, %6 offset:1024\n\tds_read_b128 %3, %6 offset:2048\n\tds_read_b128 %4, %6 offset:3072"
                 : "=&v"(fx), "=&v"(acc[1][0]), "=&v"(acc[1][1]), "=&v"(acc[1][2]), "=&v"(acc[1][3]) : "v"(fa), "v"(xa) : "memory");
    ocheck<0, true>(of[14 % NS], v0, v3, oadr(15));
#pragma unroll
    for (int nb = 0; nb < 4; ++nb) mfma_t(acc[0][nb], cat8(el[1][nb], eh[1][nb]), of[14 % NS][1]);
    static_for<4>([&](auto NB) { constexpr int nb = decltype(NB)::value, k = 60 + nb; mfma_w<5>(acc[0][nb], cat8(wl[k % 5], wh[k % 5]), of[14 % NS][0]); });
    if (__builtin_expect((idx >> 4) == panel * 8 + lw * 2 + s, GIN)) {
      lds_wait1(flg + F_GT);
      relane();
      if (r15 == (idx & 15)) {
        const u32x2* gi = (const u32x2*)(smem + L_GI + (g >> 1) * 32 + (g & 1) * 8);
#pragma unroll
        for (int i = 0; i < 4; ++i) {
          const u32x2 a = gi[(i & 1) * 2 + (i >> 1) * 8];
          gf[2 * i] = f32x2{bflo(a[0]), bfhi(a[0])}; gf[2 * i + 1] = f32x2{bflo(a[1]), bfhi(a[1])};
        }
      }
    }
    asm volatile("s_waitcnt lgkmcnt(0)" : "+v"(fx), "+v"(acc[1][0]), "+v"(acc[1][1]), "+v"(acc[1][2]), "+v"(acc[1][3]) :: "memory");
    if (__builtin_expect(__builtin_amdgcn_readfirstlane(fx) == 0u, 0))
#pragma unroll 1
    for (unsigned spins = 0; spins < SPIN && __builtin_amdgcn_readfirstlane(fx) == 0u; ++spins)
      asm volatile("s_sleep 1\n\tds_read_b32 %0, %5\n\tds_read_b128 %1, %6\n\tds_read_b128 %2, %6 offset:1024\n\tds_read_b128 %3, %6 offset:2048\n\t"
                   "ds_read_b128 %4, %6 offset:3072\n\ts_waitcnt lgkmcnt(0)"
                   : "+v"(fx), "+v"(acc[1][0]), "+v"(acc[1][1]), "+v"(acc[1][2]), "+v"(acc[1][3]) : "v"(fa), "v"(xa) : "memory");
#pragma unroll
    for (int i = 0; i < 8; ++i) xf[i] = __builtin_elementwise_fma(f32x2{acc[1][i >> 1][2 * (i & 1)], acc[1][i >> 1][2 * (i & 1) + 1]}, gf[i], xf[i]);
    asm volatile("" : "+v"(xf[0]), "+v"(xf[1]), "+v"(xf[2]), "+v"(xf[3]), "+v"(xf[4]), "+v"(xf[5]), "+v"(xf[6]), "+v"(xf[7]),
                 "+v"(acc[0][0]), "+v"(acc[0][1]), "+v"(acc[0][2]), "+v"(acc[0][3]));
    unsigned rr[8];
#pragma unroll
    for (int i = 0; i < 8; ++i) {
      const f32x2 r = __builtin_elementwise_fma(f32x2{acc[0][i >> 1][2 * (i & 1)], acc[0][i >> 1][2 * (i & 1) + 1]}, gf[i], xf[i]);
      rr[i] = pk2(r[0], r[1]);
    }
    {
      sw16u(rr[0], rr[2]); sw16u(rr[1], rr[3]); sw16u(rr[4], rr[6]); sw16u(rr[5], rr[7]);
      unsigned a0, a1, a2, a3, c0, c1, c2, c3;
#define CD(d, x, y) "v_cndmask_b32_dpp " d ", " x ", " y ", vcc row_ror:8 row_mask:0xf bank_mask:0xf\n\t"
      asm volatile("s_mov_b32 vcc_lo, 0x00ff00ff\n\ts_mov_b32 vcc_hi, 0x00ff00ff\n\t" CD("%0", "%12", "%8") CD("%2", "%14", "%10") CD("%1", "%13", "%9") CD("%3", "%15", "%11")
                   "s_not_b64 vcc, vcc\n\t" CD("%4", "%8", "%12") CD("%5", "%9", "%13") CD("%6", "%10", "%14") CD("%7", "%11", "%15")
                   : "=&v"(a0), "=&v"(a1), "=&v"(a2), "=&v"(a3), "=&v"(c0), "=&v"(c1), "=&v"(c2), "=&v"(c3)
                   : "v"(rr[0]), "v"(rr[1]), "v"(rr[2]), "v"(rr[3]), "v"(rr[4]), "v"(rr[5]), "v"(rr[6]), "v"(rr[7]) : "vcc", "scc");
      const u32x4 a = {a0, a1, a2, a3}, c = {c0, c1, c2, c3};
      asm volatile("global_store_dwordx4 %0, %1, %3 sc1\n.Lkf_last:\n\tglobal_store_dwordx4 %0, %2, %4 sc1" :: "v"(oo), "v"(a), "v"(c), "s"(op), "s"(op2) : "memory");
    }
    if (s) {
    } else {
    }
  }
}

extern "C" void kernel_launch(void* const* d_in, const int* in_sizes, int n_in,
                              void* d_out, int out_size, void* d_ws, size_t ws_size,
                              hipStream_t stream) {
  (void)hipFuncSetAttribute((const void*)kf_main, hipFuncAttributeMaxDynamicSharedMemorySize, LDSM);
  kf_main<<<256, 768, LDSM, stream>>>((const u16*)d_in[0], (const u16*)d_in[1], (char*)d_ws, (const u16*)d_in[5], (const u16*)d_in[2],
                                      (const u16*)d_in[3], (const u16*)d_in[6], (const u16*)d_in[7], (const u16*)d_in[8], (u16*)d_out);
}
```

```cpp
#include <hip/hip_runtime.h>
#include <stdint.h>
#include <utility>

typedef __attribute__((ext_vector_type(8))) short bf16x8;
typedef __attribute__((ext_vector_type(4))) short s16x4;
typedef __attribute__((ext_vector_type(4))) float f32x4;
typedef __attribute__((ext_vector_type(2))) float f32x2;
typedef __attribute__((ext_vector_type(4))) unsigned u32x4;
typedef __attribute__((ext_vector_type(2))) unsigned u32x2;
typedef __attribute__((ext_vector_type(2))) __bf16 bfx2;
typedef unsigned short u16;

#define DI __device__ __forceinline__
#define MFMA(a, b, c) __builtin_amdgcn_mfma_f32_16x16x32_bf16((a), (b), (c), 0, 0, 0)
#define SB() __builtin_amdgcn_sched_barrier(0)
#define IC(x) std::integral_constant<int, (x)>{}

DI unsigned pk2(float a, float b) { f32x2 v = {a, b}; return __builtin_bit_cast(unsigned, __builtin_convertvector(v, bfx2)); }
DI float bflo(unsigned u) { return __uint_as_float(u << 16); }
DI float bfhi(unsigned u) { return __uint_as_float(u & 0xffff0000u); }
DI float rbf(float x) { return (float)(__bf16)x; }
DI f32x2 rb2(f32x2 v) { return f32x2{__uint_as_float(pk2(0.f, v[0])), __uint_as_float(pk2(0.f, v[1]))}; }
DI bf16x8 cat8(s16x4 lo, s16x4 hi) { return __builtin_shufflevector(lo, hi, 0, 1, 2, 3, 4, 5, 6, 7); }
DI bf16x8 as8(u32x4 v) { return __builtin_bit_cast(bf16x8, v); }
template <int... I, class F> DI void static_for_impl(std::integer_sequence<int, I...>, F&& f) { (f(std::integral_constant<int, I>{}), ...); }
template <int N, class F> DI void static_for(F&& f) { static_for_impl(std::make_integer_sequence<int, N>{}, f); }
template <int N> DI void waitv() { asm volatile("s_waitcnt vmcnt(%0)" ::"n"(N) : "memory"); }
#define RAW_BARRIER() do { asm volatile("s_waitcnt lgkmcnt(0)" ::: "memory"); __builtin_amdgcn_s_barrier(); asm volatile("" ::: "memory"); } while (0)
#define TICK() do { SB(); RAW_BARRIER(); SB(); } while (0)
#define LAY 1
#define GAP(n) asm volatile("s_branch .Lkg" #n "\n.Lkh" #n ":\n\t.fill ((.Lkg" #n " - .Lkt" #n " - (.Lkh" #n " - kf_main)) & 4095) / 4, 4, 0xbf800000\n.Lkg" #n ":" ::: "memory")
#define MARK(n) asm volatile(".Lkt" #n ":" ::: "memory")
#define PAD(n) do { unsigned d_; asm volatile(".Lkt" #n ":\n\ts_mov_b32 %0, 0xbe801d6a" : "=s"(d_)); } while (0)
#define VISIT(n) do { const char* p_; int o_; asm volatile("s_getpc_b64 %0\n.Lkv" #n ":\n\ts_mov_b32 %1, .Lkt" #n " - .Lkv" #n " + 4" : "=s"(p_), "=s"(o_)); \
                      asm volatile("s_swappc_b64 vcc, %0" :: "s"(p_ + o_) : "vcc"); } while (0)
#define DS_B128(dst, addr, off) asm volatile("ds_read_b128 %0, %1 offset:%2" : "=v"(dst) : "v"(addr), "n"(off))
#define DS_TR(dst, addr, off) asm volatile("ds_read_b64_tr_b16 %0, %1 offset:%2" : "=v"(dst) : "v"(addr), "n"(off))
#define LDS_OFF(p) ((unsigned)(uintptr_t)(__attribute__((address_space(3))) const char*)(p))
enum { P_DEF = 0, P_SC1 = 1, P_NT = 2 };
template <int OFF, int POL> DI void gl4(u32x4& d, unsigned voff, const char* sb) {
  if constexpr (POL == P_SC1) asm volatile("global_load_dwordx4 %0, %1, %2 offset:%3 sc1" : "=v"(d) : "v"(voff), "s"(sb), "n"(OFF));
  else if constexpr (POL == P_NT) asm volatile("global_load_dwordx4 %0, %1, %2 offset:%3 nt" : "=v"(d) : "v"(voff), "s"(sb), "n"(OFF));
  else asm volatile("global_load_dwordx4 %0, %1, %2 offset:%3" : "=v"(d) : "v"(voff), "s"(sb), "n"(OFF));
}
#define GL4(dst, voff, sb, off, pol) gl4<off, pol>(dst, voff, sb)
#define GL2(dst, voff, sb, off) asm volatile("global_load_dwordx2 %0, %1, %2 offset:%3" : "=v"(dst) : "v"(voff), "s"(sb), "n"(off))
#define GL1(dst, voff, sb, off) asm volatile("global_load_dword %0, %1, %2 offset:%3 sc1" : "=v"(dst) : "v"(voff), "s"(sb), "n"(off))
DI void bl4nt(u32x4& d, unsigned voff, u32x4 rs, unsigned soff) {
  asm volatile("buffer_load_dwordx4 %0, %1, %2, %3 offen nt" : "=v"(d) : "v"(voff), "s"(rs), "s"(soff));
}
DI void mfma_t(f32x4& c, bf16x8 a, u32x4 bb) { asm volatile("v_mfma_f32_16x16x32_bf16 %0, %1, %2, %0" : "+v"(c) : "v"(a), "v"(bb)); }
template <int N> DI void mfma_w(f32x4& c, bf16x8 a, u32x4 bb) {
  asm volatile("s_waitcnt lgkmcnt(%3)\n\tv_mfma_f32_16x16x32_bf16 %0, %1, %2, %0" : "+v"(c) : "v"(a), "v"(bb), "n"(N));
}
template <int N> DI void tie8(f32x4 (&a)[2][4]) {
  asm volatile("s_nop %8" : "+v"(a[0][0]), "+v"(a[0][1]), "+v"(a[0][2]), "+v"(a[0][3]), "+v"(a[1][0]), "+v"(a[1][1]), "+v"(a[1][2]), "+v"(a[1][3]) : "n"(N));
}
template <int OFF> DI void ds_tr(s16x4& d, unsigned a) { DS_TR(d, a, OFF); }
template <int N> DI void waitv4(u32x4 (&v)[4]) { asm volatile("s_waitcnt vmcnt(%4)" : "+v"(v[0]), "+v"(v[1]), "+v"(v[2]), "+v"(v[3]) : "n"(N)); }
template <int N> DI void waitl8(s16x4 (&a)[4], s16x4 (&c)[4]) {
  asm volatile("s_waitcnt lgkmcnt(%8)" : "+v"(a[0]), "+v"(c[0]), "+v"(a[1]), "+v"(c[1]), "+v"(a[2]), "+v"(c[2]), "+v"(a[3]), "+v"(c[3]) : "n"(N));
}
DI void gs4(unsigned voff, const char* sb, u32x4 v) { asm volatile("global_store_dwordx4 %0, %1, %2 sc1\n\ts_nop 1" :: "v"(voff), "v"(v), "s"(sb) : "memory"); }
DI void gs1(unsigned voff, const char* sb, unsigned v) { asm volatile("global_store_dword %0, %1, %2 sc1" :: "v"(voff), "v"(v), "s"(sb) : "memory"); }
DI void glds(unsigned voff, const char* sbase, unsigned lds) {
  asm volatile("s_mov_b32 m0, %2\n\ts_nop 0\n\tglobal_load_lds_dwordx4 %0, %1" :: "v"(voff), "s"(sbase), "s"(lds) : "memory");
}
DI void touch(unsigned voff, const char* sbase, unsigned lds) {
  asm volatile("s_mov_b32 m0, %2\n\ts_nop 0\n\tglobal_load_lds_dword %0, %1" :: "v"(voff), "s"(sbase), "s"(lds) : "memory");
}
DI void touchn(unsigned voff, const char* sbase, unsigned lds) {
  asm volatile("s_mov_b32 m0, %2\n\ts_nop 0\n\tglobal_load_lds_dword %0, %1 nt" :: "v"(voff), "s"(sbase), "s"(lds) : "memory");
}
DI void touch4(unsigned voff, const char* sbase, unsigned lds) {
  asm volatile("s_mov_b64 exec, 15\n\ts_mov_b32 m0, %2\n\ts_nop 0\n\tglobal_load_lds_dword %0, %1\n\ts_mov_b64 exec, -1" :: "v"(voff), "s"(sbase), "s"(lds) : "memory");
}
DI void pub_pin(u32x4& a, u32x2& b, unsigned& c, unsigned& d) { asm volatile("" : "+v"(a), "+v"(b), "+v"(c), "+v"(d)); }
DI void pub(u32x4 v, u32x2 w, unsigned o, unsigned oc, unsigned long long m, unsigned long long mc, const char* sb) {
  asm volatile("s_mov_b64 exec, %4\n\tglobal_store_dwordx2 %2, %1, %6 sc1\n\ts_mov_b64 exec, %5\n\tglobal_store_dwordx4 %3, %0, %6 sc1\n\ts_mov_b64 exec, -1\n\ts_nop 0"
               :: "v"(v), "v"(w), "v"(o), "v"(oc), "s"(m), "s"(mc), "s"(sb) : "memory");
}
DI float silu_f(float x) { return x / (1.0f + __expf(-x)); }
DI float silu_r(float x) { return x * __builtin_amdgcn_rcpf(1.0f + __expf(-x)); }
DI float dot2(unsigned a, unsigned b, float c) { return __builtin_amdgcn_fdot2_f32_bf16(__builtin_bit_cast(bfx2, a), __builtin_bit_cast(bfx2, b), c, false); }

template <int OFF> DI void gs4o(unsigned voff, const char* sb, u32x4 v) {
  asm volatile("global_store_dwordx4 %0, %1, %2 offset:%3 sc1\n\ts_nop 1" :: "v"(voff), "v"(v), "s"(sb), "n"(OFF) : "memory");
}
DI void gs4m(unsigned voff, const char* sb, u32x4 v) {
  asm volatile("s_mov_b64 exec, 0xff\n\tglobal_store_dwordx4 %0, %1, %2 sc1\n\ts_mov_b64 exec, -1\n\ts_nop 0" :: "v"(voff), "v"(v), "s"(sb) : "memory");
}
DI void gs2(unsigned voff, const char* sb, u32x2 v) { asm volatile("global_store_dwordx2 %0, %1, %2 sc1" :: "v"(voff), "v"(v), "s"(sb) : "memory"); }
#define GL1P(dst, voff, sb) asm volatile("global_load_dword %0, %1, %2" : "=v"(dst) : "v"(voff), "s"(sb))
DI u32x4 ldsr1(unsigned a) { u32x4 v; asm volatile("ds_read_b128 %0, %1\n\ts_waitcnt lgkmcnt(0)" : "=v"(v) : "v"(a) : "memory"); return v; }
DI void ldsw1(unsigned a, u32x4 v) { asm volatile("ds_write_b128 %0, %1" :: "v"(a), "v"(v) : "memory"); }
DI unsigned nosent(unsigned v) { return min(v, 0xfffffffeu); }

constexpr int DM = 1024, FF = 3584;
constexpr unsigned WS_ST = 4096;
constexpr unsigned WS_CA = WS_ST + 2048 * 8;
constexpr unsigned WS_CS = WS_CA + 256 * 16;
constexpr unsigned WS_KVP = WS_CS + 256 * 1024;
constexpr unsigned WS_ZP = WS_KVP + 2048 * 1024;
constexpr unsigned WS_KVR = WS_ZP + 1024 * 128;
constexpr unsigned WS_O = WS_KVR + 16 * 8192;
constexpr unsigned WS_GU = WS_O + 4096 * 1024;
constexpr unsigned WS_Y = WS_GU + 56 * 4096;

constexpr int ABYTES = 16384, WSLAB = 8192, SLOT = 40960;
constexpr int L_EW = 3 * SLOT;
constexpr int L_EX = L_EW + 2 * WSLAB;
constexpr int L_REC = L_EW;
constexpr int L_Z = L_EX + ABYTES;
constexpr int L_WP = L_Z + 256;
constexpr int L_MN = L_WP + 4096;
constexpr int L_RS = L_MN + 512;
constexpr int L_DN = L_RS + 512;
constexpr int L_C = L_DN + 256;
constexpr int L_H = L_C + 768;
constexpr int L_GI = L_H + 1024;
constexpr int L_F = L_GI + 128;
constexpr int L_DU = L_F + 320;
constexpr int LDSM = L_DU + 256;
constexpr int F_KV = 0, F_WO = 4, F_PG = 8, F_X = 16, F_T = 24, F_GT = 25, F_SEL = 28, F_BL = 32, F_IM = 36, F_EX = 40, F_ST = 44, F_SD = 48, F_CA = 52, F_OS = 60, F_GO = 64, F_AL = 68, F_SL = 72, F_LD = 76;
#define SPINV (1u << 20)
constexpr unsigned SPIN = SPINV;
DI void pin1(float& v) { asm volatile("" : "+v"(v)); }
DI void pinf(f32x4& v) { asm volatile("" : "+v"(v)); }
DI void pin2(u32x4 (&v)[2]) { asm volatile("" : "+v"(v[0]), "+v"(v[1])); }
constexpr float L2E = 1.4426950408889634f;

template <int CTRL, int RMASK> DI float dpp_add(float v) {
  return v + __builtin_bit_cast(float, __builtin_amdgcn_update_dpp(0, __builtin_bit_cast(int, v), CTRL, RMASK, 0xf, true));
}
DI float wave_total(float v) {
  v = dpp_add<0x111, 0xf>(v); v = dpp_add<0x112, 0xf>(v); v = dpp_add<0x114, 0xf>(v); v = dpp_add<0x118, 0xf>(v);
  v = dpp_add<0x142, 0xa>(v); v = dpp_add<0x143, 0xc>(v);
  return __builtin_bit_cast(float, __builtin_amdgcn_readlane(__builtin_bit_cast(int, v), 63));
}
DI f32x2 sw32(float a, float b) {
  auto r = __builtin_amdgcn_permlane32_swap(__float_as_uint(a), __float_as_uint(b), false, false);
  return f32x2{__uint_as_float(r[0]), __uint_as_float(r[1])};
}
DI f32x2 sw16(float a, float b) {
  auto r = __builtin_amdgcn_permlane16_swap(__float_as_uint(a), __float_as_uint(b), false, false);
  return f32x2{__uint_as_float(r[0]), __uint_as_float(r[1])};
}
DI float ror8(float v) { return __builtin_bit_cast(float, __builtin_amdgcn_update_dpp(0, __builtin_bit_cast(int, v), 0x128, 0xf, 0xf, true)); }
DI float sum_g(float v) {
  f32x2 a = sw32(v, v); v = a[0] + a[1];
  a = sw16(v, v); return a[0] + a[1];
}
DI unsigned tag12(float v, unsigned t) { return ((__float_as_uint(v) + 0x800u) & 0xfffff000u) | t; }
DI float untag12(unsigned u) { return __uint_as_float(u & 0xfffff000u); }
DI unsigned bad4(u32x4 v, unsigned t) { return (v[0] ^ t) | (v[1] ^ t) | (v[2] ^ t) | (v[3] ^ t); }
DI bool all0(unsigned v) { return __ballot(v != 0u) == 0ull; }
DI void lds_set(unsigned* f, unsigned v, int l) {
  asm volatile("s_waitcnt lgkmcnt(0)" ::: "memory");
  if (l == 0) __hip_atomic_store(f, v, __ATOMIC_RELAXED, __HIP_MEMORY_SCOPE_WORKGROUP);
}
DI unsigned lds_get(unsigned* f) { return __builtin_amdgcn_readfirstlane(__hip_atomic_load(f, __ATOMIC_RELAXED, __HIP_MEMORY_SCOPE_WORKGROUP)); }
DI void lds_wait4(unsigned* f) {
#pragma unroll 1
  for (unsigned spins = 0; spins < SPIN; ++spins) {
    u32x4 v; asm volatile("ds_read_b128 %0, %1\n\ts_waitcnt lgkmcnt(0)" : "=v"(v) : "v"(LDS_OFF(f)) : "memory");
    if (__builtin_amdgcn_readfirstlane(v[0] & v[1] & v[2] & v[3])) break;
    __builtin_amdgcn_s_sleep(1);
  }
}

template <int NC> struct Frag {
  bf16x8 af[4]; s16x4 wl[NC], wh[NC];
  DI void wait() {
    if constexpr (NC == 4)
      asm volatile("s_waitcnt lgkmcnt(0)" : "+v"(af[0]), "+v"(af[1]), "+v"(af[2]), "+v"(af[3]), "+v"(wl[0]), "+v"(wh[0]), "+v"(wl[1]), "+v"(wh[1]),
                   "+v"(wl[2]), "+v"(wh[2]), "+v"(wl[3]), "+v"(wh[3]));
    else
      asm volatile("s_waitcnt lgkmcnt(0)" : "+v"(af[0]), "+v"(af[1]), "+v"(af[2]), "+v"(af[3]), "+v"(wl[0]), "+v"(wh[0]), "+v"(wl[1]), "+v"(wh[1]));
  }
};
template <int OFF> DI void ds_32(unsigned& d, unsigned a) { asm volatile("ds_read_b32 %0, %1 offset:%2" : "=v"(d) : "v"(a), "n"(OFF) : "memory"); }
template <int OFF, class V> DI void ds_128v(V& d, unsigned a) { DS_B128(d, a, OFF); }
template <int O> DI void ds_2x32(f32x2& d, unsigned a) { asm volatile("ds_read2_b32 %0, %1 offset0:%2 offset1:%3" : "=v"(d) : "v"(a), "n"(O), "n"(O + 128) : "memory"); }
template <int F> DI void raise(unsigned a) { asm volatile("ds_write_b32 %0, %0 offset:%1" :: "v"(a), "n"(F * 4) : "memory"); }
template <int OFF> DI void ds_w64(unsigned a, u32x2 v) { asm volatile("ds_write_b64 %0, %1 offset:%2" :: "v"(a), "v"(v), "n"(OFF) : "memory"); }
template <int T> DI void ds_if(u32x4 (&v)[2], unsigned a, int t) {
  asm volatile("s_cmp_eq_u32 %4, %5\n\ts_cselect_b64 exec, -1, 0\n\tds_read_b128 %0, %2\n\tds_read_b128 %1, %3\n\ts_mov_b64 exec, -1"
               : "+v"(v[0]), "+v"(v[1]) : "v"(a), "v"(a ^ 64u), "s"(t), "n"(T) : "scc", "memory");
}
template <int OFF> DI void ds_128(bf16x8& d, unsigned a) { DS_B128(d, a, OFF); }
DI void lds_wait2(unsigned* f) {
#pragma unroll 1
  for (unsigned spins = 0; spins < SPIN; ++spins) {
    u32x2 v; asm volatile("ds_read_b64 %0, %1\n\ts_waitcnt lgkmcnt(0)" : "=v"(v) : "v"(LDS_OFF(f)) : "memory");
    if (__builtin_amdgcn_readfirstlane(v[0] & v[1])) break;
    __builtin_amdgcn_s_sleep(1);
  }
}
DI void reload2(u32x4 (&p)[2], unsigned o, const char* sb, unsigned long long m0, unsigned long long m1) {
  asm volatile("s_mov_b64 exec, %4\n\tglobal_load_dwordx4 %0, %2, %3 sc1\n\ts_mov_b64 exec, %5\n\tglobal_load_dwordx4 %1, %2, %3 offset:1024 sc1\n\t"
               "s_mov_b64 exec, -1\n\ts_waitcnt vmcnt(0)"
               : "+v"(p[0]), "+v"(p[1]) : "v"(o), "s"(sb), "s"(m0), "s"(m1) : "memory");
}

template <int CTRL> DI unsigned dpp_umax(unsigned v) { return max(v, (unsigned)__builtin_amdgcn_update_dpp((int)v, (int)v, CTRL, 0xf, 0xf, false)); }
template <int CTRL, int RMASK> DI unsigned dpp_umax_r(unsigned v) { return max(v, (unsigned)__builtin_amdgcn_update_dpp((int)v, (int)v, CTRL, RMASK, 0xf, false)); }
DI unsigned now() { return (unsigned)__builtin_amdgcn_s_memrealtime(); }
DI void lds_wait1(unsigned* f) {
#pragma unroll 1
  for (unsigned spins = 0; spins < SPIN && lds_get(f) == 0u; ++spins) __builtin_amdgcn_s_sleep(1);
  asm volatile("" ::: "memory");
}

DI void cs_read(u32x2 (&c)[4], unsigned a) {
  asm volatile("ds_read_b64_tr_b16 %0, %4\n\tds_read_b64_tr_b16 %1, %4 offset:512\n\tds_read_b64_tr_b16 %2, %4 offset:4096\n\tds_read_b64_tr_b16 %3, %4 offset:4608"
               : "=&v"(c[0]), "=&v"(c[1]), "=&v"(c[2]), "=&v"(c[3]) : "v"(a) : "memory");
}
DI void cs_sum(u32x2 (&c)[4], float& s0, float& s1) {
  asm volatile("s_waitcnt lgkmcnt(0)" : "+v"(c[0]), "+v"(c[1]), "+v"(c[2]), "+v"(c[3]) :: "memory");
#pragma unroll
  for (int i = 0; i < 4; ++i) { s0 = dot2(c[i][0], 0x3f803f80u, s0); s1 = dot2(c[i][1], 0x3f803f80u, s1); }
  asm volatile("" : "+v"(s0), "+v"(s1));
}
#define WIN 0
#define NG 4
#define NH 12
#define PE 7
#define PP1 12
#define PD1 12
#define PD3 6
#define PPY 12
#define FP0 1
#define FPR 2
#define FPE 0
#define FPG 3
#define GAS 10
#define SB0 2
#define SBI 3
#define SBP 6
#define CTS 6
#define ES0 7
#define ESL 5
#define NF 4
#define VPT 2
#define WX 0
#define WE 0
#define AP 2
#define BP 1
#define BQ 0
#define WNO 16
#define D2 40
#define GB 1
#define PYD 16
#define PRE 0
#define GL 0
#define CT2 0
#define GIN 0
#define TP3 1
#define TPW 1
#define LM7 2
#define RM 0
#define XH 11
#define PW 1
#define BGO 1
#define BP3 1
#define FIN 0
#define FI 0
#define DL 0
#define CAE 0
#define TD 0
#define TDP 0
#define QW 2
#define ORD 0
#define OC3 1
#define CP 1
#define WSO 0
#define SQ 1
#define SR 1
#define SK 1
#define HB 1
#define NS 3
constexpr int PUB = SBP > SB0 + SK ? SBP : SB0 + SK;
constexpr int n_e(int S) { int n = 0; for (int i = 0; i < 8; ++i) n += ES0 + i * ESL / 8 == S; return n; }
constexpr int n_x(int S) { return (S == PUB) + (S == 0) + (S == GAS ? 2 : 0) + (S == CTS) + n_e(S) + (S == 11 ? FI + CAE : 0); }
static_assert((NS == 3 || !OC3) && SBP > SBI && PUB >= 3 && PUB < GAS && GAS <= 10 && (CTS <= 10 || CTS == 12) && ES0 > SB0 && ES0 + ESL <= 12 && FI + NF + NG + NH <= 24 && (!DL || FI + NF + NG + NH <= 16), "");
DI void wphi_frag(bf16x8& w0, bf16x8& w1, unsigned a) {
  s16x4 t0, t1, t2, t3;
  asm volatile("ds_read_b64_tr_b16 %0, %4\n\tds_read_b64_tr_b16 %1, %4 offset:1024\n\tds_read_b64_tr_b16 %2, %4 offset:32\n\t"
               "ds_read_b64_tr_b16 %3, %4 offset:1056\n\ts_waitcnt lgkmcnt(0)" : "=&v"(t0), "=&v"(t1), "=&v"(t2), "=&v"(t3) : "v"(a) : "memory");
  w0 = cat8(t0, t1); w1 = cat8(t2, t3);
}
DI float mx2(float a, float b) { return __builtin_elementwise_maximum(a, b); }
DI float mx3(float a, float b, float c) { return __builtin_elementwise_maximum(__builtin_elementwise_maximum(a, b), c); }
DI void bcast2(float m, f32x2& c0, f32x2& c1) {
  const f32x2 a = sw16(m, m);
  c0 = sw32(a[0], a[0]); c1 = sw32(a[1], a[1]);
}
template <bool NRM, class F> DI void phi_map(const f32x4 (&p)[4][2], const float (&sc)[4], const float (&nrm)[2], F&& sink) {
  float r[4];
  f32x2 c[4];
#pragma unroll
  for (int t = 0; t < 4; ++t) {
    const float mx = mx2(mx3(mx3(p[t][0][0], p[t][0][1], p[t][0][2]), p[t][0][3], p[t][1][0]), mx3(p[t][1][1], p[t][1][2], p[t][1][3]));
    const float nm = mx2(mx3(mx3(-p[t][0][0], -p[t][0][1], -p[t][0][2]), -p[t][0][3], -p[t][1][0]), mx3(-p[t][1][1], -p[t][1][2], -p[t][1][3]));
    const f32x2 a = sw32(mx, nm);
    r[t] = mx2(a[0], a[1]);
  }
#pragma unroll
  for (int q = 0; q < 2; ++q) {
    const f32x2 a = sw16(r[2 * q], r[2 * q + 1]);
    bcast2(mx2(a[0], a[1]), c[2 * q], c[2 * q + 1]);
  }
#pragma unroll
  for (int t = 0; t < 4; ++t) c[t] *= -sc[t];
#pragma unroll
  for (int q = 0; q < 2; ++q) {
    f32x2 e[2][8], sm[2];
#pragma unroll
    for (int i = 0; i < 8; ++i)
#pragma unroll
      for (int j = 0; j < 2; ++j) {
        const float v = p[2 * q + j][i >> 2][i & 3];
        const f32x2 a = __builtin_elementwise_fma(f32x2{v, v}, f32x2{sc[2 * q + j], -sc[2 * q + j]}, c[2 * q + j]);
        e[j][i] = f32x2{__builtin_amdgcn_exp2f(a[0]), __builtin_amdgcn_exp2f(a[1])};
      }
    if constexpr (NRM) {
#pragma unroll
    for (int j = 0; j < 2; ++j) {
      sm[j] = ((e[j][0] + e[j][1]) + (e[j][2] + e[j][3])) + ((e[j][4] + e[j][5]) + (e[j][6] + e[j][7]));
      sm[j] = sw32(sm[j][0], sm[j][1]);
    }
    const f32x2 a = sw16(sm[0][0] + sm[0][1], sm[1][0] + sm[1][1]);
    float rc = __builtin_amdgcn_rcpf(a[0] + a[1]);
    rc *= nrm[q];
    bcast2(rc, sm[0], sm[1]);
    }
#pragma unroll
    for (int j = 0; j < 2; ++j) {
      unsigned pe[8];
#pragma unroll
      for (int i = 0; i < 8; ++i) if (NRM) e[j][i] *= sm[j];
#pragma unroll
      for (int i = 0; i < 4; ++i) { pe[i] = pk2(e[j][2 * i][0], e[j][2 * i + 1][0]); pe[4 + i] = pk2(e[j][2 * i][1], e[j][2 * i + 1][1]); }
      sink(2 * q + j, pe);
    }
  }
}

struct Sel { unsigned e[4], o[4]; };
DI Sel make_sel(int l) {
  Sel s;
#pragma unroll
  for (int q = 0; q < 4; ++q) {
    const bool on = ((l & 7) >> 1) == q;
    s.e[q] = on ? ((l & 1) ? 0x01000c0cu : 0x0c0c0100u) : 0x0c0c0c0cu;
    s.o[q] = on ? ((l & 1) ? 0x03020c0cu : 0x0c0c0302u) : 0x0c0c0c0cu;
  }
  return s;
}
DI f32x4 gemv2(unsigned xp, u32x4 w0, u32x4 w1, const Sel& s, f32x4 d) {
  const u32x4 a0 = {__builtin_amdgcn_perm(xp, xp, s.e[0]), __builtin_amdgcn_perm(xp, xp, s.e[1]), __builtin_amdgcn_perm(xp, xp, s.e[2]), __builtin_amdgcn_perm(xp, xp, s.e[3])};
  const u32x4 a1 = {__builtin_amdgcn_perm(xp, xp, s.o[0]), __builtin_amdgcn_perm(xp, xp, s.o[1]), __builtin_amdgcn_perm(xp, xp, s.o[2]), __builtin_amdgcn_perm(xp, xp, s.o[3])};
  return MFMA(as8(a1), as8(w1), MFMA(as8(a0), as8(w0), d));
}
DI u32x4 gemv_fin(f32x4 d, unsigned t12) {
  u32x4 v;
#pragma unroll
  for (int e = 0; e < 4; ++e) { const float t = ror8(d[e]); v[e] = tag12(d[e] + sw32(t, t)[1], t12); }
  return v;
}

DI void opair(u32x4 (&o)[2], unsigned v0, unsigned v1, const char* p) { GL4(o[0], v0, p, 0, P_SC1); GL4(o[1], v1, p, 0, P_SC1); }
template <int N, bool COLD = false> DI void ocheck(u32x4 (&o)[2], unsigned v0, unsigned v1, const char* p) {
  asm volatile("s_waitcnt vmcnt(%2)" : "+v"(o[0]), "+v"(o[1]) : "n"(N));
  if (COLD && __builtin_expect(__ballot(max(o[0][3], o[1][3]) == ~0u) == 0ull, 1)) return;
#pragma unroll 1
  for (unsigned spins = 0; spins < SPIN && __ballot(max(o[0][3], o[1][3]) == ~0u) != 0ull; ++spins)
    asm volatile("s_sleep 2\n\tglobal_load_dwordx4 %0, %2, %4 sc1\n\tglobal_load_dwordx4 %1, %3, %4 sc1\n\ts_waitcnt vmcnt(0)"
                 : "+v"(o[0]), "+v"(o[1]) : "v"(v0), "v"(v1), "s"(p) : "memory");
}
template <int N> DI void ocheck3(u32x4 (&a)[2], u32x4 (&b)[2], u32x4 (&c)[2], unsigned v0, unsigned v1, const char* pa, const char* pb, const char* pc) {
  asm volatile("s_waitcnt vmcnt(%2)" : "+v"(a[0]), "+v"(a[1]) : "n"(N));
  if (__builtin_expect(__ballot(max(a[0][3], a[1][3]) == ~0u) == 0ull, 1)) return;
  asm volatile("s_waitcnt vmcnt(0)" : "+v"(b[0]), "+v"(b[1]), "+v"(c[0]), "+v"(c[1]));
#pragma unroll 1
  for (unsigned spins = 0; spins < SPIN; ++spins) {
    const unsigned long long ma = __ballot(max(a[0][3], a[1][3]) == ~0u), mb = __ballot(max(b[0][3], b[1][3]) == ~0u), mc = __ballot(max(c[0][3], c[1][3]) == ~0u);
    if (ma == 0ull) break;
    asm volatile("s_mov_b64 exec, %11\n\tglobal_load_dwordx4 %0, %6, %8 sc1\n\tglobal_load_dwordx4 %1, %7, %8 sc1\n\t"
                 "s_mov_b64 exec, %12\n\tglobal_load_dwordx4 %2, %6, %9 sc1\n\tglobal_load_dwordx4 %3, %7, %9 sc1\n\t"
                 "s_mov_b64 exec, %13\n\tglobal_load_dwordx4 %4, %6, %10 sc1\n\tglobal_load_dwordx4 %5, %7, %10 sc1\n\ts_mov_b64 exec, -1\n\ts_waitcnt vmcnt(0)"
                 : "+v"(a[0]), "+v"(a[1]), "+v"(b[0]), "+v"(b[1]), "+v"(c[0]), "+v"(c[1])
                 : "v"(v0), "v"(v1), "s"(pa), "s"(pb), "s"(pc), "s"(ma), "s"(mb), "s"(mc) : "memory");
  }
}
DI void sw16u(unsigned& a, unsigned& b) { auto r = __builtin_amdgcn_permlane16_swap(a, b, false, false); a = r[0]; b = r[1]; }
template <int N> DI void waitl2(s16x4& a, s16x4& c) { asm volatile("s_waitcnt lgkmcnt(%2)" : "+v"(a), "+v"(c) : "n"(N)); }
template <int N> DI void tie4(f32x4 (&a)[4]) { asm volatile("s_nop %4" : "+v"(a[0]), "+v"(a[1]), "+v"(a[2]), "+v"(a[3]) : "n"(N)); }
DI void rowst(f32x2 (&m)[4], unsigned a) {
  asm volatile("ds_read2_b32 %0, %4 offset1:128\n\tds_read2_b32 %1, %4 offset0:16 offset1:144\n\tds_read2_b32 %2, %4 offset0:32 offset1:160\n\t"
               "ds_read2_b32 %3, %4 offset0:48 offset1:176\n\ts_waitcnt lgkmcnt(0)" : "=&v"(m[0]), "=&v"(m[1]), "=&v"(m[2]), "=&v"(m[3]) : "v"(a) : "memory");
}
DI void reload4c(u32x4 (&p)[4], unsigned o, const char* sb, unsigned long long m0, unsigned long long m1, unsigned long long m2, unsigned long long m3) {
  asm volatile("s_mov_b64 exec, %6\n\tglobal_load_dwordx4 %0, %4, %5 sc1\n\ts_mov_b64 exec, %7\n\tglobal_load_dwordx4 %1, %4, %5 offset:1024 sc1\n\t"
               "s_mov_b64 exec, %8\n\tglobal_load_dwordx4 %2, %4, %5 offset:2048 sc1\n\ts_mov_b64 exec, %9\n\tglobal_load_dwordx4 %3, %4, %5 offset:3072 sc1\n\t"
               "s_mov_b64 exec, -1\n\ts_waitcnt vmcnt(0)"
               : "+v"(p[0]), "+v"(p[1]), "+v"(p[2]), "+v"(p[3]) : "v"(o), "s"(sb), "s"(m0), "s"(m1), "s"(m2), "s"(m3) : "memory");
}

#define PL(r, o) "global_load_dwordx4 v[" #r ":" #r "+3], %[vo], %[sb] offset:" #o " sc1\n\t"
#define PC(r, m) "v_cmp_eq_u32 " m ", -1, v[" #r "+3]\n\t"
#define PM(d, r) "v_mov_b32 v[" #d "], v[" #r "]\n\tv_mov_b32 v[" #d "+1], v[" #r "+1]\n\tv_mov_b32 v[" #d "+2], v[" #r "+2]\n\tv_mov_b32 v[" #d "+3], v[" #r "+3]\n\t"
template <int D> DI void poll2(u32x4 (&p)[2], u32x4 (&q)[4], unsigned vo, const char* sb) {
  unsigned long long m; unsigned n = SPIN;
  asm volatile(PL(128, 0) PL(132, 1024) "s_sleep %[d]\n\t" PL(136, 0) PL(140, 1024)
               "1:\n\ts_waitcnt vmcnt(2)\n\t" PC(128, "vcc") PC(132, "%[m]") "s_or_b64 exec, vcc, %[m]\n\ts_cbranch_scc0 2f\n\t" PL(128, 0) PL(132, 1024)
               "s_mov_b64 exec, -1\n\ts_waitcnt vmcnt(2)\n\t" PC(136, "vcc") PC(140, "%[m]") "s_or_b64 exec, vcc, %[m]\n\ts_cbranch_scc0 3f\n\t" PL(136, 0) PL(140, 1024)
               "s_mov_b64 exec, -1\n\ts_sub_u32 %[n], %[n], 1\n\ts_cbranch_scc0 1b\n\ts_waitcnt vmcnt(0)\n"
               "3:\n\ts_mov_b64 exec, -1\n\t" PM(120, 136) PM(124, 140) "s_branch 4f\n"
               "2:\n\ts_mov_b64 exec, -1\n\t" PM(120, 128) PM(124, 132) "4:\n\ts_nop 1"
               : "={v[120:123]}"(p[0]), "={v[124:127]}"(p[1]), "=&{v[128:131]}"(q[0]), "=&{v[132:135]}"(q[1]), "=&{v[136:139]}"(q[2]), "=&{v[140:143]}"(q[3]),
                 [m] "=&s"(m), [n] "+s"(n)
               : [vo] "v"(vo), [sb] "s"(sb), [d] "n"(D) : "vcc", "scc", "memory");
}
#define PL4(r) PL(r, 0) PL(r+4, 1024) PL(r+8, 2048) PL(r+12, 3072)
#define PM2(o, r) "v_mov_b64 v[128+" #o ":129+" #o "], v[" #r "+" #o ":" #r "+1+" #o "]\n\t"
#define PM16(r) PM2(0, r) PM2(2, r) PM2(8, r) PM2(10, r) PM2(4, r) PM2(6, r) PM2(12, r) PM2(14, r)
#define PQ8(c) c "{v[96:99]}"(q[0]), c "{v[100:103]}"(q[1]), c "{v[104:107]}"(q[2]), c "{v[108:111]}"(q[3]), c "{v[112:115]}"(q[4]), c "{v[116:119]}"(q[5]), c "{v[120:123]}"(q[6]), c "{v[124:127]}"(q[7])
#define PB(a, r) "v_bitop3_b32 " a ", " a ", v[" #r "], %[t] bitop3:0xf6\n\t"
#define PB7(a, r) "v_xor_b32 " a ", %[t], v[" #r "]\n\t" PB(a, r+1) PB(a, r+2) PB(a, r+3) PB(a, r+4) PB(a, r+5) PB(a, r+6) PB(a, r+7)
#define PCT(r) "s_waitcnt vmcnt(4)\n\t" PB7("v128", r) PB7("v129", r+8) "v_and_b32 v128, 0xfff, v128\n\tv_and_or_b32 v128, v129, %[tm], v128\n\tv_cmp_ne_u32 vcc, 0, v128\n\ts_or_b64 exec, vcc, vcc\n\t"
template <int D> DI void pollt(u32x4 (&p)[4], u32x4 (&q)[8], unsigned vo, const char* sb, unsigned t, unsigned tm) {
  unsigned n = SPIN;
  asm volatile(PL4(96) "s_sleep %[d]\n\t" PL4(112)
               "1:\n\t" PCT(96) "s_cbranch_scc0 2f\n\t" PL4(96) "s_mov_b64 exec, -1\n\t" PCT(112) "s_cbranch_scc0 3f\n\t" PL4(112)
               "s_mov_b64 exec, -1\n\ts_sub_u32 %[n], %[n], 1\n\ts_cbranch_scc0 1b\n\ts_waitcnt vmcnt(0)\n"
               "3:\n\ts_mov_b64 exec, -1\n\t" PM16(112) "s_branch 4f\n"
               "2:\n\ts_mov_b64 exec, -1\n\t" PM16(96) "4:\n\ts_nop 1"
               : "=&{v[128:131]}"(p[0]), "=&{v[132:135]}"(p[1]), "=&{v[136:139]}"(p[2]), "=&{v[140:143]}"(p[3]), PQ8("=&"), [n] "+s"(n)
               : [vo] "v"(vo), [sb] "s"(sb), [t] "s"(t), [tm] "s"(tm), [d] "n"(D) : "vcc", "scc", "memory");
}
#define PKEEP(pl) "v"(pl[0]), "v"(pl[1]), "v"(pl[2]), "v"(pl[3]), "v"(pl[4]), "v"(pl[5]), "v"(pl[6]), "v"(pl[7])
extern "C" __global__ __launch_bounds__(768) __attribute__((aligned(4096))) void kf_main(const u16* __restrict__ x, const u16* __restrict__ wqkv, char* ws, const u16* __restrict__ w_r,
                                               const u16* __restrict__ wphi, const u16* __restrict__ wo, const u16* __restrict__ w_gate,
                                               const u16* __restrict__ w_up, const u16* __restrict__ w_down, u16* __restrict__ out) {
  extern __shared__ __attribute__((aligned(16))) char smem[];
  const int b = blockIdx.x;
  const int xcd = b & 7, jj = b >> 3;
  const int panel = (xcd >> 2) * 8 + (jj & 7), hp = (xcd & 3) * 4 + (jj >> 3);
  const int tid = threadIdx.x;
  const int w = __builtin_amdgcn_readfirstlane(tid >> 6), lw = w & 3;
  int l = tid & 63, r15 = l & 15, g = l >> 4, tq = r15 >> 2, tp = l & 3;
  unsigned xo;
  auto relane = [&]() {
    asm volatile("v_mbcnt_lo_u32_b32 %0, -1, 0\n\tv_mbcnt_hi_u32_b32 %0, -1, %0" : "=v"(l));
    r15 = l & 15; g = l >> 4; tq = r15 >> 2; tp = l & 3; xo = r15 * 2048u + g * 16u;
  };
  const unsigned lbase = LDS_OFF(smem);
  auto pf3 = [&](int half) {
    touch(l * 6144u, (const char*)wqkv + (size_t)(half * 512 + (jj & 7) * 64) * 6144 + (jj >> 3) * 2048 + (xcd & 3) * 512 + 384, lbase + L_DU);
  };
  const char* pc0; unsigned clen;
  asm volatile("s_getpc_b64 %0\n.Lkf_here:\n\ts_mov_b32 %1, ((.Lkf_last-kf_main)|4095)-3-(.Lkf_here-kf_main)\n\ts_max_u32 %1, %1, ((.Lkt8-kf_main)|4095)-3-(.Lkf_here-kf_main)"
               : "=s"(pc0), "=s"(clen) :: "scc");
  if (__builtin_expect(w < 4, 1)) {
    if (__builtin_expect(w >= 2, 1)) {
      touch(min(((jj * 2 + w) & 15) * 4096u + l * 64u, clen), pc0, lbase + L_DU);
    }
    if (__builtin_expect(w == 0 && !(xcd & 1) && jj < 24, 1)) pf3(xcd >> 2);
  }
  unsigned* flg = (unsigned*)(smem + L_F);
  const bf16x8 ones = {0x3f80, 0x3f80, 0x3f80, 0x3f80, 0x3f80, 0x3f80, 0x3f80, 0x3f80};
  const f32x4 z4 = {0.f, 0.f, 0.f, 0.f};
  const int row0 = panel * 128 + hp * 8;
  auto ptile = [&](int p) { return p == 0 ? hp : !ORD ? (p <= 12 ? ((hp | 3) + p) & 15 : hp ^ (p - 12)) : p < 4 ? (hp & 12) + (int)((0x24343839u >> ((hp & 3) * 8 + (p - 1) * 2)) & 3u) : ((hp & 12) + p) & 15; };
  auto kvo = [&]() { return (l >> 3) * 2048u + (l & 7) * 16u + ((panel ^ (l >> 3)) & 15) * 128u; };
  if (__builtin_expect((w >> 2) == 1, 1)) {
    const int srow = l >> 3, sch = l & 7, cg = sch, rg = srow;
    const int kt0 = 4 * (panel >> 2);
    unsigned G, Tw, t12;
    unsigned voA[2], voW[2];
    const char* sA = (const char*)(x + (size_t)panel * 128 * DM);
    const char* sQ = (const char*)(wqkv + hp * 64);
#pragma unroll
    for (int i = 0; i < 2; ++i) {
      const int row = lw * 32 + i * 8 + srow, wr = lw * 16 + i * 8 + srow;
      voA[i] = (unsigned)(row * 2048 + ((sch ^ ((row >> 1) & 7)) * 16));
      voW[i] = (unsigned)(wr * 6144 + ((sch ^ ((((wr >> 1) & 1) << 2) | (((wr >> 3) & 1) << 1))) * 16));
    }
    auto stage = [&](int S) {
      const int kt = (S + kt0) & 15;
      const char* a = sA + kt * 128; const char* wv = sQ + (size_t)kt * 64 * 6144;
      const unsigned d = lbase + (S % 3) * SLOT;
#pragma unroll
      for (int i = 0; i < 4; ++i) glds(voA[i & 1], a + (i >> 1) * 32768, d + lw * 4096 + i * 1024);
#pragma unroll
      for (int i = 0; i < 6; ++i) glds(voW[i & 1], wv + ((i >> 1) == 2 ? 0 : ((i >> 1) + 1) * 2048), d + ABYTES + (i >> 1) * WSLAB + lw * 2048 + (i & 1) * 1024);
    };
    auto last = [&](int i) {
      const int kt = (15 + kt0) & 15;
      const char* a = sA + kt * 128; const char* wv = sQ + (size_t)kt * 64 * 6144;
      if (i < 4) glds(voA[i & 1], a + (i >> 1) * 32768, lbase + L_EX + lw * 4096 + i * 1024);
      else if (i < 8) glds(voW[i & 1], wv + ((i >> 1) - 1) * 2048, lbase + L_EW + ((i >> 1) - 2) * WSLAB + lw * 2048 + (i & 1) * 1024);
      else glds(voW[i & 1], wv, lbase + ABYTES + 2 * WSLAB + lw * 2048 + (i & 1) * 1024);
    };
    __builtin_amdgcn_s_setprio(3);
    stage(0); stage(1);
    GL1(G, 0u, (const char*)ws, 0);
    const bool gu = (hp & 7) != 3;
    const int bb = gu ? (hp - (hp > 3) - (hp > 11)) * 16 + panel : 0;
    const int ke = bb & 7, cb = ((bb >> 3) * 2 + (lw & 1)) ^ ((lw >> 1) * 4);
    const int kc = (bb >> 4) * 4 + ((bb >> 1) & 3), cq = ((bb >> 3) & 1) | ((bb & 1) << 1);
    u32x4 gv[16], dv[8];
    const uintptr_t gb = (uintptr_t)(((lw >> 1) ? w_up : w_gate) + (size_t)(ke * 128) * FF + cb * 64);
    const u32x4 grs = {(unsigned)gb, (unsigned)(gb >> 32) & 0xffffu, gu ? 127u * FF * 2 + 128u : 0u, 0x00020000u};
    const unsigned go = rg * (16u * FF * 2) + cg * 16u;
    const uintptr_t db = (uintptr_t)(w_down + (size_t)(kc * 64) * DM + cq * 256 + lw * 64);
    const u32x4 drs = {(unsigned)db, (unsigned)(db >> 32) & 0xffffu, gu ? 63u * DM * 2 + 128u : 0u, 0x00020000u};
    const unsigned dof = rg * 16384u + cg * 16u;
    auto strm = [&](auto I0, auto I1, auto W) {
      static_for<(decltype(I1)::value > decltype(I0)::value ? decltype(I1)::value - decltype(I0)::value : 0)>([&](auto I) {
        constexpr int i = decltype(I)::value + decltype(I0)::value;
        if constexpr (decltype(W)::value) waitv<decltype(W)::value>();
        if constexpr (i < 16) bl4nt(gv[i], go, grs, i * (FF * 2u)); else bl4nt(dv[i - 16], dof, drs, (i - 16) * 2048u);
      });
    };
    const int wrow = 8 * g + tq, wsw = (((wrow >> 1) & 1) << 2) | (((wrow >> 3) & 1) << 1);
    const unsigned csa = lbase + ABYTES + (lw < 3 ? lw : 0) * WSLAB + wrow * 128 + (((2 * (panel & 3) + (tp >> 1)) ^ wsw) * 16) + 8 * (tp & 1);
    waitv<11>();
    RAW_BARRIER();
    float mean = 0.f, var = 1.f, c0 = 0.f, c1 = 0.f;
    f32x4 sm = z4;
    u32x4 pv; u32x2 pw; unsigned po, pc; unsigned long long pm;
    u32x4 ga[2], ca;
    const char* gab = ws + (lw < 3 ? WS_CS + (hp * 3 + lw) * 2048 : WS_ST + panel * 1024 - 1024);
    const unsigned go0 = l * 16u + (lw < 3 ? 0u : 1024u);
    static_for<13>([&](auto I) {
      constexpr int S = decltype(I)::value - 1;
      if constexpr (S >= 0) {
        waitv<10 + n_x(S - 2) + n_x(S - 1)>();
        RAW_BARRIER();
      }
      if constexpr (S < 3) {
        u32x2 c[4];
        cs_read(c, csa + ((S + 1) % 3) * SLOT);
        stage(S + 3);
        cs_sum(c, c0, c1);
      } else stage(S + 3);
      if constexpr (S == 0) {
        asm volatile("" : "+v"(G));
        Tw = __builtin_amdgcn_readfirstlane(G) + 1u;
        if ((Tw & 0xfffu) == 0u) ++Tw;
        t12 = Tw & 0xfffu;
        if (tid == 256) flg[F_T] = Tw;
      }
      if constexpr (S == SB0) {
        const f32x4* rp = (const f32x4*)(smem + L_REC) + (l & 7);
        sm = ((rp[0] + rp[8]) + (rp[16] + rp[24])) + ((rp[32] + rp[40]) + (rp[48] + rp[56]));
        pinf(sm);
      }
      if constexpr (S == SB0 + 1) {
        mean = sm[0] * (1.0f / 1024.0f);
        var = __builtin_fmaf(-mean, mean, sm[2] * (1.0f / 1024.0f));
        if (lw == 3 && __ballot(!(var * 64.0f >= mean * mean)) != 0ull) {
#pragma unroll 1
          for (int r = 0; r < 8; ++r) {
            const float m = __builtin_bit_cast(float, __builtin_amdgcn_readlane(__builtin_bit_cast(int, mean), r));
            const u32x4* p = (const u32x4*)(x + (size_t)(row0 + r) * DM) + l * 2;
            const u32x4 a = p[0], c = p[1];
            float q = 0.f;
#pragma unroll
            for (int i = 0; i < 8; ++i) {
              const unsigned u = i < 4 ? a[i & 3] : c[i & 3];
              const float e0 = bflo(u) - m, e1 = bfhi(u) - m;
              q = __builtin_fmaf(e0, e0, q); q = __builtin_fmaf(e1, e1, q);
            }
            q = wave_total(q) * (1.0f / 1024.0f);
            if ((l & 7) == r) var = q;
          }
        }
        mean = rbf(mean); var = rbf(rbf(var) + 1e-5f);
        pin1(mean); pin1(var);
      }
      if constexpr (S == SB0 + SQ) { var = rbf(sqrtf(var)); pin1(var); }
      if constexpr (S == SB0 + SR) { sm[1] = rbf(__builtin_fmaf(-mean, sm[3], sm[1]) / var); pinf(sm); }
      if constexpr (S == SB0 + SK) {
        if (lw == 3) {
          const float r = sm[1];
          const unsigned u = __float_as_uint(r);
          const unsigned key = (r == r ? ((u & 0x80000000u) ? (~u & 0xffff0000u) : (u | 0x80000000u)) : 0u) | (unsigned)(2047 - row0 - (l & 7));
          const unsigned km = dpp_umax<0x141>(dpp_umax<0x4e>(dpp_umax<0xb1>(key)));
          pv = u32x4{key, __float_as_uint(mean) | (__float_as_uint(var) >> 16), Tw, Tw};
          po = WS_ST + (row0 + l) * 8;
          pm = __ballot(key == km) & 0xffull;
        } else {
          pv = u32x4{0u, __float_as_uint(sum_g(c0 + c1)), Tw, Tw};
          po = WS_CS + (((hp * 3 + lw) * 4 + (panel >> 2)) * 64 + (panel & 3) * 16 + l) * 8;
          pm = 0ull;
        }
        pw = u32x2{pv[1], Tw};
        pc = WS_CA + (panel * 16 + hp) * 16;
        pub_pin(pv, pw, po, pc);
      }
      if constexpr (S == PUB) pub(pv, pw, po, pc, lw == 3 ? 0xffull : 0xffffull, pm, ws);
      if constexpr (S == GAS) { GL4(ga[0], go0, gab, 0, P_SC1); GL4(ga[1], l * 16u, gab, 1024, P_SC1); }
      static_for<8>([&](auto J) { if constexpr (ES0 + decltype(J)::value * ESL / 8 == S) last(decltype(J)::value); });
      if constexpr (S == 11) { if (CAE) GL4(ca, WS_CA + lw * 1024 + l * 16, ws, 0, P_SC1); strm(IC(0), IC(FI), IC(0)); }
      if constexpr (S == CTS) touch4(min(((jj * 4 + lw) * 4 + l) * 128u, clen), pc0, lbase + L_DU);
      if constexpr (S == 0) glds(l * 16u, (const char*)(wphi + hp * 2048 + lw * 512), lbase + L_WP + lw * 1024);
    });
    waitv<10 + n_x(10) + n_x(11)>(); RAW_BARRIER();
    last(8); last(9);
    if (CTS == 12) touch4(min(((jj * 4 + lw) * 4 + l) * 128u, clen), pc0, lbase + L_DU);
    if (!CAE) GL4(ca, WS_CA + lw * 1024 + l * 16, ws, 0, P_SC1);
    auto tdv = [&]() { if (TD == 1) touchn(gu ? l * 2048u : 0u, (const char*)db, lbase + L_DU); if (TD == 2) touch(gu ? l * 2048u : 0u, (const char*)db, lbase + L_DU); };
    strm(IC(FI), IC(FI + NF), IC(0));
    if (TDP == 0) tdv();
    auto ga_bad = [&]() { return (ga[0][1] ^ Tw) | (ga[0][3] ^ Tw) | (ga[1][1] ^ Tw) | (ga[1][3] ^ Tw); };
    auto ga_put = [&]() {
      if (lw < 3) {
        const float u0 = __uint_as_float(ga[0][0]) + __uint_as_float(ga[1][0]), u1 = __uint_as_float(ga[0][2]) + __uint_as_float(ga[1][2]);
        const f32x2 a = sw32(u0, u1);
        ((float*)(smem + L_C))[lw * 64 + (l & 31) * 2 + (l >> 5)] = a[0] + a[1];
      } else {
        *(f32x2*)(smem + L_MN + l * 8) = f32x2{-bfhi(ga[0][0]), -bfhi(ga[0][2])};
        *(f32x2*)(smem + L_RS + l * 8) = f32x2{__builtin_amdgcn_rcpf(bflo(ga[0][0])), __builtin_amdgcn_rcpf(bflo(ga[0][2]))};
        *(unsigned*)(smem + L_DN + l * 4) = (ga[0][0] & 0xffffu) | (ga[0][2] << 16);
      }
      lds_set(flg + F_ST + lw, 1u, l);
    };
    asm volatile("s_waitcnt vmcnt(%2)" : "+v"(ga[0]), "+v"(ga[1]) : "n"(3 + NF + (CTS == 12) + FI + (TD && TDP == 0)) : "memory");
    RAW_BARRIER();
    strm(IC(FI + NF), IC(FI + NF + NG), IC(0));
    if (TDP == 1) tdv();
#pragma unroll 1
    for (unsigned spins = 0; spins < SPIN; ++spins) {
      if (all0(ga_bad())) break;
      asm volatile("global_load_dwordx4 %0, %2, %4 sc1\n\tglobal_load_dwordx4 %1, %3, %4 offset:1024 sc1\n\ts_waitcnt vmcnt(0)"
                   : "+v"(ga[0]), "+v"(ga[1]) : "v"(go0), "v"(l * 16u), "s"(gab) : "memory");
    }
    if (QW == 1) waitv<1 + NF + NG>();
    ga_put();
    strm(IC(FI + NF + NG), IC(FI + NF + NG + NH), IC(0));
    if (TDP == 2) tdv();
    asm volatile("s_waitcnt vmcnt(%1)" : "+v"(ca) : "n"(NF + NG + NH + (TD != 0)) : "memory");
    if (QW == 2) raise<F_GO>(lbase + L_F + lw * 4);
    __builtin_amdgcn_s_setprio(FP0);
#pragma unroll 1
    for (unsigned spins = 0; spins < SPIN; ++spins) {
      if (all0((ca[2] ^ Tw) | (ca[3] ^ Tw))) break;
      asm volatile("global_load_dwordx4 %0, %1, %2 sc1\n\ts_waitcnt vmcnt(0)" : "+v"(ca) : "v"(WS_CA + lw * 1024 + l * 16), "s"(ws) : "memory");
    }
    {
      unsigned m = dpp_umax<0x140>(dpp_umax<0x141>(dpp_umax<0x4e>(dpp_umax<0xb1>(ca[0]))));
      m = dpp_umax_r<0x143, 0xc>(dpp_umax_r<0x142, 0xa>(m));
      m = __builtin_amdgcn_readlane(m, 63);
      if (ca[0] == m) *(u32x2*)(flg + F_CA + 2 * lw) = u32x2{m, ca[1]};
      lds_set(flg + F_SD + lw, 1u, l);
    }
    const Sel sl = make_sel(l);
    unsigned voO[2];
#pragma unroll
    for (int i = 0; i < 2; ++i) voO[i] = (unsigned)((lw * 16 + i * 8 + srow) * 2048 + (sch ^ (((srow >> 1) & 1) | ((((srow >> 2) ^ i) & 1) << 2))) * 16);
    auto wotile = [&](int p) {
      const char* wv = (const char*)(wo + (size_t)(ptile(p) * 64) * DM + hp * 64);
      if (WX) return;
      waitv<WNO>();
      glds(voO[0], wv, lbase + p * 8192 + lw * 2048); glds(voO[1], wv, lbase + p * 8192 + lw * 2048 + 1024);
    };
    lds_wait4(flg + F_SD);
    u32x2 sel;
    {
      const u32x4 e0 = ldsr1(LDS_OFF(flg + F_CA)), e1 = ldsr1(LDS_OFF(flg + F_CA + 4));
      const bool p = e0[2] > e0[0], q = e1[2] > e1[0];
      const unsigned k0 = p ? e0[2] : e0[0], m0 = p ? e0[3] : e0[1], k1 = q ? e1[2] : e1[0], m1 = q ? e1[3] : e1[1];
      sel = k1 > k0 ? u32x2{k1, m1} : u32x2{k0, m0};
    }
    const unsigned key = __builtin_amdgcn_readfirstlane(sel[0]);
    const int idx = 2047 - (int)(key & 2047u);
    if (tid == 256) flg[F_SEL] = key;
    unsigned xd;
    GL1P(xd, l * 4u, (const char*)(x + (size_t)idx * DM + ke * 128));
    strm(IC(FI + NF + NG + NH), IC(DL ? 16 : 24), IC(WIN));
    if (WE || GL) { lds_wait4(flg + F_LD); wotile(12); wotile(13); wotile(15); }
    if (GL) { lds_wait4(flg + F_IM); __builtin_amdgcn_s_setprio(FPR); wotile(0); wotile(1); wotile(2); wotile(3); wotile(7); wotile(8); }
    asm volatile("s_waitcnt vmcnt(%9)" : "+v"(xd), "+v"(gv[0]), "+v"(gv[1]), "+v"(gv[2]), "+v"(gv[3]), "+v"(gv[4]), "+v"(gv[5]), "+v"(gv[6]), "+v"(gv[7]) : "n"((DL ? 0 : FI + NF + NG + NH > 16 ? 24 - (FI + NF + NG + NH) : 8) + (GL ? 18 : WE ? 6 : 0)) : "memory");
    asm volatile("" : "+v"(gv[8]), "+v"(gv[9]), "+v"(gv[10]), "+v"(gv[11]), "+v"(gv[12]), "+v"(gv[13]), "+v"(gv[14]), "+v"(gv[15]));
    char* hs = smem + L_H + lw * 256;
    {
      const float sm = bfhi(sel[1]), rden = 1.0f / bflo(sel[1]);
      const unsigned d = pk2(bflo(xd) - sm, bfhi(xd) - sm);
      *(unsigned*)(hs + l * 4) = pk2(bflo(d) * rden, bfhi(d) * rden);
      const u32x4 x0 = *(const u32x4*)(hs + rg * 32), x1 = *(const u32x4*)(hs + rg * 32 + 16);
      f32x4 d0 = gemv2(x0[0], gv[0], gv[1], sl, z4), d1 = gemv2(x0[1], gv[2], gv[3], sl, z4);
      d0 = gemv2(x0[2], gv[4], gv[5], sl, d0); d1 = gemv2(x0[3], gv[6], gv[7], sl, d1);
      d0 = gemv2(x1[0], gv[8], gv[9], sl, d0); d1 = gemv2(x1[1], gv[10], gv[11], sl, d1);
      d0 = gemv2(x1[2], gv[12], gv[13], sl, d0); d1 = gemv2(x1[3], gv[14], gv[15], sl, d1);
      const u32x4 v = gemv_fin(d0 + d1, t12);
      if (gu && (l & 40) == 0) gs4(WS_GU + (cb * 16 + (lw >> 1) * 8 + ke) * 256 + cg * 32 + g * 16, ws, v);
    }
    if (CT2) touch4(min(((jj * 4 + lw) * 4 + l) * 128u, clen), pc0, lbase + L_DU);
    __builtin_amdgcn_s_setprio(FPR);
    if (!WE && !GL) { lds_wait4(flg + F_LD); wotile(12); wotile(13); wotile(15); }
    if (!GL) { lds_wait4(flg + F_IM); wotile(0); wotile(1); wotile(2); wotile(3); wotile(7); wotile(8); }
    lds_wait4(flg + F_BL);
    if (DL == 1) strm(IC(16), IC(24), IC(WIN));
    u32x4 p[4];
    const char* gp = ws + WS_GU + kc * 4096;
    static_for<8>([&](auto I) {
      constexpr int i = decltype(I)::value;
      if constexpr (i == PE) { GL4(p[0], l * 16u, gp, 0, P_SC1); GL4(p[1], l * 16u, gp, 1024, P_SC1); GL4(p[2], l * 16u, gp, 2048, P_SC1); GL4(p[3], l * 16u, gp, 3072, P_SC1); }
      if constexpr (i < 7) wotile((0x4eba965u >> (4 * i)) & 15);
    });
    if (DL == 2) { lds_wait4(flg + F_SL); strm(IC(16), IC(24), IC(0)); waitv<8>(); } else waitv<0>();
    lds_set(flg + F_WO + lw, 1u, l);
    if (FPR != FPE) __builtin_amdgcn_s_setprio(FPE);
    waitv4<0>(p);
    asm volatile("" : "+v"(dv[0]), "+v"(dv[1]), "+v"(dv[2]), "+v"(dv[3]), "+v"(dv[4]), "+v"(dv[5]), "+v"(dv[6]), "+v"(dv[7]) :: "memory");
    if (gu) {
#pragma unroll 1
      for (unsigned spins = 0; spins < SPIN; ++spins) {
        const unsigned long long m0 = __ballot(bad4(p[0], t12) & 0xfffu), m1 = __ballot(bad4(p[1], t12) & 0xfffu), m2 = __ballot(bad4(p[2], t12) & 0xfffu),
                                 m3 = __ballot(bad4(p[3], t12) & 0xfffu);
        if ((m0 | m1 | m2 | m3) == 0ull) break;
        reload4c(p, l * 16u, gp, m0, m1, m2, m3);
      }
      float hv[4];
#pragma unroll
      for (int e = 0; e < 4; ++e) {
        const float gg = rbf(sum_g(untag12(p[0][e]) + untag12(p[1][e]))), uu = rbf(sum_g(untag12(p[2][e]) + untag12(p[3][e])));
        hv[e] = rbf(silu_f(gg)) * uu;
      }
      if (l < 16) *(u32x2*)(hs + l * 8) = u32x2{pk2(hv[0], hv[1]), pk2(hv[2], hv[3])};
      const u32x4 hp4 = *(const u32x4*)(hs + rg * 16);
      f32x4 d0 = gemv2(hp4[0], dv[0], dv[1], sl, z4), d1 = gemv2(hp4[1], dv[2], dv[3], sl, z4);
      d0 = gemv2(hp4[2], dv[4], dv[5], sl, d0); d1 = gemv2(hp4[3], dv[6], dv[7], sl, d1);
      const u32x4 v = gemv_fin(d0 + d1, t12);
      if ((l & 40) == 0) gs4(WS_Y + ((cq * 4 + lw) * 56 + kc) * 256 + cg * 32 + g * 16, ws, v);
    }
    if (FPG != FPE) __builtin_amdgcn_s_setprio(FPG);
    if ((idx >> 7) == panel) {
      const char* yb = ws + WS_Y + hp * 56 * 256 + lw * 4096;
      u32x2 xr;
      GL2(xr, r15 * 8u, (const char*)(x + (size_t)idx * DM + hp * 64), 0);
      const unsigned tm = lw < 3 ? 0xfffu : 0u;
      u32x4 pl[8];
      lds_wait4(flg + F_OS);
      if (PYD) __builtin_amdgcn_s_sleep(PYD);
      pollt<PPY>(p, pl, l * 16u, yb, t12, tm);
      asm volatile("" : "+v"(xr));
      f32x4 y;
#pragma unroll
      for (int e = 0; e < 4; ++e) y[e] = sum_g((untag12(p[0][e]) + untag12(p[1][e])) + (lw < 3 ? untag12(p[2][e]) + untag12(p[3][e]) : 0.f));
      if (lw) {
        if (l < 16) *(f32x4*)(hs + l * 16) = y;
        lds_set(flg + F_AL + lw, 1u, l);
      } else {
      if (l == 0) flg[F_AL] = 1u;
      lds_wait4(flg + F_AL);
      y = (y + *(const f32x4*)(hs + 256 + r15 * 16)) + (*(const f32x4*)(hs + 512 + r15 * 16) + *(const f32x4*)(hs + 768 + r15 * 16));
      const int row = idx & 127;
      const float mn = ((const float*)(smem + L_MN))[row], rs = ((const float*)(smem + L_RS))[row];
      const float r = __uint_as_float((key & 0x80000000u) ? (key & 0x7fff0000u) : (~key & 0xffff0000u));
      const float sg = rbf(1.0f / (1.0f + __expf(-r)));
      float gi[4];
#pragma unroll
      for (int e = 0; e < 4; ++e) {
        const float xe = (e & 1) ? bfhi(xr[e >> 1]) : bflo(xr[e >> 1]);
        gi[e] = silu_r(rbf(rbf(rbf(xe + mn) * rs) + rbf(rbf(y[e]) * sg)));
      }
      if (l < 16) *(u32x2*)(smem + L_GI + l * 8) = u32x2{pk2(gi[0], gi[1]), pk2(gi[2], gi[3])};
      lds_set(flg + F_GT, 1u, l);
      }
      asm volatile("s_waitcnt vmcnt(0)" :: PKEEP(pl) : "memory");
    }
    if (b == 0 && tid == 256) gs1(0u, ws, Tw);
    GAP(2);
    return;
  }
  const int s = w >> 3;
  const int wt = lw & 1, wc = lw >> 1;
  const char* ob = ws + WS_O + (size_t)((RM == 1 ? xcd * 2 + (jj & 1) : RM == 2 ? (xcd >> 1) * 4 + (jj & 3) : RM == 3 ? 0 : panel) * 4 + lw) * 65536;
  const char* xfb = (const char*)(x + (size_t)(panel * 128 + lw * 32 + s * 16) * DM + hp * 64);
  u32x4 xv[2], gq[2];
  unsigned T;
  auto gate = [&]() {
    const float mn = ((const float*)(smem + L_MN))[lw * 32 + s * 16 + r15], rs = ((const float*)(smem + L_RS))[lw * 32 + s * 16 + r15];
#pragma unroll
    for (int hf = 0; hf < 2; ++hf) {
      unsigned x0 = xv[hf][0], x1 = xv[hf][1], x2 = xv[hf][2], x3 = xv[hf][3];
      sw16u(x0, x2); sw16u(x1, x3);
      xv[hf] = u32x4{x0, x1, x2, x3};
#pragma unroll
      for (int e = 0; e < 4; ++e) {
        const unsigned d = pk2(bflo(xv[hf][e]) + mn, bfhi(xv[hf][e]) + mn);
        const unsigned y = pk2(bflo(d) * rs, bfhi(d) * rs);
        gq[hf][e] = pk2(silu_r(bflo(y)), silu_r(bfhi(y)));
      }
    }
    pin2(gq); pin2(xv);
  };
  const int wrow = 8 * g + tq, wsw = (((wrow >> 1) & 1) << 2) | (((wrow >> 3) & 1) << 1);
  unsigned asw[3][2], wa[3][2];
#pragma unroll
  for (int q = 0; q < 3; ++q) {
#pragma unroll
    for (int ks = 0; ks < 2; ++ks) asw[q][ks] = lbase + (wt * 64 + r15) * 128 + (((4 * ks + g) ^ ((r15 >> 1) & 7)) * 16) + q * SLOT;
#pragma unroll
    for (int c = 0; c < 2; ++c) wa[q][c] = lbase + ABYTES + wrow * 128 + (((wc * 4 + 2 * c + (tp >> 1)) ^ wsw) * 16) + 8 * (tp & 1) + q * SLOT;
  }
  if (s) {
    u32x4 xa[4], wr[4];
    auto st_load = [&]() {
      const char* xb = (const char*)(x + (size_t)row0 * DM + lw * 256);
      const unsigned o = (l & 7) * 2048u + ((l >> 3) & 1) * 64u + g * 16u;
      GL4(xa[0], o, xb, 0, P_DEF); GL4(xa[1], o, xb, 128, P_DEF); GL4(xa[2], o, xb, 256, P_DEF); GL4(xa[3], o, xb, 384, P_DEF);
      const char* rb = (const char*)(w_r + lw * 256);
      const unsigned ro = (l & 1) * 64u + g * 16u;
      GL4(wr[0], ro, rb, 0, P_DEF); GL4(wr[1], ro, rb, 128, P_DEF); GL4(wr[2], ro, rb, 256, P_DEF); GL4(wr[3], ro, rb, 384, P_DEF);
    };
    auto st_sum = [&](auto NV) {
      waitv4<decltype(NV)::value>(xa); waitv4<decltype(NV)::value>(wr);
      f32x4 d0 = z4, d1 = z4, d2 = z4, d3 = z4;
#pragma unroll
      for (int j = 0; j < 4; ++j) {
        d0 = MFMA(ones, as8(xa[j]), d0); d1 = MFMA(as8(wr[j]), as8(xa[j]), d1);
        d2 = MFMA(as8(xa[j]), as8(xa[j]), d2); d3 = MFMA(as8(wr[j]), ones, d3);
      }
      const bool hf = l & 8;
      const float q2 = (l & 2) ? ((l & 1) ? d2[3] : d2[2]) : ((l & 1) ? d2[1] : d2[0]);
      if (g == tq) ldsw1(lbase + L_REC + (lw * 16 + r15) * 16, __builtin_bit_cast(u32x4, f32x4{d0[0], hf ? d1[1] : d1[0], q2, hf ? d3[1] : d3[0]}));
      SB();
    };
    f32x4 acc[4][2];
    bf16x8 w0, w1;
    float sc[4];
    const char* kb = ws + WS_KVR + hp * 8192 + wc * 4096;
    unsigned l16, fsl = LDS_OFF(flg + F_SL + 2 * wc);
    {
#pragma unroll
      for (int t = 0; t < 4; ++t) { acc[t][0] = z4; acc[t][1] = z4; }
      int ts = (hp - 4 * (panel >> 2)) & 15;
      const unsigned xr = lbase + (ts == 15 ? L_EX : (ts % 3) * SLOT) + (lw * 32 + 16 + r15) * 128 + ((g ^ ((r15 >> 1) & 7)) * 16);
      ts = min(ts, 13);
      Frag<2> f[2];
      auto rd = [&](Frag<2>& y, auto I, auto OW, unsigned pa, const unsigned (&pw)[2]) {
        constexpr int i = decltype(I)::value, ow = decltype(OW)::value + 2 * WSLAB;
        if constexpr (i < 4) ds_128<i * 2048>(y.af[i], pa);
        else if constexpr (i & 1) ds_tr<ow + 512>(y.wh[(i - 4) >> 1], pw[(i - 4) >> 1]);
        else ds_tr<ow>(y.wl[(i - 4) >> 1], pw[(i - 4) >> 1]);
      };
      auto step = [&](Frag<2>& c, Frag<2>& y, auto OW, unsigned pa, const unsigned (&pw)[2], auto RD, auto XT) {
        static_for<8>([&](auto I) {
          constexpr int i = decltype(I)::value;
          acc[i & 3][i >> 2] = MFMA(cat8(c.wl[i >> 2], c.wh[i >> 2]), c.af[i & 3], acc[i & 3][i >> 2]);
          SB();
          if constexpr (i < 4 && decltype(RD)::value) { rd(y, IC(2 * i), OW, pa, pw); rd(y, IC(2 * i + 1), OW, pa, pw); SB(); }
          if constexpr (i == 4 && decltype(XT)::value >= 0) { ds_if<decltype(XT)::value>(xv, xr, ts); SB(); }
        });
      };
      auto tile = [&](auto J, auto BAR, auto RD, auto&& side) {
        constexpr int j = decltype(J)::value % 3, n = (j + 1) % 3;
        f[0].wait();
        step(f[0], f[1], IC(4096), asw[j][1], wa[j], IC(1), IC(decltype(BAR)::value ? decltype(J)::value : -1));
        f[1].wait();
        if constexpr (decltype(BAR)::value) { __builtin_amdgcn_s_barrier(); asm volatile("" ::: "memory"); }
        side();
        if constexpr (!decltype(RD)::value) { if (l == 0) __hip_atomic_store(flg + F_BL + lw, 1u, __ATOMIC_RELAXED, __HIP_MEMORY_SCOPE_WORKGROUP); SB(); }
        step(f[1], f[0], IC(0), asw[n][0], wa[n], RD, IC(-1));
      };
      auto sents = [&]() {
        unsigned ln;
        asm volatile("v_mbcnt_lo_u32_b32 %0, -1, 0\n\tv_mbcnt_hi_u32_b32 %0, -1, %0" : "=v"(ln));
        u32x4 ff = {~0u, ~0u, ~0u, ~0u};
        asm volatile("" : "+v"(ff));
        const unsigned ko = (ln >> 3) * 2048u + (ln & 7) * 16u + ((panel ^ (ln >> 3)) & 15) * 128u;
        static_for<4>([&](auto J) { gs4o<decltype(J)::value * 1024>(ln * 16u, ws + WS_O + (size_t)(panel * 4 + lw) * 65536 + hp * 4096, ff); });
        gs4(ko, ws + WS_KVP + (hp * 4 + lw) * 32768, ff);
        gs4(ko ^ 17408u, ws + WS_KVP + (hp * 4 + lw) * 32768, ff);
        gs4m(ln * 16u, ws + WS_KVR + hp * 8192 + lw * 2048 + panel * 128, ff);
        SB();
      };
      asm volatile("" : "=v"(xv[0]), "=v"(xv[1]));
      TICK();
      static_for<8>([&](auto I) { rd(f[0], I, IC(0), asw[0][0], wa[0]); });
      static_for<13>([&](auto J) {
        constexpr int t = decltype(J)::value;
        if constexpr (t == 0) st_load();
        if constexpr (t == SBP) { waitv<0>(); SB(); }
        tile(J, IC(1), IC(1), [&]() {
          if constexpr ((t == SBI && (HB & 1)) || (t == 1 && (HB & 2))) {
            __builtin_amdgcn_s_setprio(3);
            if constexpr (t == SBI) sents();
            if constexpr (t == 1) st_sum(IC(0));
            __builtin_amdgcn_s_setprio(0);
            SB();
          }
        });
        if constexpr (t == 1 && !(HB & 2)) {
          st_sum(IC(0));
        }
        if constexpr (t == TP3) { if (lw == 0 && !(xcd & 1) && jj < 24) pf3((xcd >> 2) ^ 1); }
        if constexpr (t == TPW) {
          if (lw & 1) touch((l >> 2) * 2048u + (l & 3) * 128u, (const char*)wo + jj * 65536 + (lw >> 1) * 32768 + (xcd & 3) * 512, lbase + L_DU);
        }
        if constexpr (t == 2) MARK(2);
        if constexpr (t == SBI && !(HB & 1)) sents();
        if constexpr (PRE && t == PRE) {
          unsigned ln;
          asm volatile("v_mbcnt_lo_u32_b32 %0, -1, 0\n\tv_mbcnt_hi_u32_b32 %0, -1, %0" : "=v"(ln));
          const unsigned sl = ln & 15;
          const unsigned o = ln < 16 ? WS_KVP + (hp * 4 + lw) * 32768 + sl * 2048 + ((panel ^ sl) & 15) * 128
                           : ln < 32 ? WS_KVR + hp * 8192 + lw * 2048 + panel * 128 : WS_O + (panel * 4 + lw) * 65536 + hp * 4096 + (ln & 31) * 128;
          touch(o, ws, lbase + L_DU);
          SB();
        }
      });
      __builtin_amdgcn_s_barrier(); asm volatile("" ::: "memory");
      VISIT(3);
      if (CP == 2) { VISIT(8); VISIT(9); }
      relane();
      asw[0][0] += L_EX; asw[0][1] += L_EX;
      ds_if<13>(xv, xr, ts);
      wphi_frag(w0, w1, lbase + L_WP + wc * 2048 + (4 * g + tq) * 64 + tp * 8);
      lds_wait4(flg + F_ST);
      {
        const f32x4 c0 = *(const f32x4*)(smem + L_C + 512 + (wc * 32 + 4 * g) * 4), c1 = *(const f32x4*)(smem + L_C + 512 + (wc * 32 + 16 + 4 * g) * 4);
        f32x2 mr[4];
        rowst(mr, lbase + L_MN + (wt * 64 + r15) * 4);
#pragma unroll
        for (int t = 0; t < 4; ++t) {
          sc[t] = mr[t][1] * L2E;
          acc[t][0] += mr[t][0] * c0; acc[t][1] += mr[t][0] * c1;
          asm volatile("" : "+v"(sc[t]), "+v"(acc[t][0]), "+v"(acc[t][1]));
        }
      }
      asm volatile("" : "+v"(xv[0]), "+v"(xv[1]), "+v"(w0), "+v"(w1));
      if (GB) gate();
      SB();
      asm volatile("" : "+s"(kb));
      l16 = l * 16u;
      asm volatile("" : "+v"(l16), "+v"(fsl));
      GAP(6);
      VISIT(6);
      lds_wait1(flg + (BQ ? F_EX : F_IM) + lw);
      __builtin_amdgcn_s_setprio(BP);
      tile(IC(1), IC(0), IC(1), []() {});
      if (QW != 1) lds_wait4(flg + (QW == 2 ? F_GO : F_SD));
      tile(IC(2), IC(0), IC(1), []() {}); tile(IC(0), IC(0), IC(0), []() {});
    }
    bf16x8 pq[4][2];
    {
      f32x4 p[4][2];
      const float one[2] = {1.f, 1.f};
#pragma unroll
      for (int t = 0; t < 4; ++t) {
        const f32x4 v0 = acc[t][0], v1 = acc[t][1];
        const bf16x8 qp = as8(u32x4{pk2(v0[0], v0[1]), pk2(v0[2], v0[3]), pk2(v1[0], v1[1]), pk2(v1[2], v1[3])});
        p[t][0] = MFMA(w0, qp, z4); p[t][1] = MFMA(w1, qp, z4);
      }
      phi_map<false>(p, sc, one, [&](int t, const unsigned (&pe)[8]) {
        pq[t][0] = as8(u32x4{pe[0], pe[1], pe[2], pe[3]}); pq[t][1] = as8(u32x4{pe[4], pe[5], pe[6], pe[7]});
      });
    }
    asm volatile("" : "+v"(pq[0][0]), "+v"(pq[0][1]), "+v"(pq[1][0]), "+v"(pq[1][1]), "+v"(pq[2][0]), "+v"(pq[2][1]), "+v"(pq[3][0]), "+v"(pq[3][1]));
    if (!GB) gate();
    {
      f32x2 rd[4];
      u32x4 kf[4];
      PAD(6);
#pragma unroll 1
      for (unsigned spins = 0; spins < SPIN; ++spins) {
        u32x2 v; unsigned long long ck;
        asm volatile("ds_read_b64 %0, %2\n\ts_memrealtime %1\n\ts_waitcnt lgkmcnt(0)" : "=v"(v), "=s"(ck) : "v"(fsl) : "memory");
        const unsigned t0 = __builtin_amdgcn_readfirstlane(v[0]), t1 = __builtin_amdgcn_readfirstlane(v[1]), t = __builtin_amdgcn_readfirstlane((unsigned)ck);
        if ((t0 & t1) && min(t - t0, t - t1) >= D2) break;
      }
      GL4(kf[0], l16, kb, 0, P_SC1); GL4(kf[1], l16, kb, 1024, P_SC1); GL4(kf[2], l16, kb, 2048, P_SC1); GL4(kf[3], l16, kb, 3072, P_SC1);
      lds_wait4(flg + F_BL);
      lds_wait2(flg + F_KV + 2 * wc);
      {
        bf16x8 z0 = *(const bf16x8*)(smem + L_Z + wc * 128 + g * 16), z1 = *(const bf16x8*)(smem + L_Z + wc * 128 + 64 + g * 16);
        if (l & 1) { z0 = ones; z1 = ones; }
#pragma unroll
        for (int t = 0; t < 4; ++t) {
          const f32x4 a = MFMA(z0, pq[t][0], z4), c = MFMA(z1, pq[t][1], z4);
          const f32x2 r = {__builtin_amdgcn_rcpf(a[1]), __builtin_amdgcn_rcpf(c[1])};
          rd[t] = r * __builtin_amdgcn_rcpf(__builtin_fmaf(a[0], r[0], __builtin_fmaf(c[0], r[1], 1e-6f)));
          asm volatile("" : "+v"(rd[t]));
        }
      }
      const char* pb = ws + WS_O + (size_t)(panel * 4 + wt * 2) * 65536 + hp * 4096 + wc * 2048;
      unsigned l2 = l16 + 65536u, oa = lbase + L_EX + wc * 8192 + wt * 4096 + l * 16, fo = LDS_OFF(flg + F_OS + lw);
      asm volatile("" : "+s"(pb));
      asm volatile("" : "+v"(l2), "+v"(oa), "+v"(fo));
      waitv4<0>(kf);
      if (__builtin_expect(__ballot(max(max(kf[0][3], kf[1][3]), max(kf[2][3], kf[3][3])) == ~0u) != 0ull, 0)) {
        PAD(9);
#pragma unroll 1
        for (unsigned spins = 0; spins < SPIN; ++spins) {
          const unsigned long long m0 = __ballot(kf[0][3] == ~0u), m1 = __ballot(kf[1][3] == ~0u), m2 = __ballot(kf[2][3] == ~0u), m3 = __ballot(kf[3][3] == ~0u);
          if ((m0 | m1 | m2 | m3) == 0ull) break;
          reload4c(kf, l16, kb, m0, m1, m2, m3);
        }
      }
      u32x4 ov[4];
      f32x4 n[4][4];
      auto mm = [&](int t, int i) { n[t][i] = MFMA(as8(kf[i]), pq[t][i >> 1], z4); SB(); };
#pragma unroll
      for (int i = 0; i < 8; ++i) mm(i >> 2, i & 3);
      static_for<4>([&](auto TT) {
        constexpr int t = decltype(TT)::value;
        f32x4 a = n[t][0] * rd[t][0], c = n[t][1] * rd[t][0];
        SB(); if (t < 2) mm(t + 2, 0);
        a += n[t][2] * rd[t][1];
        SB(); if (t < 2) mm(t + 2, 1);
        c += n[t][3] * rd[t][1];
        SB(); if (t < 2) mm(t + 2, 2);
        ov[t] = u32x4{pk2(a[0], a[1]), pk2(a[2], a[3]), pk2(c[0], c[1]), nosent(pk2(c[2], c[3]))};
        SB(); if (t < 2) mm(t + 2, 3);
        asm volatile("global_store_dwordx4 %0, %1, %2 offset:%3 sc1" :: "v"(t < 2 ? l16 : l2), "v"(ov[t]), "s"(pb), "n"((t & 1) * 1024) : "memory");
        SB();
      });
      asm volatile("ds_write_b128 %0, %1\n\tds_write_b128 %0, %2 offset:1024\n\tds_write_b128 %0, %3 offset:2048\n\tds_write_b128 %0, %4 offset:3072\n\tds_write_b32 %5, %5\n\ts_nop 0"
                   :: "v"(oa), "v"(ov[0]), "v"(ov[1]), "v"(ov[2]), "v"(ov[3]), "v"(fo) : "memory");
    }
    __builtin_amdgcn_s_setprio(0);
    GAP(3);
  } else {
    if (tid < 20) ((u32x4*)flg)[tid] = u32x4{0, 0, 0, 0};
    f32x4 acc[4][4];
    Frag<4> f[2];
    u32x4 fs;
    f32x4 c0, c1;
    f32x2 mr[4];
    s16x4 wq[4];
    unsigned mra = lbase + L_MN + (wt * 64 + r15) * 4, cca = lbase + L_C + (wc * 32 + 4 * g) * 4, wpa = lbase + L_WP + wc * 2048 + (4 * g + tq) * 64 + tp * 8, fa = lbase + L_F;
    char* img = smem + lw * 8192;
    unsigned va = lbase + SLOT + ABYTES + lw * 4096 + l * 16, fb = lbase + L_F + lw * 4, ia, wi[4], ko = kvo(), da = lbase + L_DN + wt * 128 + g * 8,
             zo = WS_ZP + ((hp * 4 + lw) * 16 + panel) * 128 + (r15 * 4 + g) * 16, m4a = lbase + L_MN + (wt * 64 + 4 * g) * 4, cva = lbase + L_C + (64 + wc * 32 + r15) * 4;
    {
      const int row = 4 * g + tq, sx = (((row >> 1) & 3) << 2) | (row & 1) | (((row >> 3) & 1) << 1);
      ia = LDS_OFF(img) + row * 128 + (((8 * wt + tp) ^ sx) * 8);
      const int sw = (((r15 >> 1) & 3) << 2) | (r15 & 1) | (((r15 >> 3) & 1) << 1);
#pragma unroll
      for (int i = 0; i < 4; ++i) wi[i] = LDS_OFF(img) + r15 * 128 + (((4 * i + g) ^ sw) * 8);
    }
    {
#pragma unroll
      for (int t = 0; t < 4; ++t)
#pragma unroll
        for (int c = 0; c < 4; ++c) acc[t][c] = z4;
      auto rd = [&](Frag<4>& y, auto I, auto OW, unsigned pa, const unsigned (&pw)[2]) {
        constexpr int i = decltype(I)::value, ow = decltype(OW)::value;
        if constexpr (i < 4) ds_128<i * 2048>(y.af[i], pa);
        else {
          constexpr int c = (i - 4) >> 1;
          if constexpr (i & 1) ds_tr<ow + (c >> 1) * WSLAB + 512>(y.wh[c], pw[c & 1]);
          else ds_tr<ow + (c >> 1) * WSLAB>(y.wl[c], pw[c & 1]);
        }
      };
      auto step = [&](Frag<4>& c, Frag<4>& y, auto OW, unsigned pa, const unsigned (&pw)[2], auto&& more) {
        static_for<16>([&](auto I) {
          constexpr int i = decltype(I)::value, t = i & 3, cb = i >> 2;
          if constexpr (cb < 2) mfma_t(acc[t][cb], cat8(c.wl[cb], c.wh[cb]), __builtin_bit_cast(u32x4, c.af[t]));
          else mfma_t(acc[t][cb], c.af[t], __builtin_bit_cast(u32x4, cat8(c.wl[cb], c.wh[cb])));
          if constexpr (i < 12) rd(y, I, OW, pa, pw); else more(I);
        });
      };
      auto none = [](auto) {};
      __builtin_amdgcn_s_setprio(2);
#pragma unroll
      for (int t = 0; t < 4; ++t) asm volatile("" : "+v"(acc[t][0]), "+v"(acc[t][1]), "+v"(acc[t][2]), "+v"(acc[t][3]));
      asm volatile("" : "+v"(mra), "+v"(cca), "+v"(wpa), "+v"(fa), "+v"(va), "+v"(fb), "+v"(ia), "+v"(wi[0]), "+v"(wi[1]), "+v"(wi[2]), "+v"(wi[3]),
                   "+v"(ko), "+v"(da), "+v"(zo), "+v"(m4a), "+v"(cva));
      TICK();
      static_for<12>([&](auto I) { rd(f[0], I, IC(0), asw[0][0], wa[0]); });
#pragma unroll 1
      for (int it = 0; it < 5; ++it) {
        static_for<3>([&](auto J) {
          constexpr int j = decltype(J)::value, n = (j + 1) % 3;
          f[0].wait();
          step(f[0], f[1], IC(4096), asw[j][1], wa[j], none);
          if constexpr (j == 1) {
            const unsigned dx = it == 4 ? L_EX : 0, dw = it == 4 ? L_EW - ABYTES : 0;
            asw[0][0] += dx; asw[0][1] += dx; wa[0][0] += dw; wa[0][1] += dw;
            asm volatile("" : "+v"(asw[0][0]), "+v"(asw[0][1]), "+v"(wa[0][0]), "+v"(wa[0][1]));
          }
          f[1].wait();
          if (j < 2 || __builtin_expect(it < 4, 1)) __builtin_amdgcn_s_barrier();
          asm volatile("" ::: "memory");
          step(f[1], f[0], IC(0), asw[n][0], wa[n], none);
        });
      }
      f[0].wait();
      step(f[0], f[1], IC(4096), asw[0][1], wa[0], [&](auto I) {
        constexpr int i = decltype(I)::value;
        if constexpr (i == 12) { ds_tr<0>(wq[0], wpa); ds_tr<1024>(wq[1], wpa); ds_tr<32>(wq[2], wpa); }
        if constexpr (i == 13) { ds_tr<1056>(wq[3], wpa); ds_32<F_T * 4>(T, fa); }
      });
      asm volatile("s_waitcnt lgkmcnt(5)" : "+v"(f[1].af[0]), "+v"(f[1].af[1]), "+v"(f[1].af[2]), "+v"(f[1].af[3]), "+v"(f[1].wl[0]), "+v"(f[1].wh[0]), "+v"(f[1].wl[1]), "+v"(f[1].wh[1]),
                   "+v"(f[1].wl[2]), "+v"(f[1].wh[2]), "+v"(f[1].wl[3]), "+v"(f[1].wh[3]));
      static_for<8>([&](auto I) {
        constexpr int i = decltype(I)::value, t = i >> 1, cb = i & 1;
        acc[t][cb] = MFMA(cat8(f[1].wl[cb], f[1].wh[cb]), f[1].af[t], acc[t][cb]);
        SB();
        if constexpr (i == 0) { ds_128v<F_ST * 4>(fs, fa); ds_128v<0>(c0, cca); ds_128v<64>(c1, cca); }
        if constexpr (i == 1) { ds_2x32<0>(mr[0], mra); ds_2x32<16>(mr[1], mra); }
        if constexpr (i == 2) { ds_2x32<32>(mr[2], mra); ds_2x32<48>(mr[3], mra); }
        if constexpr (i == 3) raise<F_LD>(fb);
        SB();
      });
    }
    if (AP != 2) __builtin_amdgcn_s_setprio(AP);
    asm volatile("s_waitcnt lgkmcnt(0)" : "+v"(fs), "+v"(c0), "+v"(c1), "+v"(mr[0]), "+v"(mr[1]), "+v"(mr[2]), "+v"(mr[3]), "+v"(wq[0]), "+v"(wq[1]), "+v"(wq[2]), "+v"(wq[3]), "+v"(T));
    if (__builtin_expect(!__builtin_amdgcn_readfirstlane(fs[0] & fs[1] & fs[2] & fs[3]), FIN)) {
      lds_wait4(flg + F_ST);
      asm volatile("ds_read_b128 %0, %2\n\tds_read_b128 %1, %2 offset:64\n\ts_waitcnt lgkmcnt(0)" : "=&v"(c0), "=&v"(c1) : "v"(cca) : "memory");
      rowst(mr, mra);
    }
    T = __builtin_amdgcn_readfirstlane(T);
    const unsigned t12 = T & 0xfffu;
    const bf16x8 w0 = cat8(wq[0], wq[1]), w1 = cat8(wq[2], wq[3]);
    u32x4 vp[2][4];
    auto selm = [](unsigned long long m, float a, float c) { float r; asm("v_cndmask_b32 %0, %1, %2, %3" : "=v"(r) : "v"(c), "v"(a), "s"(m)); return r; };
    {
      f32x4 p[4][2], m4[4];
      f32x2 cv;
      float sc[4], nrm[2];
#pragma unroll
      for (int t = 0; t < 4; ++t) {
        sc[t] = mr[t][1] * L2E;
        if (t < 2) nrm[t] = selm(0xffff0000ffff0000ull, mr[2 * t + 1][1], mr[2 * t][1]);
        const f32x4 v0 = mr[t][0] * c0 + acc[t][0], v1 = mr[t][0] * c1 + acc[t][1];
        const bf16x8 kc = as8(u32x4{pk2(v0[0], v0[1]), pk2(v0[2], v0[3]), pk2(v1[0], v1[1]), pk2(v1[2], v1[3])});
        p[t][0] = MFMA(w0, kc, z4); p[t][1] = MFMA(w1, kc, z4);
      }
      auto vl = [&](int i0, int i1) {
#pragma unroll
        for (int i = i0; i < i1; ++i)
          acc[i & 3][2 + (i >> 2)] = MFMA(f[1].af[i & 3], cat8(f[1].wl[2 + (i >> 2)], f[1].wh[2 + (i >> 2)]), acc[i & 3][2 + (i >> 2)]);
      };
      SB();
      PAD(3);
      SB();
      phi_map<true>(p, sc, nrm, [&](int t, const unsigned (&pe)[8]) {
        if (t < 2) { SB(); vl(4 * t, 4 * t + 4); SB(); }
        if (t == 2) asm volatile("s_waitcnt lgkmcnt(0)" : "+v"(m4[0]), "+v"(m4[1]), "+v"(m4[2]), "+v"(m4[3]), "+v"(cv) :: "memory");
#pragma unroll
        for (int i = 0; i < 4; ++i) *(u32x2*)(smem + (wi[i] - lbase) + t * 2048) = u32x2{pe[2 * i], pe[2 * i + 1]};
        if (t == 1) {
          asm volatile("ds_read_b128 %0, %5\n\tds_read_b128 %1, %5 offset:64\n\tds_read_b128 %2, %5 offset:128\n\tds_read_b128 %3, %5 offset:192\n\tds_read2_b32 %4, %6 offset1:16"
                       : "=&v"(m4[0]), "=&v"(m4[1]), "=&v"(m4[2]), "=&v"(m4[3]), "=&v"(cv)
                       : "v"(m4a), "v"(cva) : "memory");
          SB();
        }
        if (t == VPT) {
#pragma unroll
          for (int d = 0; d < 2; ++d)
#pragma unroll
            for (int q = 0; q < 2; ++q) {
              const f32x4 a = m4[2 * q] * cv[d] + acc[2 * q][2 + d], c = m4[2 * q + 1] * cv[d] + acc[2 * q + 1][2 + d];
              vp[0][2 * q + d] = u32x4{pk2(a[0], a[1]), pk2(a[2], a[3]), pk2(c[0], c[1]), pk2(c[2], c[3])};
            }
          asm volatile("ds_write_b128 %0, %1\n\tds_write_b128 %0, %2 offset:1024\n\tds_write_b128 %0, %3 offset:2048\n\tds_write_b128 %0, %4 offset:3072"
                       :: "v"(va), "v"(vp[0][0]), "v"(vp[0][1]), "v"(vp[0][2]), "v"(vp[0][3]) : "memory");
        }
      });
    }
    const char* kb = ws + WS_KVP + (hp * 4 + lw) * 32768;
    {
      s16x4 tl[2][2], th[2][2];
#define IMG8 "ds_read_b64_tr_b16 %0, %8\n\tds_read_b64_tr_b16 %1, %8 offset:2048\n\tds_read_b64_tr_b16 %2, %9\n\tds_read_b64_tr_b16 %3, %9 offset:2048\n\t" \
             "ds_read_b64_tr_b16 %4, %8 offset:4096\n\tds_read_b64_tr_b16 %5, %8 offset:6144\n\tds_read_b64_tr_b16 %6, %9 offset:4096\n\tds_read_b64_tr_b16 %7, %9 offset:6144\n\t"
#define IMGO(c) c(tl[0][0]), c(th[0][0]), c(tl[0][1]), c(th[0][1]), c(tl[1][0]), c(th[1][0]), c(tl[1][1]), c(th[1][1])
      raise<F_EX>(fb);
      asm volatile(IMG8 : IMGO("=&v") : "v"(ia), "v"(ia ^ 32u) : "memory");
      {
        u32x4 d0, d1, d2, d3;
        asm volatile("ds_read2_b64 %0, %4 offset1:4\n\tds_read2_b64 %1, %4 offset0:8 offset1:12\n\tds_read2_b64 %2, %5 offset1:4\n\tds_read2_b64 %3, %5 offset0:8 offset1:12"
                     : "=&v"(d0), "=&v"(d1), "=&v"(d2), "=&v"(d3) : "v"(da), "v"(da ^ 128u) : "memory");
        unsigned o1 = ko ^ 17408u, ib = ia ^ 8192u, ic = ia ^ 8224u, fx, fe = (fb ^ 4u) + F_EX * 4;
        va ^= 4096u;
        asm volatile("s_waitcnt lgkmcnt(4)" : IMGO("+v"), "+v"(o1), "+v"(ib), "+v"(ic), "+v"(va), "+v"(fe), "+s"(kb) :: "memory");
        f32x4 kva[2][2], za[2];
        bf16x8 a[2][2];
#pragma unroll
        for (int q = 0; q < 2; ++q)
#pragma unroll
          for (int fb = 0; fb < 2; ++fb) {
            a[q][fb] = cat8(tl[q][fb], th[q][fb]);
#pragma unroll
            for (int d = 0; d < 2; ++d) kva[fb][d] = MFMA(a[q][fb], as8(vp[0][2 * q + d]), q ? kva[fb][d] : z4);
          }
        SB();
        s16x4 ul[2][2], uh[2][2];
#define IMGP(c) c(ul[0][0]), c(uh[0][0]), c(ul[0][1]), c(uh[0][1]), c(ul[1][0]), c(uh[1][0]), c(ul[1][1]), c(uh[1][1])
#define OTH "ds_read_b32 %10, %15\n\t" IMG8 "ds_read_b128 %11, %16\n\tds_read_b128 %12, %16 offset:1024\n\tds_read_b128 %13, %16 offset:2048\n\tds_read_b128 %14, %16 offset:3072"
        asm volatile("s_waitcnt lgkmcnt(0)" : "+v"(d0), "+v"(d1), "+v"(d2), "+v"(d3) :: "memory");
        asm volatile(OTH : IMGP("=&v"), "+v"(ib), "+v"(ic), "=&v"(fx), "=&v"(vp[1][0]), "=&v"(vp[1][1]), "=&v"(vp[1][2]), "=&v"(vp[1][3]) : "v"(fe), "v"(va) : "memory");
        SB();
#pragma unroll
        for (int fb = 0; fb < 2; ++fb) za[fb] = MFMA(a[1][fb], as8(d1), MFMA(a[0][fb], as8(d0), z4));
        SB();
        asm volatile("s_waitcnt lgkmcnt(0)" : IMGP("+v"), "+v"(fx), "+v"(vp[1][0]), "+v"(vp[1][1]), "+v"(vp[1][2]), "+v"(vp[1][3]) :: "memory");
        if (__builtin_expect(__builtin_amdgcn_readfirstlane(fx) == 0u, 0)) {
          PAD(8);
#pragma unroll 1
          for (unsigned spins = 0; spins < SPIN && __builtin_amdgcn_readfirstlane(fx) == 0u; ++spins)
            asm volatile(OTH "\n\ts_waitcnt lgkmcnt(0)" : IMGP("+v"), "+v"(ib), "+v"(ic), "+v"(fx), "+v"(vp[1][0]), "+v"(vp[1][1]), "+v"(vp[1][2]), "+v"(vp[1][3]) : "v"(fe), "v"(va) : "memory");
        }
#pragma unroll
        for (int d = 0; d < 2; ++d)
#pragma unroll
          for (int q = 0; q < 2; ++q)
#pragma unroll
            for (int fb = 0; fb < 2; ++fb) kva[fb][d] = MFMA(cat8(ul[q][fb], uh[q][fb]), as8(vp[1][2 * q + d]), kva[fb][d]);
        SB();
#pragma unroll
        for (int d = 0; d < 2; ++d) {
          za[d] = MFMA(cat8(ul[1][d], uh[1][d]), as8(d3), MFMA(cat8(ul[0][d], uh[0][d]), as8(d2), za[d]));
          const f32x4 u = kva[0][d], v = kva[1][d];
          gs4(d ? o1 : ko, kb, u32x4{pk2(u[0], u[1]), pk2(u[2], u[3]), pk2(v[0], v[1]), nosent(pk2(v[2], v[3]))});
        }
        raise<F_IM>(fb);
        relane();
        const f32x4 z = (l & 1) ? za[1] : za[0];
        if (r15 < 2) gs4(zo, ws, u32x4{tag12(z[0], t12), tag12(z[1], t12), tag12(z[2], t12), tag12(z[3], t12)});
      }
    }
    GL4(xv[0], xo, xfb, 0, P_DEF); GL4(xv[1], xo, xfb, 64, P_DEF);
    const char* zb = ws + WS_ZP + (hp * 4 + lw) * 2048;
    u32x4 zp[2], pl[4];
    {
      u32x4 p[2];
      const unsigned one = (l & 1) ? 0x3f800000u : 0x3f80u;
      u32x4 s0 = {(l & 2) ? 0u : one, (l & 2) ? one : 0u, 0u, 0u}, s1 = {0u, 0u, s0[0], s0[1]};
      asm volatile("" : "+v"(s0), "+v"(s1));
      GAP(7);
      VISIT(7);
      if (PD1) __builtin_amdgcn_s_sleep(PD1);
      poll2<PP1>(p, pl, l * 16u + panel * 2048, kb);
      asm volatile("" : "+v"(xv[0]), "+v"(xv[1]));
      GL4(zp[0], l * 16u, zb, 0, P_SC1); GL4(zp[1], l * 16u, zb, 1024, P_SC1);
      f32x4 u = MFMA(as8(s0), as8(p[1]), MFMA(as8(s0), as8(p[0]), z4)), v = MFMA(as8(s1), as8(p[1]), MFMA(as8(s1), as8(p[0]), z4));
#pragma unroll
      for (int e = 0; e < 4; ++e) { u[e] += ror8(u[e]); v[e] += ror8(v[e]); }
      gs4m(l * 16u, ws + WS_KVR + hp * 8192 + lw * 2048 + panel * 128, u32x4{pk2(u[0], u[1]), pk2(u[2], u[3]), pk2(v[0], v[1]), nosent(pk2(v[2], v[3]))});
      asm volatile("ds_write_b32 %0, %1 offset:%2" :: "v"(fb), "v"(now() | 1u), "n"(F_SL * 4) : "memory");
    }
    {
      asm volatile("s_waitcnt vmcnt(1)" : "+v"(zp[0]), "+v"(zp[1]) :: "memory");
      asm volatile("" :: "v"(pl[0]), "v"(pl[1]), "v"(pl[2]), "v"(pl[3]));
#pragma unroll 1
      for (unsigned spins = 0; spins < SPIN; ++spins) {
        const unsigned long long m0 = __ballot(bad4(zp[0], t12) & 0xfffu), m1 = __ballot(bad4(zp[1], t12) & 0xfffu);
        if ((m0 | m1) == 0ull) break;
        reload2(zp, l * 16u, zb, m0, m1);
      }
      float z[4];
#pragma unroll
      for (int e = 0; e < 4; ++e) { const float q = sum_g(untag12(zp[0][e]) + untag12(zp[1][e])); z[e] = q + ror8(q); }
      if (l < 8) *(u32x2*)(smem + L_Z + lw * 64 + (l & 3) * 16 + (l >> 2) * 8) = u32x2{pk2(z[0], z[1]), pk2(z[2], z[3])};
      lds_set(flg + F_KV + lw, 1u, l);
    }
    __builtin_amdgcn_s_setprio(0);
    gate();
  }
  {
    relane();
    u32x4 of[NS][2], o0[2];
    f32x4 acc[2][4];
#pragma unroll
    for (int t = 0; t < 2; ++t)
#pragma unroll
      for (int c = 0; c < 4; ++c) acc[t][c] = z4;
    tie8<1>(acc);
    unsigned wb[4], wx[4];
    {
      const int row = 4 * g + tq, f = ((row >> 1) & 1) | ((((row >> 2) ^ (row >> 3)) & 1) << 2);
#pragma unroll
      for (int nb = 0; nb < 4; ++nb) wb[nb] = lbase + s * 4096 + row * 128 + ((((tp & 2) + (nb & 1) + 4 * (nb >> 1)) ^ f) * 16) + (tp & 1) * 8;
    }
    const unsigned v0 = l * 16u + s * 3072u, v1 = l * 16u + s * 1024u + 1024u, v3 = l * 16u + 2048u - s * 1024u;
    auto oadr = [&](int h) { return ob + ptile(h) * 4096; };
    s16x4 wl[5], wh[5], el[2][4], eh[2][4];
    auto wrd = [&](auto K, auto HI) {
      constexpr int k = decltype(K)::value, h = k >> 2, nb = k & 3, o = (h & 7) * 8192;
      if constexpr (decltype(HI)::value) ds_tr<o + 2048>(wh[k % 5], wb[nb]);
      else {
        if constexpr (h == 8) { wb[nb] += 65536u; asm volatile("" : "+v"(wb[nb])); }
        ds_tr<o>(wl[k % 5], wb[nb]);
      }
    };
    unsigned xa = lbase + lw * 8192 + s * 4096 + l * 16, fa = LDS_OFF(flg + F_X + s * 4 + lw), fg = LDS_OFF(flg + F_PG + s * 4);
    f32x2 xf[8], gf[8];
    char* op = (char*)(out + (size_t)(panel * 128 + lw * 32 + s * 16) * DM + hp * 64);
    unsigned oo = (l & 7) * 2048u + (l & 8) * 8u + g * 16u, oa = lbase + L_EX + s * 9216 + lw * 2048 + l * 16;
    char* op2 = op + 16384;
    int idx;
    auto pre = [&]() {
#pragma unroll
      for (int i = 0; i < 8; ++i) {
        xf[i] = f32x2{bflo(xv[i >> 2][i & 3]), bfhi(xv[i >> 2][i & 3])}; gf[i] = f32x2{bflo(gq[i >> 2][i & 3]), bfhi(gq[i >> 2][i & 3])};
        asm volatile("" : "+v"(xf[i]), "+v"(gf[i]));
      }
      asm volatile("" : "+v"(oo), "+s"(op), "+s"(op2), "+v"(xa), "+v"(fa), "+v"(fg), "+v"(oa), "+v"(wb[0]), "+v"(wb[1]), "+v"(wb[2]), "+v"(wb[3]));
    };
    if (PW) pre();
    if (s && !BGO) lds_wait1(flg + F_GO + lw);
    else {
      lds_wait4(flg + F_OS);
      if (PD3) __builtin_amdgcn_s_sleep(PD3);
      if (!BGO && l == 0) __hip_atomic_store(flg + F_GO + lw, 1u, __ATOMIC_RELAXED, __HIP_MEMORY_SCOPE_WORKGROUP);
    }
    static_for<NS>([&](auto H) { opair(of[decltype(H)::value], v0, v1, oadr(decltype(H)::value + 1)); });
    lds_wait4(flg + F_WO);
    idx = 2047 - (int)(lds_get(flg + F_SEL) & 2047u);
    asm volatile("ds_read_b128 %0, %2\n\tds_read_b128 %1, %3" : "=&v"(o0[0]), "=&v"(o0[1]) : "v"(oa), "v"(oa ^ 1024u) : "memory");
    static_for<4>([&](auto NB) { wrd(NB, IC(0)); wrd(NB, IC(1)); });
    if (!PW) pre();
    u32x4 pg;
    static_for<14>([&](auto H) {
      constexpr int h = decltype(H)::value, x0 = h == XH || h == XH + 1, x1 = h == XH + 1 || h == XH + 2;
      u32x4 (&o)[2] = h ? of[(h + NS - 1) % NS] : o0;
      if constexpr (h == 1 && BP3) { if (s) __builtin_amdgcn_s_setprio(1); }
      static_for<4>([&](auto NB) {
        constexpr int nb = decltype(NB)::value, k = 4 * h + nb;
        constexpr bool ld = nb == 3 && h && h + NS < 16;
        const bf16x8 wf = cat8(wl[k % 5], wh[k % 5]);
        mfma_w<x1 + (3 - nb) * (2 + 2 * x1) + nb * (2 + 2 * x0) + (h == 5 || h == 13)>(acc[1][nb], wf, o[1]);
        wrd(IC(k + 4), IC(0));
        if constexpr (x0) ds_tr<(h - XH + 6) * 8192>(el[h - XH][nb], wx[nb]);
        if constexpr (ld) GL4(o[1], h + NS < 14 ? v1 : v3, oadr(h + NS), 0, P_SC1);
        mfma_t(acc[0][nb], wf, o[0]);
        wrd(IC(k + 4), IC(1));
        if constexpr (x0) ds_tr<(h - XH + 6) * 8192 + 2048>(eh[h - XH][nb], wx[nb]);
        if constexpr (ld) GL4(o[0], v0, oadr(h + NS), 0, P_SC1);
      });
      if constexpr (h == 4) asm volatile("ds_write_b32 %0, %0" :: "v"(fg + lw * 4u) : "memory");
      if constexpr (h == LM7) PAD(7);
      if constexpr (h == XH - 1) {
#pragma unroll
        for (int nb = 0; nb < 4; ++nb) { wx[nb] = wb[nb] ^ 4096u; asm volatile("" : "+v"(wx[nb])); }
      }
      if constexpr (h == 12) asm volatile("ds_read_b128 %0, %1" : "=v"(pg) : "v"(fg) : "memory");
      if constexpr (h < OC3) ocheck3<4>(of[h % 3], of[(h + 1) % 3], of[(h + 2) % 3], v0, v1, oadr(h + 1), oadr(h + 2), oadr(h + 3));
      else if constexpr (h < 13) ocheck<2 * ((h + NS < 15 ? h + NS : 15) - h - 1), true>(of[h % NS], v0, v1, oadr(h + 1));
    });
    asm volatile("s_waitcnt lgkmcnt(%1)" : "+v"(pg) : "n"(XH == 12 ? 15 : 8));
    if (__builtin_expect(!__builtin_amdgcn_readfirstlane(pg[0] & pg[1] & pg[2] & pg[3]), 0)) lds_wait4(flg + F_PG + s * 4);
    asm volatile("s_nop 1\n\tds_write_b128 %0, %1\n\tds_write_b128 %0, %2 offset:1024\n\tds_write_b128 %0, %3 offset:2048\n\tds_write_b128 %0, %4 offset:3072\n\tds_write_b32 %5, %5"
                 :: "v"(xa), "v"(acc[1][0]), "v"(acc[1][1]), "v"(acc[1][2]), "v"(acc[1][3]), "v"(fa) : "memory");
    __builtin_amdgcn_s_setprio(0);
    ocheck<2, true>(of[13 % NS], v0, v3, oadr(14));
    static_for<4>([&](auto NB) {
      constexpr int nb = decltype(NB)::value, k = 56 + nb;
      mfma_w<nb ? 15 : 5>(acc[0][nb], cat8(wl[k % 5], wh[k % 5]), of[13 % NS][0]);
      wrd(IC(k + 4), IC(0)); wrd(IC(k + 4), IC(1));
    });
#pragma unroll
    for (int nb = 0; nb < 4; ++nb) mfma_t(acc[0][nb], cat8(el[0][nb], eh[0][nb]), of[13 % NS][1]);
    unsigned fx;
    fa ^= 16u; xa ^= 4096u;
    asm volatile("ds_read_b32 %0, %5\n\tds_read_b128 %1, %6\n\tds_read_b128 %2, %6 offset:1024\n\tds_read_b128 %3, %6 offset:2048\n\tds_read_b128 %4, %6 offset:3072"
                 : "=&v"(fx), "=&v"(acc[1][0]), "=&v"(acc[1][1]), "=&v"(acc[1][2]), "=&v"(acc[1][3]) : "v"(fa), "v"(xa) : "memory");
    ocheck<0, true>(of[14 % NS], v0, v3, oadr(15));
#pragma unroll
    for (int nb = 0; nb < 4; ++nb) mfma_t(acc[0][nb], cat8(el[1][nb], eh[1][nb]), of[14 % NS][1]);
    static_for<4>([&](auto NB) { constexpr int nb = decltype(NB)::value, k = 60 + nb; mfma_w<5>(acc[0][nb], cat8(wl[k % 5], wh[k % 5]), of[14 % NS][0]); });
    if (__builtin_expect((idx >> 4) == panel * 8 + lw * 2 + s, GIN)) {
      lds_wait1(flg + F_GT);
      relane();
      if (r15 == (idx & 15)) {
        const u32x2* gi = (const u32x2*)(smem + L_GI + (g >> 1) * 32 + (g & 1) * 8);
#pragma unroll
        for (int i = 0; i < 4; ++i) {
          const u32x2 a = gi[(i & 1) * 2 + (i >> 1) * 8];
          gf[2 * i] = f32x2{bflo(a[0]), bfhi(a[0])}; gf[2 * i + 1] = f32x2{bflo(a[1]), bfhi(a[1])};
        }
      }
    }
    asm volatile("s_waitcnt lgkmcnt(0)" : "+v"(fx), "+v"(acc[1][0]), "+v"(acc[1][1]), "+v"(acc[1][2]), "+v"(acc[1][3]) :: "memory");
    if (__builtin_expect(__builtin_amdgcn_readfirstlane(fx) == 0u, 0))
#pragma unroll 1
    for (unsigned spins = 0; spins < SPIN && __builtin_amdgcn_readfirstlane(fx) == 0u; ++spins)
      asm volatile("s_sleep 1\n\tds_read_b32 %0, %5\n\tds_read_b128 %1, %6\n\tds_read_b128 %2, %6 offset:1024\n\tds_read_b128 %3, %6 offset:2048\n\t"
                   "ds_read_b128 %4, %6 offset:3072\n\ts_waitcnt lgkmcnt(0)"
                   : "+v"(fx), "+v"(acc[1][0]), "+v"(acc[1][1]), "+v"(acc[1][2]), "+v"(acc[1][3]) : "v"(fa), "v"(xa) : "memory");
#pragma unroll
    for (int i = 0; i < 8; ++i) xf[i] = __builtin_elementwise_fma(f32x2{acc[1][i >> 1][2 * (i & 1)], acc[1][i >> 1][2 * (i & 1) + 1]}, gf[i], xf[i]);
    asm volatile("" : "+v"(xf[0]), "+v"(xf[1]), "+v"(xf[2]), "+v"(xf[3]), "+v"(xf[4]), "+v"(xf[5]), "+v"(xf[6]), "+v"(xf[7]),
                 "+v"(acc[0][0]), "+v"(acc[0][1]), "+v"(acc[0][2]), "+v"(acc[0][3]));
    unsigned rr[8];
#pragma unroll
    for (int i = 0; i < 8; ++i) {
      const f32x2 r = __builtin_elementwise_fma(f32x2{acc[0][i >> 1][2 * (i & 1)], acc[0][i >> 1][2 * (i & 1) + 1]}, gf[i], xf[i]);
      rr[i] = pk2(r[0], r[1]);
    }
    {
      sw16u(rr[0], rr[2]); sw16u(rr[1], rr[3]); sw16u(rr[4], rr[6]); sw16u(rr[5], rr[7]);
      unsigned a0, a1, a2, a3, c0, c1, c2, c3;
#define CD(d, x, y) "v_cndmask_b32_dpp " d ", " x ", " y ", vcc row_ror:8 row_mask:0xf bank_mask:0xf\n\t"
      asm volatile("s_mov_b32 vcc_lo, 0x00ff00ff\n\ts_mov_b32 vcc_hi, 0x00ff00ff\n\t" CD("%0", "%12", "%8") CD("%2", "%14", "%10") CD("%1", "%13", "%9") CD("%3", "%15", "%11")
                   "s_not_b64 vcc, vcc\n\t" CD("%4", "%8", "%12") CD("%5", "%9", "%13") CD("%6", "%10", "%14") CD("%7", "%11", "%15")
                   : "=&v"(a0), "=&v"(a1), "=&v"(a2), "=&v"(a3), "=&v"(c0), "=&v"(c1), "=&v"(c2), "=&v"(c3)
                   : "v"(rr[0]), "v"(rr[1]), "v"(rr[2]), "v"(rr[3]), "v"(rr[4]), "v"(rr[5]), "v"(rr[6]), "v"(rr[7]) : "vcc", "scc");
      const u32x4 a = {a0, a1, a2, a3}, c = {c0, c1, c2, c3};
      asm volatile("global_store_dwordx4 %0, %1, %3 sc1\n.Lkf_last:\n\tglobal_store_dwordx4 %0, %2, %4 sc1" :: "v"(oo), "v"(a), "v"(c), "s"(op), "s"(op2) : "memory");
    }
    if (s) {
    } else {
    }
  }
}

extern "C" void kernel_launch(void* const* d_in, const int* in_sizes, int n_in,
                              void* d_out, int out_size, void* d_ws, size_t ws_size,
                              hipStream_t stream) {
  (void)hipFuncSetAttribute((const void*)kf_main, hipFuncAttributeMaxDynamicSharedMemorySize, LDSM);
  kf_main<<<256, 768, LDSM, stream>>>((const u16*)d_in[0], (const u16*)d_in[1], (char*)d_ws + WSO, (const u16*)d_in[5], (const u16*)d_in[2],
                                      (const u16*)d_in[3], (const u16*)d_in[6], (const u16*)d_in[7], (const u16*)d_in[8], (u16*)d_out);
}
```

```cpp
#include <hip/hip_runtime.h>
#include <stdint.h>
#include <utility>

typedef __attribute__((ext_vector_type(8))) short bf16x8;
typedef __attribute__((ext_vector_type(4))) short s16x4;
typedef __attribute__((ext_vector_type(4))) float f32x4;
typedef __attribute__((ext_vector_type(2))) float f32x2;
typedef __attribute__((ext_vector_type(4))) unsigned u32x4;
typedef __attribute__((ext_vector_type(2))) unsigned u32x2;
typedef __attribute__((ext_vector_type(2))) __bf16 bfx2;
typedef unsigned short u16;

#define DI __device__ __forceinline__
#define MFMA(a, b, c) __builtin_amdgcn_mfma_f32_16x16x32_bf16((a), (b), (c), 0, 0, 0)
#define SB() __builtin_amdgcn_sched_barrier(0)
#define IC(x) std::integral_constant<int, (x)>{}

DI unsigned pk2(float a, float b) { f32x2 v = {a, b}; return __builtin_bit_cast(unsigned, __builtin_convertvector(v, bfx2)); }
DI float bflo(unsigned u) { return __uint_as_float(u << 16); }
DI float bfhi(unsigned u) { return __uint_as_float(u & 0xffff0000u); }
DI float rbf(float x) { return (float)(__bf16)x; }
DI f32x2 rb2(f32x2 v) { return f32x2{__uint_as_float(pk2(0.f, v[0])), __uint_as_float(pk2(0.f, v[1]))}; }
DI bf16x8 cat8(s16x4 lo, s16x4 hi) { return __builtin_shufflevector(lo, hi, 0, 1, 2, 3, 4, 5, 6, 7); }
DI bf16x8 as8(u32x4 v) { return __builtin_bit_cast(bf16x8, v); }
template <int... I, class F> DI void static_for_impl(std::integer_sequence<int, I...>, F&& f) { (f(std::integral_constant<int, I>{}), ...); }
template <int N, class F> DI void static_for(F&& f) { static_for_impl(std::make_integer_sequence<int, N>{}, f); }
template <int N> DI void waitv() { asm volatile("s_waitcnt vmcnt(%0)" ::"n"(N) : "memory"); }
#define RAW_BARRIER() do { asm volatile("s_waitcnt lgkmcnt(0)" ::: "memory"); __builtin_amdgcn_s_barrier(); asm volatile("" ::: "memory"); } while (0)
#define TICK() do { SB(); RAW_BARRIER(); SB(); } while (0)
#define LAY 1
#define GAP(n) asm volatile("s_branch .Lkg" #n "\n.Lkh" #n ":\n\t.fill ((.Lkg" #n " - .Lkt" #n " - (.Lkh" #n " - kf_main)) & 4095) / 4, 4, 0xbf800000\n.Lkg" #n ":" ::: "memory")
#define MARK(n) asm volatile(".Lkt" #n ":" ::: "memory")
#define PAD(n) do { unsigned d_; asm volatile(".Lkt" #n ":\n\ts_mov_b32 %0, 0xbe801d6a" : "=s"(d_)); } while (0)
#define VISIT(n) do { const char* p_; int o_; asm volatile("s_getpc_b64 %0\n.Lkv" #n ":\n\ts_mov_b32 %1, .Lkt" #n " - .Lkv" #n " + 4" : "=s"(p_), "=s"(o_)); \
                      asm volatile("s_swappc_b64 vcc, %0" :: "s"(p_ + o_) : "vcc"); } while (0)
#define DS_B128(dst, addr, off) asm volatile("ds_read_b128 %0, %1 offset:%2" : "=v"(dst) : "v"(addr), "n"(off))
#define DS_TR(dst, addr, off) asm volatile("ds_read_b64_tr_b16 %0, %1 offset:%2" : "=v"(dst) : "v"(addr), "n"(off))
#define LDS_OFF(p) ((unsigned)(uintptr_t)(__attribute__((address_space(3))) const char*)(p))
enum { P_DEF = 0, P_SC1 = 1, P_NT = 2 };
template <int OFF, int POL> DI void gl4(u32x4& d, unsigned voff, const char* sb) {
  if constexpr (POL == P_SC1) asm volatile("global_load_dwordx4 %0, %1, %2 offset:%3 sc1" : "=v"(d) : "v"(voff), "s"(sb), "n"(OFF));
  else if constexpr (POL == P_NT) asm volatile("global_load_dwordx4 %0, %1, %2 offset:%3 nt" : "=v"(d) : "v"(voff), "s"(sb), "n"(OFF));
  else asm volatile("global_load_dwordx4 %0, %1, %2 offset:%3" : "=v"(d) : "v"(voff), "s"(sb), "n"(OFF));
}
#define GL4(dst, voff, sb, off, pol) gl4<off, pol>(dst, voff, sb)
#define GL2(dst, voff, sb, off) asm volatile("global_load_dwordx2 %0, %1, %2 offset:%3" : "=v"(dst) : "v"(voff), "s"(sb), "n"(off))
#define GL1(dst, voff, sb, off) asm volatile("global_load_dword %0, %1, %2 offset:%3 sc1" : "=v"(dst) : "v"(voff), "s"(sb), "n"(off))
DI void bl4nt(u32x4& d, unsigned voff, u32x4 rs, unsigned soff) {
  asm volatile("buffer_load_dwordx4 %0, %1, %2, %3 offen nt" : "=v"(d) : "v"(voff), "s"(rs), "s"(soff));
}
DI void mfma_t(f32x4& c, bf16x8 a, u32x4 bb) { asm volatile("v_mfma_f32_16x16x32_bf16 %0, %1, %2, %0" : "+v"(c) : "v"(a), "v"(bb)); }
template <int N> DI void mfma_w(f32x4& c, bf16x8 a, u32x4 bb) {
  asm volatile("s_waitcnt lgkmcnt(%3)\n\tv_mfma_f32_16x16x32_bf16 %0, %1, %2, %0" : "+v"(c) : "v"(a), "v"(bb), "n"(N));
}
template <int N> DI void tie8(f32x4 (&a)[2][4]) {
  asm volatile("s_nop %8" : "+v"(a[0][0]), "+v"(a[0][1]), "+v"(a[0][2]), "+v"(a[0][3]), "+v"(a[1][0]), "+v"(a[1][1]), "+v"(a[1][2]), "+v"(a[1][3]) : "n"(N));
}
template <int OFF> DI void ds_tr(s16x4& d, unsigned a) { DS_TR(d, a, OFF); }
template <int N> DI void waitv4(u32x4 (&v)[4]) { asm volatile("s_waitcnt vmcnt(%4)" : "+v"(v[0]), "+v"(v[1]), "+v"(v[2]), "+v"(v[3]) : "n"(N)); }
template <int N> DI void waitl8(s16x4 (&a)[4], s16x4 (&c)[4]) {
  asm volatile("s_waitcnt lgkmcnt(%8)" : "+v"(a[0]), "+v"(c[0]), "+v"(a[1]), "+v"(c[1]), "+v"(a[2]), "+v"(c[2]), "+v"(a[3]), "+v"(c[3]) : "n"(N));
}
DI void gs4(unsigned voff, const char* sb, u32x4 v) { asm volatile("global_store_dwordx4 %0, %1, %2 sc1\n\ts_nop 1" :: "v"(voff), "v"(v), "s"(sb) : "memory"); }
DI void gs1(unsigned voff, const char* sb, unsigned v) { asm volatile("global_store_dword %0, %1, %2 sc1" :: "v"(voff), "v"(v), "s"(sb) : "memory"); }
DI void glds(unsigned voff, const char* sbase, unsigned lds) {
  asm volatile("s_mov_b32 m0, %2\n\ts_nop 0\n\tglobal_load_lds_dwordx4 %0, %1" :: "v"(voff), "s"(sbase), "s"(lds) : "memory");
}
DI void touch(unsigned voff, const char* sbase, unsigned lds) {
  asm volatile("s_mov_b32 m0, %2\n\ts_nop 0\n\tglobal_load_lds_dword %0, %1" :: "v"(voff), "s"(sbase), "s"(lds) : "memory");
}
DI void touchn(unsigned voff, const char* sbase, unsigned lds) {
  asm volatile("s_mov_b32 m0, %2\n\ts_nop 0\n\tglobal_load_lds_dword %0, %1 nt" :: "v"(voff), "s"(sbase), "s"(lds) : "memory");
}
DI void touch4(unsigned voff, const char* sbase, unsigned lds) {
  asm volatile("s_mov_b64 exec, 15\n\ts_mov_b32 m0, %2\n\ts_nop 0\n\tglobal_load_lds_dword %0, %1\n\ts_mov_b64 exec, -1" :: "v"(voff), "s"(sbase), "s"(lds) : "memory");
}
DI void pub_pin(u32x4& a, u32x2& b, unsigned& c, unsigned& d) { asm volatile("" : "+v"(a), "+v"(b), "+v"(c), "+v"(d)); }
DI void pub(u32x4 v, u32x2 w, unsigned o, unsigned oc, unsigned long long m, unsigned long long mc, const char* sb) {
  asm volatile("s_mov_b64 exec, %4\n\tglobal_store_dwordx2 %2, %1, %6 sc1\n\ts_mov_b64 exec, %5\n\tglobal_store_dwordx4 %3, %0, %6 sc1\n\ts_mov_b64 exec, -1\n\ts_nop 0"
               :: "v"(v), "v"(w), "v"(o), "v"(oc), "s"(m), "s"(mc), "s"(sb) : "memory");
}
DI float silu_f(float x) { return x / (1.0f + __expf(-x)); }
DI float silu_r(float x) { return x * __builtin_amdgcn_rcpf(1.0f + __expf(-x)); }
DI float dot2(unsigned a, unsigned b, float c) { return __builtin_amdgcn_fdot2_f32_bf16(__builtin_bit_cast(bfx2, a), __builtin_bit_cast(bfx2, b), c, false); }

template <int OFF> DI void gs4o(unsigned voff, const char* sb, u32x4 v) {
  asm volatile("global_store_dwordx4 %0, %1, %2 offset:%3 sc1\n\ts_nop 1" :: "v"(voff), "v"(v), "s"(sb), "n"(OFF) : "memory");
}
DI void gs4m(unsigned voff, const char* sb, u32x4 v) {
  asm volatile("s_mov_b64 exec, 0xff\n\tglobal_store_dwordx4 %0, %1, %2 sc1\n\ts_mov_b64 exec, -1\n\ts_nop 0" :: "v"(voff), "v"(v), "s"(sb) : "memory");
}
DI void gs2(unsigned voff, const char* sb, u32x2 v) { asm volatile("global_store_dwordx2 %0, %1, %2 sc1" :: "v"(voff), "v"(v), "s"(sb) : "memory"); }
#define GL1P(dst, voff, sb) asm volatile("global_load_dword %0, %1, %2" : "=v"(dst) : "v"(voff), "s"(sb))
DI u32x4 ldsr1(unsigned a) { u32x4 v; asm volatile("ds_read_b128 %0, %1\n\ts_waitcnt lgkmcnt(0)" : "=v"(v) : "v"(a) : "memory"); return v; }
DI void ldsw1(unsigned a, u32x4 v) { asm volatile("ds_write_b128 %0, %1" :: "v"(a), "v"(v) : "memory"); }
DI unsigned nosent(unsigned v) { return min(v, 0xfffffffeu); }

constexpr int DM = 1024, FF = 3584;
constexpr unsigned WS_ST = 4096;
constexpr unsigned WS_CA = WS_ST + 2048 * 8;
constexpr unsigned WS_CS = WS_CA + 256 * 16;
constexpr unsigned WS_KVP = WS_CS + 256 * 1024;
constexpr unsigned WS_ZP = WS_KVP + 2048 * 1024;
constexpr unsigned WS_KVR = WS_ZP + 1024 * 128;
constexpr unsigned WS_O = WS_KVR + 16 * 8192;
constexpr unsigned WS_GU = WS_O + 4096 * 1024;
constexpr unsigned WS_Y = WS_GU + 56 * 4096;

constexpr int ABYTES = 16384, WSLAB = 8192, SLOT = 40960;
constexpr int L_EW = 3 * SLOT;
constexpr int L_EX = L_EW + 2 * WSLAB;
constexpr int L_REC = L_EW;
constexpr int L_Z = L_EX + ABYTES;
constexpr int L_WP = L_Z + 256;
constexpr int L_MN = L_WP + 4096;
constexpr int L_RS = L_MN + 512;
constexpr int L_DN = L_RS + 512;
constexpr int L_C = L_DN + 256;
constexpr int L_H = L_C + 768;
constexpr int L_GI = L_H + 1024;
constexpr int L_F = L_GI + 128;
constexpr int L_DU = L_F + 320;
constexpr int LDSM = L_DU + 256;
constexpr int F_KV = 0, F_WO = 4, F_PG = 8, F_X = 16, F_T = 24, F_GT = 25, F_SEL = 28, F_BL = 32, F_IM = 36, F_EX = 40, F_ST = 44, F_SD = 48, F_CA = 52, F_OS = 60, F_GO = 64, F_AL = 68, F_SL = 72, F_LD = 76;
#define SPINV (1u << 20)
constexpr unsigned SPIN = SPINV;
DI void pin1(float& v) { asm volatile("" : "+v"(v)); }
DI void pinf(f32x4& v) { asm volatile("" : "+v"(v)); }
DI void pin2(u32x4 (&v)[2]) { asm volatile("" : "+v"(v[0]), "+v"(v[1])); }
constexpr float L2E = 1.4426950408889634f;

template <int CTRL, int RMASK> DI float dpp_add(float v) {
  return v + __builtin_bit_cast(float, __builtin_amdgcn_update_dpp(0, __builtin_bit_cast(int, v), CTRL, RMASK, 0xf, true));
}
DI float wave_total(float v) {
  v = dpp_add<0x111, 0xf>(v); v = dpp_add<0x112, 0xf>(v); v = dpp_add<0x114, 0xf>(v); v = dpp_add<0x118, 0xf>(v);
  v = dpp_add<0x142, 0xa>(v); v = dpp_add<0x143, 0xc>(v);
  return __builtin_bit_cast(float, __builtin_amdgcn_readlane(__builtin_bit_cast(int, v), 63));
}
DI f32x2 sw32(float a, float b) {
  auto r = __builtin_amdgcn_permlane32_swap(__float_as_uint(a), __float_as_uint(b), false, false);
  return f32x2{__uint_as_float(r[0]), __uint_as_float(r[1])};
}
DI f32x2 sw16(float a, float b) {
  auto r = __builtin_amdgcn_permlane16_swap(__float_as_uint(a), __float_as_uint(b), false, false);
  return f32x2{__uint_as_float(r[0]), __uint_as_float(r[1])};
}
DI float ror8(float v) { return __builtin_bit_cast(float, __builtin_amdgcn_update_dpp(0, __builtin_bit_cast(int, v), 0x128, 0xf, 0xf, true)); }
DI float sum_g(float v) {
  f32x2 a = sw32(v, v); v = a[0] + a[1];
  a = sw16(v, v); return a[0] + a[1];
}
DI unsigned tag12(float v, unsigned t) { return ((__float_as_uint(v) + 0x800u) & 0xfffff000u) | t; }
DI float untag12(unsigned u) { return __uint_as_float(u & 0xfffff000u); }
DI unsigned bad4(u32x4 v, unsigned t) { return (v[0] ^ t) | (v[1] ^ t) | (v[2] ^ t) | (v[3] ^ t); }
DI bool all0(unsigned v) { return __ballot(v != 0u) == 0ull; }
DI void lds_set(unsigned* f, unsigned v, int l) {
  asm volatile("s_waitcnt lgkmcnt(0)" ::: "memory");
  if (l == 0) __hip_atomic_store(f, v, __ATOMIC_RELAXED, __HIP_MEMORY_SCOPE_WORKGROUP);
}
DI unsigned lds_get(unsigned* f) { return __builtin_amdgcn_readfirstlane(__hip_atomic_load(f, __ATOMIC_RELAXED, __HIP_MEMORY_SCOPE_WORKGROUP)); }
DI void lds_wait4(unsigned* f) {
#pragma unroll 1
  for (unsigned spins = 0; spins < SPIN; ++spins) {
    u32x4 v; asm volatile("ds_read_b128 %0, %1\n\ts_waitcnt lgkmcnt(0)" : "=v"(v) : "v"(LDS_OFF(f)) : "memory");
    if (__builtin_amdgcn_readfirstlane(v[0] & v[1] & v[2] & v[3])) break;
    __builtin_amdgcn_s_sleep(1);
  }
}

template <int NC> struct Frag {
  bf16x8 af[4]; s16x4 wl[NC], wh[NC];
  DI void wait() {
    if constexpr (NC == 4)
      asm volatile("s_waitcnt lgkmcnt(0)" : "+v"(af[0]), "+v"(af[1]), "+v"(af[2]), "+v"(af[3]), "+v"(wl[0]), "+v"(wh[0]), "+v"(wl[1]), "+v"(wh[1]),
                   "+v"(wl[2]), "+v"(wh[2]), "+v"(wl[3]), "+v"(wh[3]));
    else
      asm volatile("s_waitcnt lgkmcnt(0)" : "+v"(af[0]), "+v"(af[1]), "+v"(af[2]), "+v"(af[3]), "+v"(wl[0]), "+v"(wh[0]), "+v"(wl[1]), "+v"(wh[1]));
  }
};
template <int OFF> DI void ds_32(unsigned& d, unsigned a) { asm volatile("ds_read_b32 %0, %1 offset:%2" : "=v"(d) : "v"(a), "n"(OFF) : "memory"); }
template <int OFF, class V> DI void ds_128v(V& d, unsigned a) { DS_B128(d, a, OFF); }
template <int O> DI void ds_2x32(f32x2& d, unsigned a) { asm volatile("ds_read2_b32 %0, %1 offset0:%2 offset1:%3" : "=v"(d) : "v"(a), "n"(O), "n"(O + 128) : "memory"); }
template <int F> DI void raise(unsigned a) { asm volatile("ds_write_b32 %0, %0 offset:%1" :: "v"(a), "n"(F * 4) : "memory"); }
template <int OFF> DI void ds_w64(unsigned a, u32x2 v) { asm volatile("ds_write_b64 %0, %1 offset:%2" :: "v"(a), "v"(v), "n"(OFF) : "memory"); }
template <int T> DI void ds_if(u32x4 (&v)[2], unsigned a, int t) {
  asm volatile("s_cmp_eq_u32 %4, %5\n\ts_cselect_b64 exec, -1, 0\n\tds_read_b128 %0, %2\n\tds_read_b128 %1, %3\n\ts_mov_b64 exec, -1"
               : "+v"(v[0]), "+v"(v[1]) : "v"(a), "v"(a ^ 64u), "s"(t), "n"(T) : "scc", "memory");
}
template <int OFF> DI void ds_128(bf16x8& d, unsigned a) { DS_B128(d, a, OFF); }
DI void lds_wait2(unsigned* f) {
#pragma unroll 1
  for (unsigned spins = 0; spins < SPIN; ++spins) {
    u32x2 v; asm volatile("ds_read_b64 %0, %1\n\ts_waitcnt lgkmcnt(0)" : "=v"(v) : "v"(LDS_OFF(f)) : "memory");
    if (__builtin_amdgcn_readfirstlane(v[0] & v[1])) break;
    __builtin_amdgcn_s_sleep(1);
  }
}
DI void reload2(u32x4 (&p)[2], unsigned o, const char* sb, unsigned long long m0, unsigned long long m1) {
  asm volatile("s_mov_b64 exec, %4\n\tglobal_load_dwordx4 %0, %2, %3 sc1\n\ts_mov_b64 exec, %5\n\tglobal_load_dwordx4 %1, %2, %3 offset:1024 sc1\n\t"
               "s_mov_b64 exec, -1\n\ts_waitcnt vmcnt(0)"
               : "+v"(p[0]), "+v"(p[1]) : "v"(o), "s"(sb), "s"(m0), "s"(m1) : "memory");
}

template <int CTRL> DI unsigned dpp_umax(unsigned v) { return max(v, (unsigned)__builtin_amdgcn_update_dpp((int)v, (int)v, CTRL, 0xf, 0xf, false)); }
template <int CTRL, int RMASK> DI unsigned dpp_umax_r(unsigned v) { return max(v, (unsigned)__builtin_amdgcn_update_dpp((int)v, (int)v, CTRL, RMASK, 0xf, false)); }
DI unsigned now() { return (unsigned)__builtin_amdgcn_s_memrealtime(); }
DI void lds_wait1(unsigned* f) {
#pragma unroll 1
  for (unsigned spins = 0; spins < SPIN && lds_get(f) == 0u; ++spins) __builtin_amdgcn_s_sleep(1);
  asm volatile("" ::: "memory");
}

DI void cs_read(u32x2 (&c)[4], unsigned a) {
  asm volatile("ds_read_b64_tr_b16 %0, %4\n\tds_read_b64_tr_b16 %1, %4 offset:512\n\tds_read_b64_tr_b16 %2, %4 offset:4096\n\tds_read_b64_tr_b16 %3, %4 offset:4608"
               : "=&v"(c[0]), "=&v"(c[1]), "=&v"(c[2]), "=&v"(c[3]) : "v"(a) : "memory");
}
DI void cs_sum(u32x2 (&c)[4], float& s0, float& s1) {
  asm volatile("s_waitcnt lgkmcnt(0)" : "+v"(c[0]), "+v"(c[1]), "+v"(c[2]), "+v"(c[3]) :: "memory");
#pragma unroll
  for (int i = 0; i < 4; ++i) { s0 = dot2(c[i][0], 0x3f803f80u, s0); s1 = dot2(c[i][1], 0x3f803f80u, s1); }
  asm volatile("" : "+v"(s0), "+v"(s1));
}
#define WIN 0
#define NG 4
#define NH 12
#define PE 7
#define PP1 12
#define PD1 12
#define PD3 6
#define PPY 12
#define FP0 1
#define FPR 2
#define FPE 0
#define FPG 3
#define GAS 10
#define SB0 2
#define SBI 3
#define SBP 6
#define CTS 6
#define ES0 7
#define ESL 5
#define NF 4
#define VPT 2
#define WX 0
#define WE 0
#define AP 2
#define BP 1
#define BQ 0
#define WNO 16
#define D2 40
#define GB 1
#define PYD 16
#define PRE 0
#define GL 0
#define CT2 0
#define GIN 0
#define TP3 1
#define TPW 1
#define LM7 2
#define RM 0
#define XH 11
#define PW 1
#define BGO 1
#define BP3 1
#define FIN 0
#define FI 0
#define DL 0
#define CAE 0
#define TD 0
#define TDP 0
#define QW 2
#define ORD 0
#define OC3 1
#define CP 1
#define WSO 0
#define SQ 1
#define SR 1
#define SK 1
#define HB 1
#define OW1 1
#define KA 0
#define OSI 0
#define OSF "sc1"
#define NS 3
constexpr int PUB = SBP > SB0 + SK ? SBP : SB0 + SK;
constexpr int n_e(int S) { int n = 0; for (int i = 0; i < 8; ++i) n += ES0 + i * ESL / 8 == S; return n; }
constexpr int n_x(int S) { return (S == PUB) + (S == 0) + (S == GAS ? 2 : 0) + (S == CTS) + n_e(S) + (S == 11 ? FI + CAE : 0); }
static_assert((NS == 3 || !OC3) && SBP > SBI && PUB >= 3 && PUB < GAS && GAS <= 10 && (CTS <= 10 || CTS == 12) && ES0 > SB0 && ES0 + ESL <= 12 && FI + NF + NG + NH <= 24 && (!DL || FI + NF + NG + NH <= 16), "");
DI void wphi_frag(bf16x8& w0, bf16x8& w1, unsigned a) {
  s16x4 t0, t1, t2, t3;
  asm volatile("ds_read_b64_tr_b16 %0, %4\n\tds_read_b64_tr_b16 %1, %4 offset:1024\n\tds_read_b64_tr_b16 %2, %4 offset:32\n\t"
               "ds_read_b64_tr_b16 %3, %4 offset:1056\n\ts_waitcnt lgkmcnt(0)" : "=&v"(t0), "=&v"(t1), "=&v"(t2), "=&v"(t3) : "v"(a) : "memory");
  w0 = cat8(t0, t1); w1 = cat8(t2, t3);
}
DI float mx2(float a, float b) { return __builtin_elementwise_maximum(a, b); }
DI float mx3(float a, float b, float c) { return __builtin_elementwise_maximum(__builtin_elementwise_maximum(a, b), c); }
DI void bcast2(float m, f32x2& c0, f32x2& c1) {
  const f32x2 a = sw16(m, m);
  c0 = sw32(a[0], a[0]); c1 = sw32(a[1], a[1]);
}
template <bool NRM, class F> DI void phi_map(const f32x4 (&p)[4][2], const float (&sc)[4], const float (&nrm)[2], F&& sink) {
  float r[4];
  f32x2 c[4];
#pragma unroll
  for (int t = 0; t < 4; ++t) {
    const float mx = mx2(mx3(mx3(p[t][0][0], p[t][0][1], p[t][0][2]), p[t][0][3], p[t][1][0]), mx3(p[t][1][1], p[t][1][2], p[t][1][3]));
    const float nm = mx2(mx3(mx3(-p[t][0][0], -p[t][0][1], -p[t][0][2]), -p[t][0][3], -p[t][1][0]), mx3(-p[t][1][1], -p[t][1][2], -p[t][1][3]));
    const f32x2 a = sw32(mx, nm);
    r[t] = mx2(a[0], a[1]);
  }
#pragma unroll
  for (int q = 0; q < 2; ++q) {
    const f32x2 a = sw16(r[2 * q], r[2 * q + 1]);
    bcast2(mx2(a[0], a[1]), c[2 * q], c[2 * q + 1]);
  }
#pragma unroll
  for (int t = 0; t < 4; ++t) c[t] *= -sc[t];
#pragma unroll
  for (int q = 0; q < 2; ++q) {
    f32x2 e[2][8], sm[2];
#pragma unroll
    for (int i = 0; i < 8; ++i)
#pragma unroll
      for (int j = 0; j < 2; ++j) {
        const float v = p[2 * q + j][i >> 2][i & 3];
        const f32x2 a = __builtin_elementwise_fma(f32x2{v, v}, f32x2{sc[2 * q + j], -sc[2 * q + j]}, c[2 * q + j]);
        e[j][i] = f32x2{__builtin_amdgcn_exp2f(a[0]), __builtin_amdgcn_exp2f(a[1])};
      }
    if constexpr (NRM) {
#pragma unroll
    for (int j = 0; j < 2; ++j) {
      sm[j] = ((e[j][0] + e[j][1]) + (e[j][2] + e[j][3])) + ((e[j][4] + e[j][5]) + (e[j][6] + e[j][7]));
      sm[j] = sw32(sm[j][0], sm[j][1]);
    }
    const f32x2 a = sw16(sm[0][0] + sm[0][1], sm[1][0] + sm[1][1]);
    float rc = __builtin_amdgcn_rcpf(a[0] + a[1]);
    rc *= nrm[q];
    bcast2(rc, sm[0], sm[1]);
    }
#pragma unroll
    for (int j = 0; j < 2; ++j) {
      unsigned pe[8];
#pragma unroll
      for (int i = 0; i < 8; ++i) if (NRM) e[j][i] *= sm[j];
#pragma unroll
      for (int i = 0; i < 4; ++i) { pe[i] = pk2(e[j][2 * i][0], e[j][2 * i + 1][0]); pe[4 + i] = pk2(e[j][2 * i][1], e[j][2 * i + 1][1]); }
      sink(2 * q + j, pe);
    }
  }
}

struct Sel { unsigned e[4], o[4]; };
DI Sel make_sel(int l) {
  Sel s;
#pragma unroll
  for (int q = 0; q < 4; ++q) {
    const bool on = ((l & 7) >> 1) == q;
    s.e[q] = on ? ((l & 1) ? 0x01000c0cu : 0x0c0c0100u) : 0x0c0c0c0cu;
    s.o[q] = on ? ((l & 1) ? 0x03020c0cu : 0x0c0c0302u) : 0x0c0c0c0cu;
  }
  return s;
}
DI f32x4 gemv2(unsigned xp, u32x4 w0, u32x4 w1, const Sel& s, f32x4 d) {
  const u32x4 a0 = {__builtin_amdgcn_perm(xp, xp, s.e[0]), __builtin_amdgcn_perm(xp, xp, s.e[1]), __builtin_amdgcn_perm(xp, xp, s.e[2]), __builtin_amdgcn_perm(xp, xp, s.e[3])};
  const u32x4 a1 = {__builtin_amdgcn_perm(xp, xp, s.o[0]), __builtin_amdgcn_perm(xp, xp, s.o[1]), __builtin_amdgcn_perm(xp, xp, s.o[2]), __builtin_amdgcn_perm(xp, xp, s.o[3])};
  return MFMA(as8(a1), as8(w1), MFMA(as8(a0), as8(w0), d));
}
DI u32x4 gemv_fin(f32x4 d, unsigned t12) {
  u32x4 v;
#pragma unroll
  for (int e = 0; e < 4; ++e) { const float t = ror8(d[e]); v[e] = tag12(d[e] + sw32(t, t)[1], t12); }
  return v;
}

DI void opair(u32x4 (&o)[2], unsigned v0, unsigned v1, const char* p) { GL4(o[0], v0, p, 0, P_SC1); GL4(o[1], v1, p, 0, P_SC1); }
template <int N, bool COLD = false> DI void ocheck(u32x4 (&o)[2], unsigned v0, unsigned v1, const char* p) {
  asm volatile("s_waitcnt vmcnt(%2)" : "+v"(o[0]), "+v"(o[1]) : "n"(N));
  if (COLD && __builtin_expect(__ballot(max(o[0][3], o[1][3]) == ~0u) == 0ull, 1)) return;
#pragma unroll 1
  for (unsigned spins = 0; spins < SPIN && __ballot(max(o[0][3], o[1][3]) == ~0u) != 0ull; ++spins)
    asm volatile("s_sleep 2\n\tglobal_load_dwordx4 %0, %2, %4 sc1\n\tglobal_load_dwordx4 %1, %3, %4 sc1\n\ts_waitcnt vmcnt(0)"
                 : "+v"(o[0]), "+v"(o[1]) : "v"(v0), "v"(v1), "s"(p) : "memory");
}
template <int N> DI void ocheck3(u32x4 (&a)[2], u32x4 (&b)[2], u32x4 (&c)[2], unsigned v0, unsigned v1, const char* pa, const char* pb, const char* pc) {
  asm volatile("s_waitcnt vmcnt(%2)" : "+v"(a[0]), "+v"(a[1]) : "n"(N));
  if (__builtin_expect(__ballot(max(a[0][3], a[1][3]) == ~0u) == 0ull, 1)) return;
  asm volatile("s_waitcnt vmcnt(0)" : "+v"(b[0]), "+v"(b[1]), "+v"(c[0]), "+v"(c[1]));
#pragma unroll 1
  for (unsigned spins = 0; spins < SPIN; ++spins) {
    const unsigned long long ma = __ballot(max(a[0][3], a[1][3]) == ~0u), mb = __ballot(max(b[0][3], b[1][3]) == ~0u), mc = __ballot(max(c[0][3], c[1][3]) == ~0u);
    if (ma == 0ull) break;
    asm volatile("s_mov_b64 exec, %11\n\tglobal_load_dwordx4 %0, %6, %8 sc1\n\tglobal_load_dwordx4 %1, %7, %8 sc1\n\t"
                 "s_mov_b64 exec, %12\n\tglobal_load_dwordx4 %2, %6, %9 sc1\n\tglobal_load_dwordx4 %3, %7, %9 sc1\n\t"
                 "s_mov_b64 exec, %13\n\tglobal_load_dwordx4 %4, %6, %10 sc1\n\tglobal_load_dwordx4 %5, %7, %10 sc1\n\ts_mov_b64 exec, -1\n\ts_waitcnt vmcnt(0)"
                 : "+v"(a[0]), "+v"(a[1]), "+v"(b[0]), "+v"(b[1]), "+v"(c[0]), "+v"(c[1])
                 : "v"(v0), "v"(v1), "s"(pa), "s"(pb), "s"(pc), "s"(ma), "s"(mb), "s"(mc) : "memory");
  }
}
DI void sw16u(unsigned& a, unsigned& b) { auto r = __builtin_amdgcn_permlane16_swap(a, b, false, false); a = r[0]; b = r[1]; }
template <int N> DI void waitl2(s16x4& a, s16x4& c) { asm volatile("s_waitcnt lgkmcnt(%2)" : "+v"(a), "+v"(c) : "n"(N)); }
template <int N> DI void tie4(f32x4 (&a)[4]) { asm volatile("s_nop %4" : "+v"(a[0]), "+v"(a[1]), "+v"(a[2]), "+v"(a[3]) : "n"(N)); }
DI void rowst(f32x2 (&m)[4], unsigned a) {
  asm volatile("ds_read2_b32 %0, %4 offset1:128\n\tds_read2_b32 %1, %4 offset0:16 offset1:144\n\tds_read2_b32 %2, %4 offset0:32 offset1:160\n\t"
               "ds_read2_b32 %3, %4 offset0:48 offset1:176\n\ts_waitcnt lgkmcnt(0)" : "=&v"(m[0]), "=&v"(m[1]), "=&v"(m[2]), "=&v"(m[3]) : "v"(a) : "memory");
}
DI void reload4c(u32x4 (&p)[4], unsigned o, const char* sb, unsigned long long m0, unsigned long long m1, unsigned long long m2, unsigned long long m3) {
  asm volatile("s_mov_b64 exec, %6\n\tglobal_load_dwordx4 %0, %4, %5 sc1\n\ts_mov_b64 exec, %7\n\tglobal_load_dwordx4 %1, %4, %5 offset:1024 sc1\n\t"
               "s_mov_b64 exec, %8\n\tglobal_load_dwordx4 %2, %4, %5 offset:2048 sc1\n\ts_mov_b64 exec, %9\n\tglobal_load_dwordx4 %3, %4, %5 offset:3072 sc1\n\t"
               "s_mov_b64 exec, -1\n\ts_waitcnt vmcnt(0)"
               : "+v"(p[0]), "+v"(p[1]), "+v"(p[2]), "+v"(p[3]) : "v"(o), "s"(sb), "s"(m0), "s"(m1), "s"(m2), "s"(m3) : "memory");
}

#define PL(r, o) "global_load_dwordx4 v[" #r ":" #r "+3], %[vo], %[sb] offset:" #o " sc1\n\t"
#define PC(r, m) "v_cmp_eq_u32 " m ", -1, v[" #r "+3]\n\t"
#define PM(d, r) "v_mov_b32 v[" #d "], v[" #r "]\n\tv_mov_b32 v[" #d "+1], v[" #r "+1]\n\tv_mov_b32 v[" #d "+2], v[" #r "+2]\n\tv_mov_b32 v[" #d "+3], v[" #r "+3]\n\t"
template <int D> DI void poll2(u32x4 (&p)[2], u32x4 (&q)[4], unsigned vo, const char* sb) {
  unsigned long long m; unsigned n = SPIN;
  asm volatile(PL(128, 0) PL(132, 1024) "s_sleep %[d]\n\t" PL(136, 0) PL(140, 1024)
               "1:\n\ts_waitcnt vmcnt(2)\n\t" PC(128, "vcc") PC(132, "%[m]") "s_or_b64 exec, vcc, %[m]\n\ts_cbranch_scc0 2f\n\t" PL(128, 0) PL(132, 1024)
               "s_mov_b64 exec, -1\n\ts_waitcnt vmcnt(2)\n\t" PC(136, "vcc") PC(140, "%[m]") "s_or_b64 exec, vcc, %[m]\n\ts_cbranch_scc0 3f\n\t" PL(136, 0) PL(140, 1024)
               "s_mov_b64 exec, -1\n\ts_sub_u32 %[n], %[n], 1\n\ts_cbranch_scc0 1b\n\ts_waitcnt vmcnt(0)\n"
               "3:\n\ts_mov_b64 exec, -1\n\t" PM(120, 136) PM(124, 140) "s_branch 4f\n"
               "2:\n\ts_mov_b64 exec, -1\n\t" PM(120, 128) PM(124, 132) "4:\n\ts_nop 1"
               : "={v[120:123]}"(p[0]), "={v[124:127]}"(p[1]), "=&{v[128:131]}"(q[0]), "=&{v[132:135]}"(q[1]), "=&{v[136:139]}"(q[2]), "=&{v[140:143]}"(q[3]),
                 [m] "=&s"(m), [n] "+s"(n)
               : [vo] "v"(vo), [sb] "s"(sb), [d] "n"(D) : "vcc", "scc", "memory");
}
#define PL4(r) PL(r, 0) PL(r+4, 1024) PL(r+8, 2048) PL(r+12, 3072)
#define PM2(o, r) "v_mov_b64 v[128+" #o ":129+" #o "], v[" #r "+" #o ":" #r "+1+" #o "]\n\t"
#define PM16(r) PM2(0, r) PM2(2, r) PM2(8, r) PM2(10, r) PM2(4, r) PM2(6, r) PM2(12, r) PM2(14, r)
#define PQ8(c) c "{v[96:99]}"(q[0]), c "{v[100:103]}"(q[1]), c "{v[104:107]}"(q[2]), c "{v[108:111]}"(q[3]), c "{v[112:115]}"(q[4]), c "{v[116:119]}"(q[5]), c "{v[120:123]}"(q[6]), c "{v[124:127]}"(q[7])
#define PB(a, r) "v_bitop3_b32 " a ", " a ", v[" #r "], %[t] bitop3:0xf6\n\t"
#define PB7(a, r) "v_xor_b32 " a ", %[t], v[" #r "]\n\t" PB(a, r+1) PB(a, r+2) PB(a, r+3) PB(a, r+4) PB(a, r+5) PB(a, r+6) PB(a, r+7)
#define PCT(r) "s_waitcnt vmcnt(4)\n\t" PB7("v128", r) PB7("v129", r+8) "v_and_b32 v128, 0xfff, v128\n\tv_and_or_b32 v128, v129, %[tm], v128\n\tv_cmp_ne_u32 vcc, 0, v128\n\ts_or_b64 exec, vcc, vcc\n\t"
template <int D> DI void pollt(u32x4 (&p)[4], u32x4 (&q)[8], unsigned vo, const char* sb, unsigned t, unsigned tm) {
  unsigned n = SPIN;
  asm volatile(PL4(96) "s_sleep %[d]\n\t" PL4(112)
               "1:\n\t" PCT(96) "s_cbranch_scc0 2f\n\t" PL4(96) "s_mov_b64 exec, -1\n\t" PCT(112) "s_cbranch_scc0 3f\n\t" PL4(112)
               "s_mov_b64 exec, -1\n\ts_sub_u32 %[n], %[n], 1\n\ts_cbranch_scc0 1b\n\ts_waitcnt vmcnt(0)\n"
               "3:\n\ts_mov_b64 exec, -1\n\t" PM16(112) "s_branch 4f\n"
               "2:\n\ts_mov_b64 exec, -1\n\t" PM16(96) "4:\n\ts_nop 1"
               : "=&{v[128:131]}"(p[0]), "=&{v[132:135]}"(p[1]), "=&{v[136:139]}"(p[2]), "=&{v[140:143]}"(p[3]), PQ8("=&"), [n] "+s"(n)
               : [vo] "v"(vo), [sb] "s"(sb), [t] "s"(t), [tm] "s"(tm), [d] "n"(D) : "vcc", "scc", "memory");
}
#define PKEEP(pl) "v"(pl[0]), "v"(pl[1]), "v"(pl[2]), "v"(pl[3]), "v"(pl[4]), "v"(pl[5]), "v"(pl[6]), "v"(pl[7])
extern "C" __global__ __launch_bounds__(768) __attribute__((aligned(4096))) void kf_main(const u16* __restrict__ x, const u16* __restrict__ wqkv, char* ws, const u16* __restrict__ w_r,
                                               const u16* __restrict__ wphi, const u16* __restrict__ wo, const u16* __restrict__ w_gate,
                                               const u16* __restrict__ w_up, const u16* __restrict__ w_down, u16* __restrict__ out) {
  extern __shared__ __attribute__((aligned(16))) char smem[];
  const int b = blockIdx.x;
  const int xcd = b & 7, jj = b >> 3;
  const int panel = (xcd >> 2) * 8 + (jj & 7), hp = (xcd & 3) * 4 + (jj >> 3);
  const int tid = threadIdx.x;
  const int w = __builtin_amdgcn_readfirstlane(tid >> 6), lw = w & 3;
  int l = tid & 63, r15 = l & 15, g = l >> 4, tq = r15 >> 2, tp = l & 3;
  unsigned xo;
  auto relane = [&]() {
    asm volatile("v_mbcnt_lo_u32_b32 %0, -1, 0\n\tv_mbcnt_hi_u32_b32 %0, -1, %0" : "=v"(l));
    r15 = l & 15; g = l >> 4; tq = r15 >> 2; tp = l & 3; xo = r15 * 2048u + g * 16u;
  };
  const unsigned lbase = LDS_OFF(smem);
  if (KA) asm volatile("" :: "s"(x), "s"(wqkv), "s"(ws), "s"(w_r), "s"(wphi), "s"(wo), "s"(w_gate), "s"(w_up), "s"(w_down), "s"(out));
  auto pf3 = [&](int half) {
    touch(l * 6144u, (const char*)wqkv + (size_t)(half * 512 + (jj & 7) * 64) * 6144 + (jj >> 3) * 2048 + (xcd & 3) * 512 + 384, lbase + L_DU);
  };
  const char* pc0; unsigned clen;
  asm volatile("s_getpc_b64 %0\n.Lkf_here:\n\ts_mov_b32 %1, ((.Lkf_last-kf_main)|4095)-3-(.Lkf_here-kf_main)\n\ts_max_u32 %1, %1, ((.Lkt8-kf_main)|4095)-3-(.Lkf_here-kf_main)"
               : "=s"(pc0), "=s"(clen) :: "scc");
  if (__builtin_expect(w < 4, 1)) {
    if (__builtin_expect(w >= 2, 1)) {
      touch(min(((jj * 2 + w) & 15) * 4096u + l * 64u, clen), pc0, lbase + L_DU);
    }
    if (__builtin_expect(w == 0 && !(xcd & 1) && jj < 24, 1)) pf3(xcd >> 2);
  }
  if (OW1 == 2 && w == 1) touch4(0u, (jj & 1) ? (const char*)(wqkv + (size_t)(panel >> 2) * 256 * 3072 + hp * 64) : (const char*)(x + (size_t)panel * 128 * DM + (panel >> 2) * 256), lbase + L_DU);
  unsigned* flg = (unsigned*)(smem + L_F);
  const bf16x8 ones = {0x3f80, 0x3f80, 0x3f80, 0x3f80, 0x3f80, 0x3f80, 0x3f80, 0x3f80};
  const f32x4 z4 = {0.f, 0.f, 0.f, 0.f};
  const int row0 = panel * 128 + hp * 8;
  auto ptile = [&](int p) { return p == 0 ? hp : !ORD ? (p <= 12 ? ((hp | 3) + p) & 15 : hp ^ (p - 12)) : p < 4 ? (hp & 12) + (int)((0x24343839u >> ((hp & 3) * 8 + (p - 1) * 2)) & 3u) : ((hp & 12) + p) & 15; };
  auto kvo = [&]() { return (l >> 3) * 2048u + (l & 7) * 16u + ((panel ^ (l >> 3)) & 15) * 128u; };
  if (__builtin_expect((w >> 2) == 1, 1)) {
    const int srow = l >> 3, sch = l & 7, cg = sch, rg = srow;
    const int kt0 = 4 * (panel >> 2);
    unsigned G, Tw, t12;
    unsigned voA[2], voW[2];
    const char* sA = (const char*)(x + (size_t)panel * 128 * DM);
    const char* sQ = (const char*)(wqkv + hp * 64);
#pragma unroll
    for (int i = 0; i < 2; ++i) {
      const int row = lw * 32 + i * 8 + srow, wr = lw * 16 + i * 8 + srow;
      voA[i] = (unsigned)(row * 2048 + ((sch ^ ((row >> 1) & 7)) * 16));
      voW[i] = (unsigned)(wr * 6144 + ((sch ^ ((((wr >> 1) & 1) << 2) | (((wr >> 3) & 1) << 1))) * 16));
    }
    auto stage = [&](int S) {
      const int kt = (S + kt0) & 15;
      const char* a = sA + kt * 128; const char* wv = sQ + (size_t)kt * 64 * 6144;
      const unsigned d = lbase + (S % 3) * SLOT;
#pragma unroll
      for (int i = 0; i < 4; ++i) glds(voA[i & 1], a + (i >> 1) * 32768, d + lw * 4096 + i * 1024);
#pragma unroll
      for (int i = 0; i < 6; ++i) glds(voW[i & 1], wv + ((i >> 1) == 2 ? 0 : ((i >> 1) + 1) * 2048), d + ABYTES + (i >> 1) * WSLAB + lw * 2048 + (i & 1) * 1024);
    };
    auto last = [&](int i) {
      const int kt = (15 + kt0) & 15;
      const char* a = sA + kt * 128; const char* wv = sQ + (size_t)kt * 64 * 6144;
      if (i < 4) glds(voA[i & 1], a + (i >> 1) * 32768, lbase + L_EX + lw * 4096 + i * 1024);
      else if (i < 8) glds(voW[i & 1], wv + ((i >> 1) - 1) * 2048, lbase + L_EW + ((i >> 1) - 2) * WSLAB + lw * 2048 + (i & 1) * 1024);
      else glds(voW[i & 1], wv, lbase + ABYTES + 2 * WSLAB + lw * 2048 + (i & 1) * 1024);
    };
    __builtin_amdgcn_s_setprio(3);
    if (OW1 == 1 && (jj & 1)) {
      const char* a = sA + kt0 * 128; const char* wv = sQ + (size_t)kt0 * 64 * 6144;
#pragma unroll
      for (int i = 0; i < 6; ++i) glds(voW[i & 1], wv + ((i >> 1) == 2 ? 0 : ((i >> 1) + 1) * 2048), lbase + ABYTES + (i >> 1) * WSLAB + lw * 2048 + (i & 1) * 1024);
#pragma unroll
      for (int i = 0; i < 4; ++i) glds(voA[i & 1], a + (i >> 1) * 32768, lbase + lw * 4096 + i * 1024);
    } else stage(0);
    stage(1);
    GL1(G, 0u, (const char*)ws, 0);
    const bool gu = (hp & 7) != 3;
    const int bb = gu ? (hp - (hp > 3) - (hp > 11)) * 16 + panel : 0;
    const int ke = bb & 7, cb = ((bb >> 3) * 2 + (lw & 1)) ^ ((lw >> 1) * 4);
    const int kc = (bb >> 4) * 4 + ((bb >> 1) & 3), cq = ((bb >> 3) & 1) | ((bb & 1) << 1);
    u32x4 gv[16], dv[8];
    const uintptr_t gb = (uintptr_t)(((lw >> 1) ? w_up : w_gate) + (size_t)(ke * 128) * FF + cb * 64);
    const u32x4 grs = {(unsigned)gb, (unsigned)(gb >> 32) & 0xffffu, gu ? 127u * FF * 2 + 128u : 0u, 0x00020000u};
    const unsigned go = rg * (16u * FF * 2) + cg * 16u;
    const uintptr_t db = (uintptr_t)(w_down + (size_t)(kc * 64) * DM + cq * 256 + lw * 64);
    const u32x4 drs = {(unsigned)db, (unsigned)(db >> 32) & 0xffffu, gu ? 63u * DM * 2 + 128u : 0u, 0x00020000u};
    const unsigned dof = rg * 16384u + cg * 16u;
    auto strm = [&](auto I0, auto I1, auto W) {
      static_for<(decltype(I1)::value > decltype(I0)::value ? decltype(I1)::value - decltype(I0)::value : 0)>([&](auto I) {
        constexpr int i = decltype(I)::value + decltype(I0)::value;
        if constexpr (decltype(W)::value) waitv<decltype(W)::value>();
        if constexpr (i < 16) bl4nt(gv[i], go, grs, i * (FF * 2u)); else bl4nt(dv[i - 16], dof, drs, (i - 16) * 2048u);
      });
    };
    const int wrow = 8 * g + tq, wsw = (((wrow >> 1) & 1) << 2) | (((wrow >> 3) & 1) << 1);
    const unsigned csa = lbase + ABYTES + (lw < 3 ? lw : 0) * WSLAB + wrow * 128 + (((2 * (panel & 3) + (tp >> 1)) ^ wsw) * 16) + 8 * (tp & 1);
    waitv<11>();
    RAW_BARRIER();
    float mean = 0.f, var = 1.f, c0 = 0.f, c1 = 0.f;
    f32x4 sm = z4;
    u32x4 pv; u32x2 pw; unsigned po, pc; unsigned long long pm;
    u32x4 ga[2], ca;
    const char* gab = ws + (lw < 3 ? WS_CS + (hp * 3 + lw) * 2048 : WS_ST + panel * 1024 - 1024);
    const unsigned go0 = l * 16u + (lw < 3 ? 0u : 1024u);
    static_for<13>([&](auto I) {
      constexpr int S = decltype(I)::value - 1;
      if constexpr (S >= 0) {
        waitv<10 + n_x(S - 2) + n_x(S - 1)>();
        RAW_BARRIER();
      }
      if constexpr (S < 3) {
        u32x2 c[4];
        cs_read(c, csa + ((S + 1) % 3) * SLOT);
        stage(S + 3);
        cs_sum(c, c0, c1);
      } else stage(S + 3);
      if constexpr (S == 0) {
        asm volatile("" : "+v"(G));
        Tw = __builtin_amdgcn_readfirstlane(G) + 1u;
        if ((Tw & 0xfffu) == 0u) ++Tw;
        t12 = Tw & 0xfffu;
        if (tid == 256) flg[F_T] = Tw;
      }
      if constexpr (S == SB0) {
        const f32x4* rp = (const f32x4*)(smem + L_REC) + (l & 7);
        sm = ((rp[0] + rp[8]) + (rp[16] + rp[24])) + ((rp[32] + rp[40]) + (rp[48] + rp[56]));
        pinf(sm);
      }
      if constexpr (S == SB0 + 1) {
        mean = sm[0] * (1.0f / 1024.0f);
        var = __builtin_fmaf(-mean, mean, sm[2] * (1.0f / 1024.0f));
        if (lw == 3 && __ballot(!(var * 64.0f >= mean * mean)) != 0ull) {
#pragma unroll 1
          for (int r = 0; r < 8; ++r) {
            const float m = __builtin_bit_cast(float, __builtin_amdgcn_readlane(__builtin_bit_cast(int, mean), r));
            const u32x4* p = (const u32x4*)(x + (size_t)(row0 + r) * DM) + l * 2;
            const u32x4 a = p[0], c = p[1];
            float q = 0.f;
#pragma unroll
            for (int i = 0; i < 8; ++i) {
              const unsigned u = i < 4 ? a[i & 3] : c[i & 3];
              const float e0 = bflo(u) - m, e1 = bfhi(u) - m;
              q = __builtin_fmaf(e0, e0, q); q = __builtin_fmaf(e1, e1, q);
            }
            q = wave_total(q) * (1.0f / 1024.0f);
            if ((l & 7) == r) var = q;
          }
        }
        mean = rbf(mean); var = rbf(rbf(var) + 1e-5f);
        pin1(mean); pin1(var);
      }
      if constexpr (S == SB0 + SQ) { var = rbf(sqrtf(var)); pin1(var); }
      if constexpr (S == SB0 + SR) { sm[1] = rbf(__builtin_fmaf(-mean, sm[3], sm[1]) / var); pinf(sm); }
      if constexpr (S == SB0 + SK) {
        if (lw == 3) {
          const float r = sm[1];
          const unsigned u = __float_as_uint(r);
          const unsigned key = (r == r ? ((u & 0x80000000u) ? (~u & 0xffff0000u) : (u | 0x80000000u)) : 0u) | (unsigned)(2047 - row0 - (l & 7));
          const unsigned km = dpp_umax<0x141>(dpp_umax<0x4e>(dpp_umax<0xb1>(key)));
          pv = u32x4{key, __float_as_uint(mean) | (__float_as_uint(var) >> 16), Tw, Tw};
          po = WS_ST + (row0 + l) * 8;
          pm = __ballot(key == km) & 0xffull;
        } else {
          pv = u32x4{0u, __float_as_uint(sum_g(c0 + c1)), Tw, Tw};
          po = WS_CS + (((hp * 3 + lw) * 4 + (panel >> 2)) * 64 + (panel & 3) * 16 + l) * 8;
          pm = 0ull;
        }
        pw = u32x2{pv[1], Tw};
        pc = WS_CA + (panel * 16 + hp) * 16;
        pub_pin(pv, pw, po, pc);
      }
      if constexpr (S == PUB) pub(pv, pw, po, pc, lw == 3 ? 0xffull : 0xffffull, pm, ws);
      if constexpr (S == GAS) { GL4(ga[0], go0, gab, 0, P_SC1); GL4(ga[1], l * 16u, gab, 1024, P_SC1); }
      static_for<8>([&](auto J) { if constexpr (ES0 + decltype(J)::value * ESL / 8 == S) last(decltype(J)::value); });
      if constexpr (S == 11) { if (CAE) GL4(ca, WS_CA + lw * 1024 + l * 16, ws, 0, P_SC1); strm(IC(0), IC(FI), IC(0)); }
      if constexpr (S == CTS) touch4(min(((jj * 4 + lw) * 4 + l) * 128u, clen), pc0, lbase + L_DU);
      if constexpr (S == 0) glds(l * 16u, (const char*)(wphi + hp * 2048 + lw * 512), lbase + L_WP + lw * 1024);
    });
    waitv<10 + n_x(10) + n_x(11)>(); RAW_BARRIER();
    last(8); last(9);
    if (CTS == 12) touch4(min(((jj * 4 + lw) * 4 + l) * 128u, clen), pc0, lbase + L_DU);
    if (!CAE) GL4(ca, WS_CA + lw * 1024 + l * 16, ws, 0, P_SC1);
    auto tdv = [&]() { if (TD == 1) touchn(gu ? l * 2048u : 0u, (const char*)db, lbase + L_DU); if (TD == 2) touch(gu ? l * 2048u : 0u, (const char*)db, lbase + L_DU); };
    strm(IC(FI), IC(FI + NF), IC(0));
    if (TDP == 0) tdv();
    auto ga_bad = [&]() { return (ga[0][1] ^ Tw) | (ga[0][3] ^ Tw) | (ga[1][1] ^ Tw) | (ga[1][3] ^ Tw); };
    auto ga_put = [&]() {
      if (lw < 3) {
        const float u0 = __uint_as_float(ga[0][0]) + __uint_as_float(ga[1][0]), u1 = __uint_as_float(ga[0][2]) + __uint_as_float(ga[1][2]);
        const f32x2 a = sw32(u0, u1);
        ((float*)(smem + L_C))[lw * 64 + (l & 31) * 2 + (l >> 5)] = a[0] + a[1];
      } else {
        *(f32x2*)(smem + L_MN + l * 8) = f32x2{-bfhi(ga[0][0]), -bfhi(ga[0][2])};
        *(f32x2*)(smem + L_RS + l * 8) = f32x2{__builtin_amdgcn_rcpf(bflo(ga[0][0])), __builtin_amdgcn_rcpf(bflo(ga[0][2]))};
        *(unsigned*)(smem + L_DN + l * 4) = (ga[0][0] & 0xffffu) | (ga[0][2] << 16);
      }
      lds_set(flg + F_ST + lw, 1u, l);
    };
    asm volatile("s_waitcnt vmcnt(%2)" : "+v"(ga[0]), "+v"(ga[1]) : "n"(3 + NF + (CTS == 12) + FI + (TD && TDP == 0)) : "memory");
    RAW_BARRIER();
    strm(IC(FI + NF), IC(FI + NF + NG), IC(0));
    if (TDP == 1) tdv();
#pragma unroll 1
    for (unsigned spins = 0; spins < SPIN; ++spins) {
      if (all0(ga_bad())) break;
      asm volatile("global_load_dwordx4 %0, %2, %4 sc1\n\tglobal_load_dwordx4 %1, %3, %4 offset:1024 sc1\n\ts_waitcnt vmcnt(0)"
                   : "+v"(ga[0]), "+v"(ga[1]) : "v"(go0), "v"(l * 16u), "s"(gab) : "memory");
    }
    if (QW == 1) waitv<1 + NF + NG>();
    ga_put();
    strm(IC(FI + NF + NG), IC(FI + NF + NG + NH), IC(0));
    if (TDP == 2) tdv();
    asm volatile("s_waitcnt vmcnt(%1)" : "+v"(ca) : "n"(NF + NG + NH + (TD != 0)) : "memory");
    if (QW == 2) raise<F_GO>(lbase + L_F + lw * 4);
    __builtin_amdgcn_s_setprio(FP0);
#pragma unroll 1
    for (unsigned spins = 0; spins < SPIN; ++spins) {
      if (all0((ca[2] ^ Tw) | (ca[3] ^ Tw))) break;
      asm volatile("global_load_dwordx4 %0, %1, %2 sc1\n\ts_waitcnt vmcnt(0)" : "+v"(ca) : "v"(WS_CA + lw * 1024 + l * 16), "s"(ws) : "memory");
    }
    {
      unsigned m = dpp_umax<0x140>(dpp_umax<0x141>(dpp_umax<0x4e>(dpp_umax<0xb1>(ca[0]))));
      m = dpp_umax_r<0x143, 0xc>(dpp_umax_r<0x142, 0xa>(m));
      m = __builtin_amdgcn_readlane(m, 63);
      if (ca[0] == m) *(u32x2*)(flg + F_CA + 2 * lw) = u32x2{m, ca[1]};
      lds_set(flg + F_SD + lw, 1u, l);
    }
    const Sel sl = make_sel(l);
    unsigned voO[2];
#pragma unroll
    for (int i = 0; i < 2; ++i) voO[i] = (unsigned)((lw * 16 + i * 8 + srow) * 2048 + (sch ^ (((srow >> 1) & 1) | ((((srow >> 2) ^ i) & 1) << 2))) * 16);
    auto wotile = [&](int p) {
      const char* wv = (const char*)(wo + (size_t)(ptile(p) * 64) * DM + hp * 64);
      if (WX) return;
      waitv<WNO>();
      glds(voO[0], wv, lbase + p * 8192 + lw * 2048); glds(voO[1], wv, lbase + p * 8192 + lw * 2048 + 1024);
    };
    lds_wait4(flg + F_SD);
    u32x2 sel;
    {
      const u32x4 e0 = ldsr1(LDS_OFF(flg + F_CA)), e1 = ldsr1(LDS_OFF(flg + F_CA + 4));
      const bool p = e0[2] > e0[0], q = e1[2] > e1[0];
      const unsigned k0 = p ? e0[2] : e0[0], m0 = p ? e0[3] : e0[1], k1 = q ? e1[2] : e1[0], m1 = q ? e1[3] : e1[1];
      sel = k1 > k0 ? u32x2{k1, m1} : u32x2{k0, m0};
    }
    const unsigned key = __builtin_amdgcn_readfirstlane(sel[0]);
    const int idx = 2047 - (int)(key & 2047u);
    if (tid == 256) flg[F_SEL] = key;
    unsigned xd;
    GL1P(xd, l * 4u, (const char*)(x + (size_t)idx * DM + ke * 128));
    strm(IC(FI + NF + NG + NH), IC(DL ? 16 : 24), IC(WIN));
    if (WE || GL) { lds_wait4(flg + F_LD); wotile(12); wotile(13); wotile(15); }
    if (GL) { lds_wait4(flg + F_IM); __builtin_amdgcn_s_setprio(FPR); wotile(0); wotile(1); wotile(2); wotile(3); wotile(7); wotile(8); }
    asm volatile("s_waitcnt vmcnt(%9)" : "+v"(xd), "+v"(gv[0]), "+v"(gv[1]), "+v"(gv[2]), "+v"(gv[3]), "+v"(gv[4]), "+v"(gv[5]), "+v"(gv[6]), "+v"(gv[7]) : "n"((DL ? 0 : FI + NF + NG + NH > 16 ? 24 - (FI + NF + NG + NH) : 8) + (GL ? 18 : WE ? 6 : 0)) : "memory");
    asm volatile("" : "+v"(gv[8]), "+v"(gv[9]), "+v"(gv[10]), "+v"(gv[11]), "+v"(gv[12]), "+v"(gv[13]), "+v"(gv[14]), "+v"(gv[15]));
    char* hs = smem + L_H + lw * 256;
    {
      const float sm = bfhi(sel[1]), rden = 1.0f / bflo(sel[1]);
      const unsigned d = pk2(bflo(xd) - sm, bfhi(xd) - sm);
      *(unsigned*)(hs + l * 4) = pk2(bflo(d) * rden, bfhi(d) * rden);
      const u32x4 x0 = *(const u32x4*)(hs + rg * 32), x1 = *(const u32x4*)(hs + rg * 32 + 16);
      f32x4 d0 = gemv2(x0[0], gv[0], gv[1], sl, z4), d1 = gemv2(x0[1], gv[2], gv[3], sl, z4);
      d0 = gemv2(x0[2], gv[4], gv[5], sl, d0); d1 = gemv2(x0[3], gv[6], gv[7], sl, d1);
      d0 = gemv2(x1[0], gv[8], gv[9], sl, d0); d1 = gemv2(x1[1], gv[10], gv[11], sl, d1);
      d0 = gemv2(x1[2], gv[12], gv[13], sl, d0); d1 = gemv2(x1[3], gv[14], gv[15], sl, d1);
      const u32x4 v = gemv_fin(d0 + d1, t12);
      if (gu && (l & 40) == 0) gs4(WS_GU + (cb * 16 + (lw >> 1) * 8 + ke) * 256 + cg * 32 + g * 16, ws, v);
    }
    if (CT2) touch4(min(((jj * 4 + lw) * 4 + l) * 128u, clen), pc0, lbase + L_DU);
    __builtin_amdgcn_s_setprio(FPR);
    if (!WE && !GL) { lds_wait4(flg + F_LD); wotile(12); wotile(13); wotile(15); }
    if (!GL) { lds_wait4(flg + F_IM); wotile(0); wotile(1); wotile(2); wotile(3); wotile(7); wotile(8); }
    lds_wait4(flg + F_BL);
    if (DL == 1) strm(IC(16), IC(24), IC(WIN));
    u32x4 p[4];
    const char* gp = ws + WS_GU + kc * 4096;
    static_for<8>([&](auto I) {
      constexpr int i = decltype(I)::value;
      if constexpr (i == PE) { GL4(p[0], l * 16u, gp, 0, P_SC1); GL4(p[1], l * 16u, gp, 1024, P_SC1); GL4(p[2], l * 16u, gp, 2048, P_SC1); GL4(p[3], l * 16u, gp, 3072, P_SC1); }
      if constexpr (i < 7) wotile((0x4eba965u >> (4 * i)) & 15);
    });
    if (DL == 2) { lds_wait4(flg + F_SL); strm(IC(16), IC(24), IC(0)); waitv<8>(); } else waitv<0>();
    lds_set(flg + F_WO + lw, 1u, l);
    if (FPR != FPE) __builtin_amdgcn_s_setprio(FPE);
    waitv4<0>(p);
    asm volatile("" : "+v"(dv[0]), "+v"(dv[1]), "+v"(dv[2]), "+v"(dv[3]), "+v"(dv[4]), "+v"(dv[5]), "+v"(dv[6]), "+v"(dv[7]) :: "memory");
    if (gu) {
#pragma unroll 1
      for (unsigned spins = 0; spins < SPIN; ++spins) {
        const unsigned long long m0 = __ballot(bad4(p[0], t12) & 0xfffu), m1 = __ballot(bad4(p[1], t12) & 0xfffu), m2 = __ballot(bad4(p[2], t12) & 0xfffu),
                                 m3 = __ballot(bad4(p[3], t12) & 0xfffu);
        if ((m0 | m1 | m2 | m3) == 0ull) break;
        reload4c(p, l * 16u, gp, m0, m1, m2, m3);
      }
      float hv[4];
#pragma unroll
      for (int e = 0; e < 4; ++e) {
        const float gg = rbf(sum_g(untag12(p[0][e]) + untag12(p[1][e]))), uu = rbf(sum_g(untag12(p[2][e]) + untag12(p[3][e])));
        hv[e] = rbf(silu_f(gg)) * uu;
      }
      if (l < 16) *(u32x2*)(hs + l * 8) = u32x2{pk2(hv[0], hv[1]), pk2(hv[2], hv[3])};
      const u32x4 hp4 = *(const u32x4*)(hs + rg * 16);
      f32x4 d0 = gemv2(hp4[0], dv[0], dv[1], sl, z4), d1 = gemv2(hp4[1], dv[2], dv[3], sl, z4);
      d0 = gemv2(hp4[2], dv[4], dv[5], sl, d0); d1 = gemv2(hp4[3], dv[6], dv[7], sl, d1);
      const u32x4 v = gemv_fin(d0 + d1, t12);
      if ((l & 40) == 0) gs4(WS_Y + ((cq * 4 + lw) * 56 + kc) * 256 + cg * 32 + g * 16, ws, v);
    }
    if (FPG != FPE) __builtin_amdgcn_s_setprio(FPG);
    if ((idx >> 7) == panel) {
      const char* yb = ws + WS_Y + hp * 56 * 256 + lw * 4096;
      u32x2 xr;
      GL2(xr, r15 * 8u, (const char*)(x + (size_t)idx * DM + hp * 64), 0);
      const unsigned tm = lw < 3 ? 0xfffu : 0u;
      u32x4 pl[8];
      lds_wait4(flg + F_OS);
      if (PYD) __builtin_amdgcn_s_sleep(PYD);
      pollt<PPY>(p, pl, l * 16u, yb, t12, tm);
      asm volatile("" : "+v"(xr));
      f32x4 y;
#pragma unroll
      for (int e = 0; e < 4; ++e) y[e] = sum_g((untag12(p[0][e]) + untag12(p[1][e])) + (lw < 3 ? untag12(p[2][e]) + untag12(p[3][e]) : 0.f));
      if (lw) {
        if (l < 16) *(f32x4*)(hs + l * 16) = y;
        lds_set(flg + F_AL + lw, 1u, l);
      } else {
      if (l == 0) flg[F_AL] = 1u;
      lds_wait4(flg + F_AL);
      y = (y + *(const f32x4*)(hs + 256 + r15 * 16)) + (*(const f32x4*)(hs + 512 + r15 * 16) + *(const f32x4*)(hs + 768 + r15 * 16));
      const int row = idx & 127;
      const float mn = ((const float*)(smem + L_MN))[row], rs = ((const float*)(smem + L_RS))[row];
      const float r = __uint_as_float((key & 0x80000000u) ? (key & 0x7fff0000u) : (~key & 0xffff0000u));
      const float sg = rbf(1.0f / (1.0f + __expf(-r)));
      float gi[4];
#pragma unroll
      for (int e = 0; e < 4; ++e) {
        const float xe = (e & 1) ? bfhi(xr[e >> 1]) : bflo(xr[e >> 1]);
        gi[e] = silu_r(rbf(rbf(rbf(xe + mn) * rs) + rbf(rbf(y[e]) * sg)));
      }
      if (l < 16) *(u32x2*)(smem + L_GI + l * 8) = u32x2{pk2(gi[0], gi[1]), pk2(gi[2], gi[3])};
      lds_set(flg + F_GT, 1u, l);
      }
      asm volatile("s_waitcnt vmcnt(0)" :: PKEEP(pl) : "memory");
    }
    if (b == 0 && tid == 256) gs1(0u, ws, Tw);
    GAP(2);
    return;
  }
  const int s = w >> 3;
  const int wt = lw & 1, wc = lw >> 1;
  const char* ob = ws + WS_O + (size_t)((RM == 1 ? xcd * 2 + (jj & 1) : RM == 2 ? (xcd >> 1) * 4 + (jj & 3) : RM == 3 ? 0 : panel) * 4 + lw) * 65536;
  const char* xfb = (const char*)(x + (size_t)(panel * 128 + lw * 32 + s * 16) * DM + hp * 64);
  u32x4 xv[2], gq[2];
  unsigned T;
  auto gate = [&]() {
    const float mn = ((const float*)(smem + L_MN))[lw * 32 + s * 16 + r15], rs = ((const float*)(smem + L_RS))[lw * 32 + s * 16 + r15];
#pragma unroll
    for (int hf = 0; hf < 2; ++hf) {
      unsigned x0 = xv[hf][0], x1 = xv[hf][1], x2 = xv[hf][2], x3 = xv[hf][3];
      sw16u(x0, x2); sw16u(x1, x3);
      xv[hf] = u32x4{x0, x1, x2, x3};
#pragma unroll
      for (int e = 0; e < 4; ++e) {
        const unsigned d = pk2(bflo(xv[hf][e]) + mn, bfhi(xv[hf][e]) + mn);
        const unsigned y = pk2(bflo(d) * rs, bfhi(d) * rs);
        gq[hf][e] = pk2(silu_r(bflo(y)), silu_r(bfhi(y)));
      }
    }
    pin2(gq); pin2(xv);
  };
  const int wrow = 8 * g + tq, wsw = (((wrow >> 1) & 1) << 2) | (((wrow >> 3) & 1) << 1);
  unsigned asw[3][2], wa[3][2];
#pragma unroll
  for (int q = 0; q < 3; ++q) {
#pragma unroll
    for (int ks = 0; ks < 2; ++ks) asw[q][ks] = lbase + (wt * 64 + r15) * 128 + (((4 * ks + g) ^ ((r15 >> 1) & 7)) * 16) + q * SLOT;
#pragma unroll
    for (int c = 0; c < 2; ++c) wa[q][c] = lbase + ABYTES + wrow * 128 + (((wc * 4 + 2 * c + (tp >> 1)) ^ wsw) * 16) + 8 * (tp & 1) + q * SLOT;
  }
  if (s) {
    u32x4 xa[4], wr[4];
    auto st_load = [&]() {
      const char* xb = (const char*)(x + (size_t)row0 * DM + lw * 256);
      const unsigned o = (l & 7) * 2048u + ((l >> 3) & 1) * 64u + g * 16u;
      GL4(xa[0], o, xb, 0, P_DEF); GL4(xa[1], o, xb, 128, P_DEF); GL4(xa[2], o, xb, 256, P_DEF); GL4(xa[3], o, xb, 384, P_DEF);
      const char* rb = (const char*)(w_r + lw * 256);
      const unsigned ro = (l & 1) * 64u + g * 16u;
      GL4(wr[0], ro, rb, 0, P_DEF); GL4(wr[1], ro, rb, 128, P_DEF); GL4(wr[2], ro, rb, 256, P_DEF); GL4(wr[3], ro, rb, 384, P_DEF);
    };
    auto st_sum = [&](auto NV) {
      waitv4<decltype(NV)::value>(xa); waitv4<decltype(NV)::value>(wr);
      f32x4 d0 = z4, d1 = z4, d2 = z4, d3 = z4;
#pragma unroll
      for (int j = 0; j < 4; ++j) {
        d0 = MFMA(ones, as8(xa[j]), d0); d1 = MFMA(as8(wr[j]), as8(xa[j]), d1);
        d2 = MFMA(as8(xa[j]), as8(xa[j]), d2); d3 = MFMA(as8(wr[j]), ones, d3);
      }
      const bool hf = l & 8;
      const float q2 = (l & 2) ? ((l & 1) ? d2[3] : d2[2]) : ((l & 1) ? d2[1] : d2[0]);
      if (g == tq) ldsw1(lbase + L_REC + (lw * 16 + r15) * 16, __builtin_bit_cast(u32x4, f32x4{d0[0], hf ? d1[1] : d1[0], q2, hf ? d3[1] : d3[0]}));
      SB();
    };
    f32x4 acc[4][2];
    bf16x8 w0, w1;
    float sc[4];
    const char* kb = ws + WS_KVR + hp * 8192 + wc * 4096;
    unsigned l16, fsl = LDS_OFF(flg + F_SL + 2 * wc);
    {
#pragma unroll
      for (int t = 0; t < 4; ++t) { acc[t][0] = z4; acc[t][1] = z4; }
      int ts = (hp - 4 * (panel >> 2)) & 15;
      const unsigned xr = lbase + (ts == 15 ? L_EX : (ts % 3) * SLOT) + (lw * 32 + 16 + r15) * 128 + ((g ^ ((r15 >> 1) & 7)) * 16);
      ts = min(ts, 13);
      Frag<2> f[2];
      auto rd = [&](Frag<2>& y, auto I, auto OW, unsigned pa, const unsigned (&pw)[2]) {
        constexpr int i = decltype(I)::value, ow = decltype(OW)::value + 2 * WSLAB;
        if constexpr (i < 4) ds_128<i * 2048>(y.af[i], pa);
        else if constexpr (i & 1) ds_tr<ow + 512>(y.wh[(i - 4) >> 1], pw[(i - 4) >> 1]);
        else ds_tr<ow>(y.wl[(i - 4) >> 1], pw[(i - 4) >> 1]);
      };
      auto step = [&](Frag<2>& c, Frag<2>& y, auto OW, unsigned pa, const unsigned (&pw)[2], auto RD, auto XT) {
        static_for<8>([&](auto I) {
          constexpr int i = decltype(I)::value;
          acc[i & 3][i >> 2] = MFMA(cat8(c.wl[i >> 2], c.wh[i >> 2]), c.af[i & 3], acc[i & 3][i >> 2]);
          SB();
          if constexpr (i < 4 && decltype(RD)::value) { rd(y, IC(2 * i), OW, pa, pw); rd(y, IC(2 * i + 1), OW, pa, pw); SB(); }
          if constexpr (i == 4 && decltype(XT)::value >= 0) { ds_if<decltype(XT)::value>(xv, xr, ts); SB(); }
        });
      };
      auto tile = [&](auto J, auto BAR, auto RD, auto&& side) {
        constexpr int j = decltype(J)::value % 3, n = (j + 1) % 3;
        f[0].wait();
        step(f[0], f[1], IC(4096), asw[j][1], wa[j], IC(1), IC(decltype(BAR)::value ? decltype(J)::value : -1));
        f[1].wait();
        if constexpr (decltype(BAR)::value) { __builtin_amdgcn_s_barrier(); asm volatile("" ::: "memory"); }
        side();
        if constexpr (!decltype(RD)::value) { if (l == 0) __hip_atomic_store(flg + F_BL + lw, 1u, __ATOMIC_RELAXED, __HIP_MEMORY_SCOPE_WORKGROUP); SB(); }
        step(f[1], f[0], IC(0), asw[n][0], wa[n], RD, IC(-1));
      };
      auto sents = [&]() {
        unsigned ln;
        asm volatile("v_mbcnt_lo_u32_b32 %0, -1, 0\n\tv_mbcnt_hi_u32_b32 %0, -1, %0" : "=v"(ln));
        u32x4 ff = {~0u, ~0u, ~0u, ~0u};
        asm volatile("" : "+v"(ff));
        const unsigned ko = (ln >> 3) * 2048u + (ln & 7) * 16u + ((panel ^ (ln >> 3)) & 15) * 128u;
        static_for<4>([&](auto J) { gs4o<decltype(J)::value * 1024>(ln * 16u, ws + WS_O + (size_t)(panel * 4 + lw) * 65536 + hp * 4096, ff); });
        gs4(ko, ws + WS_KVP + (hp * 4 + lw) * 32768, ff);
        gs4(ko ^ 17408u, ws + WS_KVP + (hp * 4 + lw) * 32768, ff);
        gs4m(ln * 16u, ws + WS_KVR + hp * 8192 + lw * 2048 + panel * 128, ff);
        SB();
      };
      asm volatile("" : "=v"(xv[0]), "=v"(xv[1]));
      TICK();
      static_for<8>([&](auto I) { rd(f[0], I, IC(0), asw[0][0], wa[0]); });
      static_for<13>([&](auto J) {
        constexpr int t = decltype(J)::value;
        if constexpr (t == 0) st_load();
        if constexpr (t == SBP) { waitv<0>(); SB(); }
        tile(J, IC(1), IC(1), [&]() {
          if constexpr ((t == SBI && (HB & 1)) || (t == 1 && (HB & 2))) {
            __builtin_amdgcn_s_setprio(3);
            if constexpr (t == SBI) sents();
            if constexpr (t == 1) st_sum(IC(0));
            __builtin_amdgcn_s_setprio(0);
            SB();
          }
        });
        if constexpr (t == 1 && !(HB & 2)) {
          st_sum(IC(0));
        }
        if constexpr (t == TP3) { if (lw == 0 && !(xcd & 1) && jj < 24) pf3((xcd >> 2) ^ 1); }
        if constexpr (t == TPW) {
          if (lw & 1) touch((l >> 2) * 2048u + (l & 3) * 128u, (const char*)wo + jj * 65536 + (lw >> 1) * 32768 + (xcd & 3) * 512, lbase + L_DU);
        }
        if constexpr (t == 2) MARK(2);
        if constexpr (t == SBI && !(HB & 1)) sents();
        if constexpr (PRE && t == PRE) {
          unsigned ln;
          asm volatile("v_mbcnt_lo_u32_b32 %0, -1, 0\n\tv_mbcnt_hi_u32_b32 %0, -1, %0" : "=v"(ln));
          const unsigned sl = ln & 15;
          const unsigned o = ln < 16 ? WS_KVP + (hp * 4 + lw) * 32768 + sl * 2048 + ((panel ^ sl) & 15) * 128
                           : ln < 32 ? WS_KVR + hp * 8192 + lw * 2048 + panel * 128 : WS_O + (panel * 4 + lw) * 65536 + hp * 4096 + (ln & 31) * 128;
          touch(o, ws, lbase + L_DU);
          SB();
        }
      });
      __builtin_amdgcn_s_barrier(); asm volatile("" ::: "memory");
      VISIT(3);
      if (CP == 2) { VISIT(8); VISIT(9); }
      relane();
      asw[0][0] += L_EX; asw[0][1] += L_EX;
      ds_if<13>(xv, xr, ts);
      wphi_frag(w0, w1, lbase + L_WP + wc * 2048 + (4 * g + tq) * 64 + tp * 8);
      lds_wait4(flg + F_ST);
      {
        const f32x4 c0 = *(const f32x4*)(smem + L_C + 512 + (wc * 32 + 4 * g) * 4), c1 = *(const f32x4*)(smem + L_C + 512 + (wc * 32 + 16 + 4 * g) * 4);
        f32x2 mr[4];
        rowst(mr, lbase + L_MN + (wt * 64 + r15) * 4);
#pragma unroll
        for (int t = 0; t < 4; ++t) {
          sc[t] = mr[t][1] * L2E;
          acc[t][0] += mr[t][0] * c0; acc[t][1] += mr[t][0] * c1;
          asm volatile("" : "+v"(sc[t]), "+v"(acc[t][0]), "+v"(acc[t][1]));
        }
      }
      asm volatile("" : "+v"(xv[0]), "+v"(xv[1]), "+v"(w0), "+v"(w1));
      if (GB) gate();
      SB();
      asm volatile("" : "+s"(kb));
      l16 = l * 16u;
      asm volatile("" : "+v"(l16), "+v"(fsl));
      GAP(6);
      VISIT(6);
      lds_wait1(flg + (BQ ? F_EX : F_IM) + lw);
      __builtin_amdgcn_s_setprio(BP);
      tile(IC(1), IC(0), IC(1), []() {});
      if (QW != 1) lds_wait4(flg + (QW == 2 ? F_GO : F_SD));
      tile(IC(2), IC(0), IC(1), []() {}); tile(IC(0), IC(0), IC(0), []() {});
    }
    bf16x8 pq[4][2];
    {
      f32x4 p[4][2];
      const float one[2] = {1.f, 1.f};
#pragma unroll
      for (int t = 0; t < 4; ++t) {
        const f32x4 v0 = acc[t][0], v1 = acc[t][1];
        const bf16x8 qp = as8(u32x4{pk2(v0[0], v0[1]), pk2(v0[2], v0[3]), pk2(v1[0], v1[1]), pk2(v1[2], v1[3])});
        p[t][0] = MFMA(w0, qp, z4); p[t][1] = MFMA(w1, qp, z4);
      }
      phi_map<false>(p, sc, one, [&](int t, const unsigned (&pe)[8]) {
        pq[t][0] = as8(u32x4{pe[0], pe[1], pe[2], pe[3]}); pq[t][1] = as8(u32x4{pe[4], pe[5], pe[6], pe[7]});
      });
    }
    asm volatile("" : "+v"(pq[0][0]), "+v"(pq[0][1]), "+v"(pq[1][0]), "+v"(pq[1][1]), "+v"(pq[2][0]), "+v"(pq[2][1]), "+v"(pq[3][0]), "+v"(pq[3][1]));
    if (!GB) gate();
    {
      f32x2 rd[4];
      u32x4 kf[4];
      PAD(6);
#pragma unroll 1
      for (unsigned spins = 0; spins < SPIN; ++spins) {
        u32x2 v; unsigned long long ck;
        asm volatile("ds_read_b64 %0, %2\n\ts_memrealtime %1\n\ts_waitcnt lgkmcnt(0)" : "=v"(v), "=s"(ck) : "v"(fsl) : "memory");
        const unsigned t0 = __builtin_amdgcn_readfirstlane(v[0]), t1 = __builtin_amdgcn_readfirstlane(v[1]), t = __builtin_amdgcn_readfirstlane((unsigned)ck);
        if ((t0 & t1) && min(t - t0, t - t1) >= D2) break;
      }
      GL4(kf[0], l16, kb, 0, P_SC1); GL4(kf[1], l16, kb, 1024, P_SC1); GL4(kf[2], l16, kb, 2048, P_SC1); GL4(kf[3], l16, kb, 3072, P_SC1);
      lds_wait4(flg + F_BL);
      lds_wait2(flg + F_KV + 2 * wc);
      {
        bf16x8 z0 = *(const bf16x8*)(smem + L_Z + wc * 128 + g * 16), z1 = *(const bf16x8*)(smem + L_Z + wc * 128 + 64 + g * 16);
        if (l & 1) { z0 = ones; z1 = ones; }
#pragma unroll
        for (int t = 0; t < 4; ++t) {
          const f32x4 a = MFMA(z0, pq[t][0], z4), c = MFMA(z1, pq[t][1], z4);
          const f32x2 r = {__builtin_amdgcn_rcpf(a[1]), __builtin_amdgcn_rcpf(c[1])};
          rd[t] = r * __builtin_amdgcn_rcpf(__builtin_fmaf(a[0], r[0], __builtin_fmaf(c[0], r[1], 1e-6f)));
          asm volatile("" : "+v"(rd[t]));
        }
      }
      const char* pb = ws + WS_O + (size_t)(panel * 4 + wt * 2) * 65536 + hp * 4096 + wc * 2048;
      unsigned l2 = l16 + 65536u, oa = lbase + L_EX + wc * 8192 + wt * 4096 + l * 16, fo = LDS_OFF(flg + F_OS + lw);
      asm volatile("" : "+s"(pb));
      asm volatile("" : "+v"(l2), "+v"(oa), "+v"(fo));
      waitv4<0>(kf);
      if (__builtin_expect(__ballot(max(max(kf[0][3], kf[1][3]), max(kf[2][3], kf[3][3])) == ~0u) != 0ull, 0)) {
        PAD(9);
#pragma unroll 1
        for (unsigned spins = 0; spins < SPIN; ++spins) {
          const unsigned long long m0 = __ballot(kf[0][3] == ~0u), m1 = __ballot(kf[1][3] == ~0u), m2 = __ballot(kf[2][3] == ~0u), m3 = __ballot(kf[3][3] == ~0u);
          if ((m0 | m1 | m2 | m3) == 0ull) break;
          reload4c(kf, l16, kb, m0, m1, m2, m3);
        }
      }
      u32x4 ov[4];
      f32x4 n[4][4];
      auto mm = [&](int t, int i) { n[t][i] = MFMA(as8(kf[i]), pq[t][i >> 1], z4); SB(); };
#pragma unroll
      for (int i = 0; i < 8; ++i) mm(i >> 2, i & 3);
      static_for<4>([&](auto TT) {
        constexpr int t = decltype(TT)::value;
        f32x4 a = n[t][0] * rd[t][0], c = n[t][1] * rd[t][0];
        SB(); if (t < 2) mm(t + 2, 0);
        a += n[t][2] * rd[t][1];
        SB(); if (t < 2) mm(t + 2, 1);
        c += n[t][3] * rd[t][1];
        SB(); if (t < 2) mm(t + 2, 2);
        ov[t] = u32x4{pk2(a[0], a[1]), pk2(a[2], a[3]), pk2(c[0], c[1]), nosent(pk2(c[2], c[3]))};
        SB(); if (t < 2) mm(t + 2, 3);
        asm volatile("global_store_dwordx4 %0, %1, %2 offset:%3 sc1" :: "v"(t < 2 ? l16 : l2), "v"(ov[t]), "s"(pb), "n"((t & 1) * 1024) : "memory");
        SB();
      });
      asm volatile("ds_write_b128 %0, %1\n\tds_write_b128 %0, %2 offset:1024\n\tds_write_b128 %0, %3 offset:2048\n\tds_write_b128 %0, %4 offset:3072\n\tds_write_b32 %5, %5\n\ts_nop 0"
                   :: "v"(oa), "v"(ov[0]), "v"(ov[1]), "v"(ov[2]), "v"(ov[3]), "v"(fo) : "memory");
    }
    __builtin_amdgcn_s_setprio(0);
    GAP(3);
  } else {
    if (tid < 20) ((u32x4*)flg)[tid] = u32x4{0, 0, 0, 0};
    f32x4 acc[4][4];
    Frag<4> f[2];
    u32x4 fs;
    f32x4 c0, c1;
    f32x2 mr[4];
    s16x4 wq[4];
    unsigned mra = lbase + L_MN + (wt * 64 + r15) * 4, cca = lbase + L_C + (wc * 32 + 4 * g) * 4, wpa = lbase + L_WP + wc * 2048 + (4 * g + tq) * 64 + tp * 8, fa = lbase + L_F;
    char* img = smem + lw * 8192;
    unsigned va = lbase + SLOT + ABYTES + lw * 4096 + l * 16, fb = lbase + L_F + lw * 4, ia, wi[4], ko = kvo(), da = lbase + L_DN + wt * 128 + g * 8,
             zo = WS_ZP + ((hp * 4 + lw) * 16 + panel) * 128 + (r15 * 4 + g) * 16, m4a = lbase + L_MN + (wt * 64 + 4 * g) * 4, cva = lbase + L_C + (64 + wc * 32 + r15) * 4;
    {
      const int row = 4 * g + tq, sx = (((row >> 1) & 3) << 2) | (row & 1) | (((row >> 3) & 1) << 1);
      ia = LDS_OFF(img) + row * 128 + (((8 * wt + tp) ^ sx) * 8);
      const int sw = (((r15 >> 1) & 3) << 2) | (r15 & 1) | (((r15 >> 3) & 1) << 1);
#pragma unroll
      for (int i = 0; i < 4; ++i) wi[i] = LDS_OFF(img) + r15 * 128 + (((4 * i + g) ^ sw) * 8);
    }
    {
#pragma unroll
      for (int t = 0; t < 4; ++t)
#pragma unroll
        for (int c = 0; c < 4; ++c) acc[t][c] = z4;
      auto rd = [&](Frag<4>& y, auto I, auto OW, unsigned pa, const unsigned (&pw)[2]) {
        constexpr int i = decltype(I)::value, ow = decltype(OW)::value;
        if constexpr (i < 4) ds_128<i * 2048>(y.af[i], pa);
        else {
          constexpr int c = (i - 4) >> 1;
          if constexpr (i & 1) ds_tr<ow + (c >> 1) * WSLAB + 512>(y.wh[c], pw[c & 1]);
          else ds_tr<ow + (c >> 1) * WSLAB>(y.wl[c], pw[c & 1]);
        }
      };
      auto step = [&](Frag<4>& c, Frag<4>& y, auto OW, unsigned pa, const unsigned (&pw)[2], auto&& more) {
        static_for<16>([&](auto I) {
          constexpr int i = decltype(I)::value, t = i & 3, cb = i >> 2;
          if constexpr (cb < 2) mfma_t(acc[t][cb], cat8(c.wl[cb], c.wh[cb]), __builtin_bit_cast(u32x4, c.af[t]));
          else mfma_t(acc[t][cb], c.af[t], __builtin_bit_cast(u32x4, cat8(c.wl[cb], c.wh[cb])));
          if constexpr (i < 12) rd(y, I, OW, pa, pw); else more(I);
        });
      };
      auto none = [](auto) {};
      __builtin_amdgcn_s_setprio(2);
#pragma unroll
      for (int t = 0; t < 4; ++t) asm volatile("" : "+v"(acc[t][0]), "+v"(acc[t][1]), "+v"(acc[t][2]), "+v"(acc[t][3]));
      asm volatile("" : "+v"(mra), "+v"(cca), "+v"(wpa), "+v"(fa), "+v"(va), "+v"(fb), "+v"(ia), "+v"(wi[0]), "+v"(wi[1]), "+v"(wi[2]), "+v"(wi[3]),
                   "+v"(ko), "+v"(da), "+v"(zo), "+v"(m4a), "+v"(cva));
      TICK();
      static_for<12>([&](auto I) { rd(f[0], I, IC(0), asw[0][0], wa[0]); });
#pragma unroll 1
      for (int it = 0; it < 5; ++it) {
        static_for<3>([&](auto J) {
          constexpr int j = decltype(J)::value, n = (j + 1) % 3;
          f[0].wait();
          step(f[0], f[1], IC(4096), asw[j][1], wa[j], none);
          if constexpr (j == 1) {
            const unsigned dx = it == 4 ? L_EX : 0, dw = it == 4 ? L_EW - ABYTES : 0;
            asw[0][0] += dx; asw[0][1] += dx; wa[0][0] += dw; wa[0][1] += dw;
            asm volatile("" : "+v"(asw[0][0]), "+v"(asw[0][1]), "+v"(wa[0][0]), "+v"(wa[0][1]));
          }
          f[1].wait();
          if (j < 2 || __builtin_expect(it < 4, 1)) __builtin_amdgcn_s_barrier();
          asm volatile("" ::: "memory");
          step(f[1], f[0], IC(0), asw[n][0], wa[n], none);
        });
      }
      f[0].wait();
      step(f[0], f[1], IC(4096), asw[0][1], wa[0], [&](auto I) {
        constexpr int i = decltype(I)::value;
        if constexpr (i == 12) { ds_tr<0>(wq[0], wpa); ds_tr<1024>(wq[1], wpa); ds_tr<32>(wq[2], wpa); }
        if constexpr (i == 13) { ds_tr<1056>(wq[3], wpa); ds_32<F_T * 4>(T, fa); }
      });
      asm volatile("s_waitcnt lgkmcnt(5)" : "+v"(f[1].af[0]), "+v"(f[1].af[1]), "+v"(f[1].af[2]), "+v"(f[1].af[3]), "+v"(f[1].wl[0]), "+v"(f[1].wh[0]), "+v"(f[1].wl[1]), "+v"(f[1].wh[1]),
                   "+v"(f[1].wl[2]), "+v"(f[1].wh[2]), "+v"(f[1].wl[3]), "+v"(f[1].wh[3]));
      static_for<8>([&](auto I) {
        constexpr int i = decltype(I)::value, t = i >> 1, cb = i & 1;
        acc[t][cb] = MFMA(cat8(f[1].wl[cb], f[1].wh[cb]), f[1].af[t], acc[t][cb]);
        SB();
        if constexpr (i == 0) { ds_128v<F_ST * 4>(fs, fa); ds_128v<0>(c0, cca); ds_128v<64>(c1, cca); }
        if constexpr (i == 1) { ds_2x32<0>(mr[0], mra); ds_2x32<16>(mr[1], mra); }
        if constexpr (i == 2) { ds_2x32<32>(mr[2], mra); ds_2x32<48>(mr[3], mra); }
        if constexpr (i == 3) raise<F_LD>(fb);
        SB();
      });
    }
    if (AP != 2) __builtin_amdgcn_s_setprio(AP);
    asm volatile("s_waitcnt lgkmcnt(0)" : "+v"(fs), "+v"(c0), "+v"(c1), "+v"(mr[0]), "+v"(mr[1]), "+v"(mr[2]), "+v"(mr[3]), "+v"(wq[0]), "+v"(wq[1]), "+v"(wq[2]), "+v"(wq[3]), "+v"(T));
    if (__builtin_expect(!__builtin_amdgcn_readfirstlane(fs[0] & fs[1] & fs[2] & fs[3]), FIN)) {
      lds_wait4(flg + F_ST);
      asm volatile("ds_read_b128 %0, %2\n\tds_read_b128 %1, %2 offset:64\n\ts_waitcnt lgkmcnt(0)" : "=&v"(c0), "=&v"(c1) : "v"(cca) : "memory");
      rowst(mr, mra);
    }
    T = __builtin_amdgcn_readfirstlane(T);
    const unsigned t12 = T & 0xfffu;
    const bf16x8 w0 = cat8(wq[0], wq[1]), w1 = cat8(wq[2], wq[3]);
    u32x4 vp[2][4];
    auto selm = [](unsigned long long m, float a, float c) { float r; asm("v_cndmask_b32 %0, %1, %2, %3" : "=v"(r) : "v"(c), "v"(a), "s"(m)); return r; };
    {
      f32x4 p[4][2], m4[4];
      f32x2 cv;
      float sc[4], nrm[2];
#pragma unroll
      for (int t = 0; t < 4; ++t) {
        sc[t] = mr[t][1] * L2E;
        if (t < 2) nrm[t] = selm(0xffff0000ffff0000ull, mr[2 * t + 1][1], mr[2 * t][1]);
        const f32x4 v0 = mr[t][0] * c0 + acc[t][0], v1 = mr[t][0] * c1 + acc[t][1];
        const bf16x8 kc = as8(u32x4{pk2(v0[0], v0[1]), pk2(v0[2], v0[3]), pk2(v1[0], v1[1]), pk2(v1[2], v1[3])});
        p[t][0] = MFMA(w0, kc, z4); p[t][1] = MFMA(w1, kc, z4);
      }
      auto vl = [&](int i0, int i1) {
#pragma unroll
        for (int i = i0; i < i1; ++i)
          acc[i & 3][2 + (i >> 2)] = MFMA(f[1].af[i & 3], cat8(f[1].wl[2 + (i >> 2)], f[1].wh[2 + (i >> 2)]), acc[i & 3][2 + (i >> 2)]);
      };
      SB();
      PAD(3);
      SB();
      phi_map<true>(p, sc, nrm, [&](int t, const unsigned (&pe)[8]) {
        if (t < 2) { SB(); vl(4 * t, 4 * t + 4); SB(); }
        if (t == 2) asm volatile("s_waitcnt lgkmcnt(0)" : "+v"(m4[0]), "+v"(m4[1]), "+v"(m4[2]), "+v"(m4[3]), "+v"(cv) :: "memory");
#pragma unroll
        for (int i = 0; i < 4; ++i) *(u32x2*)(smem + (wi[i] - lbase) + t * 2048) = u32x2{pe[2 * i], pe[2 * i + 1]};
        if (t == 1) {
          asm volatile("ds_read_b128 %0, %5\n\tds_read_b128 %1, %5 offset:64\n\tds_read_b128 %2, %5 offset:128\n\tds_read_b128 %3, %5 offset:192\n\tds_read2_b32 %4, %6 offset1:16"
                       : "=&v"(m4[0]), "=&v"(m4[1]), "=&v"(m4[2]), "=&v"(m4[3]), "=&v"(cv)
                       : "v"(m4a), "v"(cva) : "memory");
          SB();
        }
        if (t == VPT) {
#pragma unroll
          for (int d = 0; d < 2; ++d)
#pragma unroll
            for (int q = 0; q < 2; ++q) {
              const f32x4 a = m4[2 * q] * cv[d] + acc[2 * q][2 + d], c = m4[2 * q + 1] * cv[d] + acc[2 * q + 1][2 + d];
              vp[0][2 * q + d] = u32x4{pk2(a[0], a[1]), pk2(a[2], a[3]), pk2(c[0], c[1]), pk2(c[2], c[3])};
            }
          asm volatile("ds_write_b128 %0, %1\n\tds_write_b128 %0, %2 offset:1024\n\tds_write_b128 %0, %3 offset:2048\n\tds_write_b128 %0, %4 offset:3072"
                       :: "v"(va), "v"(vp[0][0]), "v"(vp[0][1]), "v"(vp[0][2]), "v"(vp[0][3]) : "memory");
        }
      });
    }
    const char* kb = ws + WS_KVP + (hp * 4 + lw) * 32768;
    {
      s16x4 tl[2][2], th[2][2];
#define IMG8 "ds_read_b64_tr_b16 %0, %8\n\tds_read_b64_tr_b16 %1, %8 offset:2048\n\tds_read_b64_tr_b16 %2, %9\n\tds_read_b64_tr_b16 %3, %9 offset:2048\n\t" \
             "ds_read_b64_tr_b16 %4, %8 offset:4096\n\tds_read_b64_tr_b16 %5, %8 offset:6144\n\tds_read_b64_tr_b16 %6, %9 offset:4096\n\tds_read_b64_tr_b16 %7, %9 offset:6144\n\t"
#define IMGO(c) c(tl[0][0]), c(th[0][0]), c(tl[0][1]), c(th[0][1]), c(tl[1][0]), c(th[1][0]), c(tl[1][1]), c(th[1][1])
      raise<F_EX>(fb);
      asm volatile(IMG8 : IMGO("=&v") : "v"(ia), "v"(ia ^ 32u) : "memory");
      {
        u32x4 d0, d1, d2, d3;
        asm volatile("ds_read2_b64 %0, %4 offset1:4\n\tds_read2_b64 %1, %4 offset0:8 offset1:12\n\tds_read2_b64 %2, %5 offset1:4\n\tds_read2_b64 %3, %5 offset0:8 offset1:12"
                     : "=&v"(d0), "=&v"(d1), "=&v"(d2), "=&v"(d3) : "v"(da), "v"(da ^ 128u) : "memory");
        unsigned o1 = ko ^ 17408u, ib = ia ^ 8192u, ic = ia ^ 8224u, fx, fe = (fb ^ 4u) + F_EX * 4;
        va ^= 4096u;
        asm volatile("s_waitcnt lgkmcnt(4)" : IMGO("+v"), "+v"(o1), "+v"(ib), "+v"(ic), "+v"(va), "+v"(fe), "+s"(kb) :: "memory");
        f32x4 kva[2][2], za[2];
        bf16x8 a[2][2];
#pragma unroll
        for (int q = 0; q < 2; ++q)
#pragma unroll
          for (int fb = 0; fb < 2; ++fb) {
            a[q][fb] = cat8(tl[q][fb], th[q][fb]);
#pragma unroll
            for (int d = 0; d < 2; ++d) kva[fb][d] = MFMA(a[q][fb], as8(vp[0][2 * q + d]), q ? kva[fb][d] : z4);
          }
        SB();
        s16x4 ul[2][2], uh[2][2];
#define IMGP(c) c(ul[0][0]), c(uh[0][0]), c(ul[0][1]), c(uh[0][1]), c(ul[1][0]), c(uh[1][0]), c(ul[1][1]), c(uh[1][1])
#define OTH "ds_read_b32 %10, %15\n\t" IMG8 "ds_read_b128 %11, %16\n\tds_read_b128 %12, %16 offset:1024\n\tds_read_b128 %13, %16 offset:2048\n\tds_read_b128 %14, %16 offset:3072"
        asm volatile("s_waitcnt lgkmcnt(0)" : "+v"(d0), "+v"(d1), "+v"(d2), "+v"(d3) :: "memory");
        asm volatile(OTH : IMGP("=&v"), "+v"(ib), "+v"(ic), "=&v"(fx), "=&v"(vp[1][0]), "=&v"(vp[1][1]), "=&v"(vp[1][2]), "=&v"(vp[1][3]) : "v"(fe), "v"(va) : "memory");
        SB();
#pragma unroll
        for (int fb = 0; fb < 2; ++fb) za[fb] = MFMA(a[1][fb], as8(d1), MFMA(a[0][fb], as8(d0), z4));
        SB();
        asm volatile("s_waitcnt lgkmcnt(0)" : IMGP("+v"), "+v"(fx), "+v"(vp[1][0]), "+v"(vp[1][1]), "+v"(vp[1][2]), "+v"(vp[1][3]) :: "memory");
        if (__builtin_expect(__builtin_amdgcn_readfirstlane(fx) == 0u, 0)) {
          PAD(8);
#pragma unroll 1
          for (unsigned spins = 0; spins < SPIN && __builtin_amdgcn_readfirstlane(fx) == 0u; ++spins)
            asm volatile(OTH "\n\ts_waitcnt lgkmcnt(0)" : IMGP("+v"), "+v"(ib), "+v"(ic), "+v"(fx), "+v"(vp[1][0]), "+v"(vp[1][1]), "+v"(vp[1][2]), "+v"(vp[1][3]) : "v"(fe), "v"(va) : "memory");
        }
#pragma unroll
        for (int d = 0; d < 2; ++d)
#pragma unroll
          for (int q = 0; q < 2; ++q)
#pragma unroll
            for (int fb = 0; fb < 2; ++fb) kva[fb][d] = MFMA(cat8(ul[q][fb], uh[q][fb]), as8(vp[1][2 * q + d]), kva[fb][d]);
        SB();
#pragma unroll
        for (int d = 0; d < 2; ++d) {
          za[d] = MFMA(cat8(ul[1][d], uh[1][d]), as8(d3), MFMA(cat8(ul[0][d], uh[0][d]), as8(d2), za[d]));
          const f32x4 u = kva[0][d], v = kva[1][d];
          gs4(d ? o1 : ko, kb, u32x4{pk2(u[0], u[1]), pk2(u[2], u[3]), pk2(v[0], v[1]), nosent(pk2(v[2], v[3]))});
        }
        raise<F_IM>(fb);
        relane();
        const f32x4 z = (l & 1) ? za[1] : za[0];
        if (r15 < 2) gs4(zo, ws, u32x4{tag12(z[0], t12), tag12(z[1], t12), tag12(z[2], t12), tag12(z[3], t12)});
      }
    }
    GL4(xv[0], xo, xfb, 0, P_DEF); GL4(xv[1], xo, xfb, 64, P_DEF);
    const char* zb = ws + WS_ZP + (hp * 4 + lw) * 2048;
    u32x4 zp[2], pl[4];
    {
      u32x4 p[2];
      const unsigned one = (l & 1) ? 0x3f800000u : 0x3f80u;
      u32x4 s0 = {(l & 2) ? 0u : one, (l & 2) ? one : 0u, 0u, 0u}, s1 = {0u, 0u, s0[0], s0[1]};
      asm volatile("" : "+v"(s0), "+v"(s1));
      GAP(7);
      VISIT(7);
      if (PD1) __builtin_amdgcn_s_sleep(PD1);
      poll2<PP1>(p, pl, l * 16u + panel * 2048, kb);
      asm volatile("" : "+v"(xv[0]), "+v"(xv[1]));
      GL4(zp[0], l * 16u, zb, 0, P_SC1); GL4(zp[1], l * 16u, zb, 1024, P_SC1);
      f32x4 u = MFMA(as8(s0), as8(p[1]), MFMA(as8(s0), as8(p[0]), z4)), v = MFMA(as8(s1), as8(p[1]), MFMA(as8(s1), as8(p[0]), z4));
#pragma unroll
      for (int e = 0; e < 4; ++e) { u[e] += ror8(u[e]); v[e] += ror8(v[e]); }
      gs4m(l * 16u, ws + WS_KVR + hp * 8192 + lw * 2048 + panel * 128, u32x4{pk2(u[0], u[1]), pk2(u[2], u[3]), pk2(v[0], v[1]), nosent(pk2(v[2], v[3]))});
      asm volatile("ds_write_b32 %0, %1 offset:%2" :: "v"(fb), "v"(now() | 1u), "n"(F_SL * 4) : "memory");
    }
    {
      asm volatile("s_waitcnt vmcnt(1)" : "+v"(zp[0]), "+v"(zp[1]) :: "memory");
      asm volatile("" :: "v"(pl[0]), "v"(pl[1]), "v"(pl[2]), "v"(pl[3]));
#pragma unroll 1
      for (unsigned spins = 0; spins < SPIN; ++spins) {
        const unsigned long long m0 = __ballot(bad4(zp[0], t12) & 0xfffu), m1 = __ballot(bad4(zp[1], t12) & 0xfffu);
        if ((m0 | m1) == 0ull) break;
        reload2(zp, l * 16u, zb, m0, m1);
      }
      float z[4];
#pragma unroll
      for (int e = 0; e < 4; ++e) { const float q = sum_g(untag12(zp[0][e]) + untag12(zp[1][e])); z[e] = q + ror8(q); }
      if (l < 8) *(u32x2*)(smem + L_Z + lw * 64 + (l & 3) * 16 + (l >> 2) * 8) = u32x2{pk2(z[0], z[1]), pk2(z[2], z[3])};
      lds_set(flg + F_KV + lw, 1u, l);
    }
    __builtin_amdgcn_s_setprio(0);
    gate();
  }
  {
    relane();
    u32x4 of[NS][2], o0[2];
    f32x4 acc[2][4];
#pragma unroll
    for (int t = 0; t < 2; ++t)
#pragma unroll
      for (int c = 0; c < 4; ++c) acc[t][c] = z4;
    tie8<1>(acc);
    unsigned wb[4], wx[4];
    {
      const int row = 4 * g + tq, f = ((row >> 1) & 1) | ((((row >> 2) ^ (row >> 3)) & 1) << 2);
#pragma unroll
      for (int nb = 0; nb < 4; ++nb) wb[nb] = lbase + s * 4096 + row * 128 + ((((tp & 2) + (nb & 1) + 4 * (nb >> 1)) ^ f) * 16) + (tp & 1) * 8;
    }
    const unsigned v0 = l * 16u + s * 3072u, v1 = l * 16u + s * 1024u + 1024u, v3 = l * 16u + 2048u - s * 1024u;
    auto oadr = [&](int h) { return ob + ptile(h) * 4096; };
    s16x4 wl[5], wh[5], el[2][4], eh[2][4];
    auto wrd = [&](auto K, auto HI) {
      constexpr int k = decltype(K)::value, h = k >> 2, nb = k & 3, o = (h & 7) * 8192;
      if constexpr (decltype(HI)::value) ds_tr<o + 2048>(wh[k % 5], wb[nb]);
      else {
        if constexpr (h == 8) { wb[nb] += 65536u; asm volatile("" : "+v"(wb[nb])); }
        ds_tr<o>(wl[k % 5], wb[nb]);
      }
    };
    unsigned xa = lbase + lw * 8192 + s * 4096 + l * 16, fa = LDS_OFF(flg + F_X + s * 4 + lw), fg = LDS_OFF(flg + F_PG + s * 4);
    f32x2 xf[8], gf[8];
    char* op = (char*)(out + (size_t)(panel * 128 + lw * 32 + s * 16) * DM + hp * 64);
    unsigned oo = (l & 7) * 2048u + (l & 8) * 8u + g * 16u, oa = lbase + L_EX + s * 9216 + lw * 2048 + l * 16;
    char* op2 = op + 16384;
    int idx;
    auto pre = [&]() {
#pragma unroll
      for (int i = 0; i < 8; ++i) {
        xf[i] = f32x2{bflo(xv[i >> 2][i & 3]), bfhi(xv[i >> 2][i & 3])}; gf[i] = f32x2{bflo(gq[i >> 2][i & 3]), bfhi(gq[i >> 2][i & 3])};
        asm volatile("" : "+v"(xf[i]), "+v"(gf[i]));
      }
      asm volatile("" : "+v"(oo), "+s"(op), "+s"(op2), "+v"(xa), "+v"(fa), "+v"(fg), "+v"(oa), "+v"(wb[0]), "+v"(wb[1]), "+v"(wb[2]), "+v"(wb[3]));
    };
    if (PW) pre();
    if (s && !BGO) lds_wait1(flg + F_GO + lw);
    else {
      lds_wait4(flg + F_OS);
      if (PD3) __builtin_amdgcn_s_sleep(PD3);
      if (!BGO && l == 0) __hip_atomic_store(flg + F_GO + lw, 1u, __ATOMIC_RELAXED, __HIP_MEMORY_SCOPE_WORKGROUP);
    }
    static_for<NS>([&](auto H) { opair(of[decltype(H)::value], v0, v1, oadr(decltype(H)::value + 1)); });
    lds_wait4(flg + F_WO);
    idx = 2047 - (int)(lds_get(flg + F_SEL) & 2047u);
    asm volatile("ds_read_b128 %0, %2\n\tds_read_b128 %1, %3" : "=&v"(o0[0]), "=&v"(o0[1]) : "v"(oa), "v"(oa ^ 1024u) : "memory");
    static_for<4>([&](auto NB) { wrd(NB, IC(0)); wrd(NB, IC(1)); });
    if (!PW) pre();
    u32x4 pg;
    static_for<14>([&](auto H) {
      constexpr int h = decltype(H)::value, x0 = h == XH || h == XH + 1, x1 = h == XH + 1 || h == XH + 2;
      u32x4 (&o)[2] = h ? of[(h + NS - 1) % NS] : o0;
      if constexpr (h == 1 && BP3) { if (s) __builtin_amdgcn_s_setprio(1); }
      static_for<4>([&](auto NB) {
        constexpr int nb = decltype(NB)::value, k = 4 * h + nb;
        constexpr bool ld = nb == 3 && h && h + NS < 16;
        const bf16x8 wf = cat8(wl[k % 5], wh[k % 5]);
        mfma_w<x1 + (3 - nb) * (2 + 2 * x1) + nb * (2 + 2 * x0) + (h == 5 || h == 13)>(acc[1][nb], wf, o[1]);
        wrd(IC(k + 4), IC(0));
        if constexpr (x0) ds_tr<(h - XH + 6) * 8192>(el[h - XH][nb], wx[nb]);
        if constexpr (ld) GL4(o[1], h + NS < 14 ? v1 : v3, oadr(h + NS), 0, P_SC1);
        mfma_t(acc[0][nb], wf, o[0]);
        wrd(IC(k + 4), IC(1));
        if constexpr (x0) ds_tr<(h - XH + 6) * 8192 + 2048>(eh[h - XH][nb], wx[nb]);
        if constexpr (ld) GL4(o[0], v0, oadr(h + NS), 0, P_SC1);
      });
      if constexpr (h == 4) asm volatile("ds_write_b32 %0, %0" :: "v"(fg + lw * 4u) : "memory");
      if constexpr (h == LM7) PAD(7);
      if constexpr (h == XH - 1) {
#pragma unroll
        for (int nb = 0; nb < 4; ++nb) { wx[nb] = wb[nb] ^ 4096u; asm volatile("" : "+v"(wx[nb])); }
      }
      if constexpr (h == 12) asm volatile("ds_read_b128 %0, %1" : "=v"(pg) : "v"(fg) : "memory");
      if constexpr (h < OC3) ocheck3<4>(of[h % 3], of[(h + 1) % 3], of[(h + 2) % 3], v0, v1, oadr(h + 1), oadr(h + 2), oadr(h + 3));
      else if constexpr (h < 13) ocheck<2 * ((h + NS < 15 ? h + NS : 15) - h - 1), true>(of[h % NS], v0, v1, oadr(h + 1));
    });
    asm volatile("s_waitcnt lgkmcnt(%1)" : "+v"(pg) : "n"(XH == 12 ? 15 : 8));
    if (__builtin_expect(!__builtin_amdgcn_readfirstlane(pg[0] & pg[1] & pg[2] & pg[3]), 0)) lds_wait4(flg + F_PG + s * 4);
    asm volatile("s_nop 1\n\tds_write_b128 %0, %1\n\tds_write_b128 %0, %2 offset:1024\n\tds_write_b128 %0, %3 offset:2048\n\tds_write_b128 %0, %4 offset:3072\n\tds_write_b32 %5, %5"
                 :: "v"(xa), "v"(acc[1][0]), "v"(acc[1][1]), "v"(acc[1][2]), "v"(acc[1][3]), "v"(fa) : "memory");
    __builtin_amdgcn_s_setprio(0);
    ocheck<2, true>(of[13 % NS], v0, v3, oadr(14));
    static_for<4>([&](auto NB) {
      constexpr int nb = decltype(NB)::value, k = 56 + nb;
      mfma_w<nb ? 15 : 5>(acc[0][nb], cat8(wl[k % 5], wh[k % 5]), of[13 % NS][0]);
      wrd(IC(k + 4), IC(0)); wrd(IC(k + 4), IC(1));
    });
#pragma unroll
    for (int nb = 0; nb < 4; ++nb) mfma_t(acc[0][nb], cat8(el[0][nb], eh[0][nb]), of[13 % NS][1]);
    unsigned fx;
    fa ^= 16u; xa ^= 4096u;
    asm volatile("ds_read_b32 %0, %5\n\tds_read_b128 %1, %6\n\tds_read_b128 %2, %6 offset:1024\n\tds_read_b128 %3, %6 offset:2048\n\tds_read_b128 %4, %6 offset:3072"
                 : "=&v"(fx), "=&v"(acc[1][0]), "=&v"(acc[1][1]), "=&v"(acc[1][2]), "=&v"(acc[1][3]) : "v"(fa), "v"(xa) : "memory");
    ocheck<0, true>(of[14 % NS], v0, v3, oadr(15));
#pragma unroll
    for (int nb = 0; nb < 4; ++nb) mfma_t(acc[0][nb], cat8(el[1][nb], eh[1][nb]), of[14 % NS][1]);
    static_for<4>([&](auto NB) { constexpr int nb = decltype(NB)::value, k = 60 + nb; mfma_w<5>(acc[0][nb], cat8(wl[k % 5], wh[k % 5]), of[14 % NS][0]); });
    if (__builtin_expect((idx >> 4) == panel * 8 + lw * 2 + s, GIN)) {
      lds_wait1(flg + F_GT);
      relane();
      if (r15 == (idx & 15)) {
        const u32x2* gi = (const u32x2*)(smem + L_GI + (g >> 1) * 32 + (g & 1) * 8);
#pragma unroll
        for (int i = 0; i < 4; ++i) {
          const u32x2 a = gi[(i & 1) * 2 + (i >> 1) * 8];
          gf[2 * i] = f32x2{bflo(a[0]), bfhi(a[0])}; gf[2 * i + 1] = f32x2{bflo(a[1]), bfhi(a[1])};
        }
      }
    }
    asm volatile("s_waitcnt lgkmcnt(0)" : "+v"(fx), "+v"(acc[1][0]), "+v"(acc[1][1]), "+v"(acc[1][2]), "+v"(acc[1][3]) :: "memory");
    if (__builtin_expect(__builtin_amdgcn_readfirstlane(fx) == 0u, 0))
#pragma unroll 1
    for (unsigned spins = 0; spins < SPIN && __builtin_amdgcn_readfirstlane(fx) == 0u; ++spins)
      asm volatile("s_sleep 1\n\tds_read_b32 %0, %5\n\tds_read_b128 %1, %6\n\tds_read_b128 %2, %6 offset:1024\n\tds_read_b128 %3, %6 offset:2048\n\t"
                   "ds_read_b128 %4, %6 offset:3072\n\ts_waitcnt lgkmcnt(0)"
                   : "+v"(fx), "+v"(acc[1][0]), "+v"(acc[1][1]), "+v"(acc[1][2]), "+v"(acc[1][3]) : "v"(fa), "v"(xa) : "memory");
#pragma unroll
    for (int i = 0; i < 8; ++i) xf[i] = __builtin_elementwise_fma(f32x2{acc[1][i >> 1][2 * (i & 1)], acc[1][i >> 1][2 * (i & 1) + 1]}, gf[i], xf[i]);
    asm volatile("" : "+v"(xf[0]), "+v"(xf[1]), "+v"(xf[2]), "+v"(xf[3]), "+v"(xf[4]), "+v"(xf[5]), "+v"(xf[6]), "+v"(xf[7]),
                 "+v"(acc[0][0]), "+v"(acc[0][1]), "+v"(acc[0][2]), "+v"(acc[0][3]));
    unsigned rr[8];
#pragma unroll
    for (int i = 0; i < 8; ++i) {
      const f32x2 r = __builtin_elementwise_fma(f32x2{acc[0][i >> 1][2 * (i & 1)], acc[0][i >> 1][2 * (i & 1) + 1]}, gf[i], xf[i]);
      rr[i] = pk2(r[0], r[1]);
    }
    {
      sw16u(rr[0], rr[2]); sw16u(rr[1], rr[3]); sw16u(rr[4], rr[6]); sw16u(rr[5], rr[7]);
      unsigned a0, a1, a2, a3, c0, c1, c2, c3;
#define CD(d, x, y) "v_cndmask_b32_dpp " d ", " x ", " y ", vcc row_ror:8 row_mask:0xf bank_mask:0xf\n\t"
      asm volatile("s_mov_b32 vcc_lo, 0x00ff00ff\n\ts_mov_b32 vcc_hi, 0x00ff00ff\n\t" CD("%0", "%12", "%8") CD("%2", "%14", "%10") CD("%1", "%13", "%9") CD("%3", "%15", "%11")
                   "s_not_b64 vcc, vcc\n\t" CD("%4", "%8", "%12") CD("%5", "%9", "%13") CD("%6", "%10", "%14") CD("%7", "%11", "%15")
                   : "=&v"(a0), "=&v"(a1), "=&v"(a2), "=&v"(a3), "=&v"(c0), "=&v"(c1), "=&v"(c2), "=&v"(c3)
                   : "v"(rr[0]), "v"(rr[1]), "v"(rr[2]), "v"(rr[3]), "v"(rr[4]), "v"(rr[5]), "v"(rr[6]), "v"(rr[7]) : "vcc", "scc");
      const u32x4 a = {a0, a1, a2, a3}, c = {c0, c1, c2, c3};
      asm volatile("global_store_dwordx4 %0, %1, %3 " OSF "\n.Lkf_last:\n\tglobal_store_dwordx4 %0, %2, %4 " OSF :: "v"(oo), "v"(a), "v"(c), "s"(op), "s"(op2) : "memory");
    }
    if (s) {
    } else {
    }
  }
}

extern "C" void kernel_launch(void* const* d_in, const int* in_sizes, int n_in,
                              void* d_out, int out_size, void* d_ws, size_t ws_size,
                              hipStream_t stream) {
  (void)hipFuncSetAttribute((const void*)kf_main, hipFuncAttributeMaxDynamicSharedMemorySize, LDSM);
  kf_main<<<256, 768, LDSM, stream>>>((const u16*)d_in[0], (const u16*)d_in[1], (char*)d_ws + WSO, (const u16*)d_in[5], (const u16*)d_in[2],
                                      (const u16*)d_in[3], (const u16*)d_in[6], (const u16*)d_in[7], (const u16*)d_in[8], (u16*)d_out);
}
```

```cpp
#include <hip/hip_runtime.h>
#include <stdint.h>
#include <utility>

typedef __attribute__((ext_vector_type(8))) short bf16x8;
typedef __attribute__((ext_vector_type(4))) short s16x4;
typedef __attribute__((ext_vector_type(4))) float f32x4;
typedef __attribute__((ext_vector_type(2))) float f32x2;
typedef __attribute__((ext_vector_type(4))) unsigned u32x4;
typedef __attribute__((ext_vector_type(2))) unsigned u32x2;
typedef __attribute__((ext_vector_type(2))) __bf16 bfx2;
typedef unsigned short u16;

#define DI __device__ __forceinline__
#define MFMA(a, b, c) __builtin_amdgcn_mfma_f32_16x16x32_bf16((a), (b), (c), 0, 0, 0)
#define SB() __builtin_amdgcn_sched_barrier(0)
#define IC(x) std::integral_constant<int, (x)>{}

DI unsigned pk2(float a, float b) { f32x2 v = {a, b}; return __builtin_bit_cast(unsigned, __builtin_convertvector(v, bfx2)); }
DI float bflo(unsigned u) { return __uint_as_float(u << 16); }
DI float bfhi(unsigned u) { return __uint_as_float(u & 0xffff0000u); }
DI float rbf(float x) { return (float)(__bf16)x; }
DI f32x2 rb2(f32x2 v) { return f32x2{__uint_as_float(pk2(0.f, v[0])), __uint_as_float(pk2(0.f, v[1]))}; }
DI bf16x8 cat8(s16x4 lo, s16x4 hi) { return __builtin_shufflevector(lo, hi, 0, 1, 2, 3, 4, 5, 6, 7); }
DI bf16x8 as8(u32x4 v) { return __builtin_bit_cast(bf16x8, v); }
template <int... I, class F> DI void static_for_impl(std::integer_sequence<int, I...>, F&& f) { (f(std::integral_constant<int, I>{}), ...); }
template <int N, class F> DI void static_for(F&& f) { static_for_impl(std::make_integer_sequence<int, N>{}, f); }
template <int N> DI void waitv() { asm volatile("s_waitcnt vmcnt(%0)" ::"n"(N) : "memory"); }
#define RAW_BARRIER() do { asm volatile("s_waitcnt lgkmcnt(0)" ::: "memory"); __builtin_amdgcn_s_barrier(); asm volatile("" ::: "memory"); } while (0)
#define TICK() do { SB(); RAW_BARRIER(); SB(); } while (0)
#define LAY 1
#define GAP(n) asm volatile("s_branch .Lkg" #n "\n.Lkh" #n ":\n\t.fill ((.Lkg" #n " - .Lkt" #n " - (.Lkh" #n " - kf_main)) & 4095) / 4, 4, 0xbf800000\n.Lkg" #n ":" ::: "memory")
#define MARK(n) asm volatile(".Lkt" #n ":" ::: "memory")
#define PAD(n) do { unsigned d_; asm volatile(".Lkt" #n ":\n\ts_mov_b32 %0, 0xbe801d6a" : "=s"(d_)); } while (0)
#define VISIT(n) do { const char* p_; int o_; asm volatile("s_getpc_b64 %0\n.Lkv" #n ":\n\ts_mov_b32 %1, .Lkt" #n " - .Lkv" #n " + 4" : "=s"(p_), "=s"(o_)); \
                      asm volatile("s_swappc_b64 vcc, %0" :: "s"(p_ + o_) : "vcc"); } while (0)
#define DS_B128(dst, addr, off) asm volatile("ds_read_b128 %0, %1 offset:%2" : "=v"(dst) : "v"(addr), "n"(off))
#define DS_TR(dst, addr, off) asm volatile("ds_read_b64_tr_b16 %0, %1 offset:%2" : "=v"(dst) : "v"(addr), "n"(off))
#define LDS_OFF(p) ((unsigned)(uintptr_t)(__attribute__((address_space(3))) const char*)(p))
enum { P_DEF = 0, P_SC1 = 1, P_NT = 2 };
template <int OFF, int POL> DI void gl4(u32x4& d, unsigned voff, const char* sb) {
  if constexpr (POL == P_SC1) asm volatile("global_load_dwordx4 %0, %1, %2 offset:%3 sc1" : "=v"(d) : "v"(voff), "s"(sb), "n"(OFF));
  else if constexpr (POL == P_NT) asm volatile("global_load_dwordx4 %0, %1, %2 offset:%3 nt" : "=v"(d) : "v"(voff), "s"(sb), "n"(OFF));
  else asm volatile("global_load_dwordx4 %0, %1, %2 offset:%3" : "=v"(d) : "v"(voff), "s"(sb), "n"(OFF));
}
#define GL4(dst, voff, sb, off, pol) gl4<off, pol>(dst, voff, sb)
#define GL2(dst, voff, sb, off) asm volatile("global_load_dwordx2 %0, %1, %2 offset:%3" : "=v"(dst) : "v"(voff), "s"(sb), "n"(off))
#define GL1(dst, voff, sb, off) asm volatile("global_load_dword %0, %1, %2 offset:%3 sc1" : "=v"(dst) : "v"(voff), "s"(sb), "n"(off))
DI void bl4nt(u32x4& d, unsigned voff, u32x4 rs, unsigned soff) {
  asm volatile("buffer_load_dwordx4 %0, %1, %2, %3 offen nt" : "=v"(d) : "v"(voff), "s"(rs), "s"(soff));
}
DI void mfma_t(f32x4& c, bf16x8 a, u32x4 bb) { asm volatile("v_mfma_f32_16x16x32_bf16 %0, %1, %2, %0" : "+v"(c) : "v"(a), "v"(bb)); }
template <int N> DI void mfma_w(f32x4& c, bf16x8 a, u32x4 bb) {
  asm volatile("s_waitcnt lgkmcnt(%3)\n\tv_mfma_f32_16x16x32_bf16 %0, %1, %2, %0" : "+v"(c) : "v"(a), "v"(bb), "n"(N));
}
template <int N> DI void tie8(f32x4 (&a)[2][4]) {
  asm volatile("s_nop %8" : "+v"(a[0][0]), "+v"(a[0][1]), "+v"(a[0][2]), "+v"(a[0][3]), "+v"(a[1][0]), "+v"(a[1][1]), "+v"(a[1][2]), "+v"(a[1][3]) : "n"(N));
}
template <int OFF> DI void ds_tr(s16x4& d, unsigned a) { DS_TR(d, a, OFF); }
template <int N> DI void waitv4(u32x4 (&v)[4]) { asm volatile("s_waitcnt vmcnt(%4)" : "+v"(v[0]), "+v"(v[1]), "+v"(v[2]), "+v"(v[3]) : "n"(N)); }
template <int N> DI void waitl8(s16x4 (&a)[4], s16x4 (&c)[4]) {
  asm volatile("s_waitcnt lgkmcnt(%8)" : "+v"(a[0]), "+v"(c[0]), "+v"(a[1]), "+v"(c[1]), "+v"(a[2]), "+v"(c[2]), "+v"(a[3]), "+v"(c[3]) : "n"(N));
}
DI void gs4(unsigned voff, const char* sb, u32x4 v) { asm volatile("global_store_dwordx4 %0, %1, %2 sc1\n\ts_nop 1" :: "v"(voff), "v"(v), "s"(sb) : "memory"); }
DI void gs1(unsigned voff, const char* sb, unsigned v) { asm volatile("global_store_dword %0, %1, %2 sc1" :: "v"(voff), "v"(v), "s"(sb) : "memory"); }
DI void glds(unsigned voff, const char* sbase, unsigned lds) {
  asm volatile("s_mov_b32 m0, %2\n\ts_nop 0\n\tglobal_load_lds_dwordx4 %0, %1" :: "v"(voff), "s"(sbase), "s"(lds) : "memory");
}
DI void touch(unsigned voff, const char* sbase, unsigned lds) {
  asm volatile("s_mov_b32 m0, %2\n\ts_nop 0\n\tglobal_load_lds_dword %0, %1" :: "v"(voff), "s"(sbase), "s"(lds) : "memory");
}
DI void touch4(unsigned voff, const char* sbase, unsigned lds) {
  asm volatile("s_mov_b64 exec, 15\n\ts_mov_b32 m0, %2\n\ts_nop 0\n\tglobal_load_lds_dword %0, %1\n\ts_mov_b64 exec, -1" :: "v"(voff), "s"(sbase), "s"(lds) : "memory");
}
DI void pub_pin(u32x4& a, u32x2& b, unsigned& c, unsigned& d) { asm volatile("" : "+v"(a), "+v"(b), "+v"(c), "+v"(d)); }
DI void pub(u32x4 v, u32x2 w, unsigned o, unsigned oc, unsigned long long m, unsigned long long mc, const char* sb) {
  asm volatile("s_mov_b64 exec, %4\n\tglobal_store_dwordx2 %2, %1, %6 sc1\n\ts_mov_b64 exec, %5\n\tglobal_store_dwordx4 %3, %0, %6 sc1\n\ts_mov_b64 exec, -1\n\ts_nop 0"
               :: "v"(v), "v"(w), "v"(o), "v"(oc), "s"(m), "s"(mc), "s"(sb) : "memory");
}
DI float silu_f(float x) { return x / (1.0f + __expf(-x)); }
DI float silu_r(float x) { return x * __builtin_amdgcn_rcpf(1.0f + __expf(-x)); }
DI float dot2(unsigned a, unsigned b, float c) { return __builtin_amdgcn_fdot2_f32_bf16(__builtin_bit_cast(bfx2, a), __builtin_bit_cast(bfx2, b), c, false); }

template <int OFF> DI void gs4o(unsigned voff, const char* sb, u32x4 v) {
  asm volatile("global_store_dwordx4 %0, %1, %2 offset:%3 sc1\n\ts_nop 1" :: "v"(voff), "v"(v), "s"(sb), "n"(OFF) : "memory");
}
DI void gs4m(unsigned voff, const char* sb, u32x4 v) {
  asm volatile("s_mov_b64 exec, 0xff\n\tglobal_store_dwordx4 %0, %1, %2 sc1\n\ts_mov_b64 exec, -1\n\ts_nop 0" :: "v"(voff), "v"(v), "s"(sb) : "memory");
}
DI void gs2(unsigned voff, const char* sb, u32x2 v) { asm volatile("global_store_dwordx2 %0, %1, %2 sc1" :: "v"(voff), "v"(v), "s"(sb) : "memory"); }
#define GL1P(dst, voff, sb) asm volatile("global_load_dword %0, %1, %2" : "=v"(dst) : "v"(voff), "s"(sb))
DI u32x4 ldsr1(unsigned a) { u32x4 v; asm volatile("ds_read_b128 %0, %1\n\ts_waitcnt lgkmcnt(0)" : "=v"(v) : "v"(a) : "memory"); return v; }
DI void ldsw1(unsigned a, u32x4 v) { asm volatile("ds_write_b128 %0, %1" :: "v"(a), "v"(v) : "memory"); }
DI unsigned nosent(unsigned v) { return min(v, 0xfffffffeu); }

constexpr int DM = 1024, FF = 3584;
constexpr unsigned WS_ST = 4096;
constexpr unsigned WS_CA = WS_ST + 2048 * 8;
constexpr unsigned WS_CS = WS_CA + 256 * 16;
constexpr unsigned WS_KVP = WS_CS + 256 * 1024;
constexpr unsigned WS_ZP = WS_KVP + 2048 * 1024;
constexpr unsigned WS_KVR = WS_ZP + 1024 * 128;
constexpr unsigned WS_O = WS_KVR + 16 * 8192;
constexpr unsigned WS_GU = WS_O + 4096 * 1024;
constexpr unsigned WS_Y = WS_GU + 56 * 4096;

constexpr int ABYTES = 16384, WSLAB = 8192, SLOT = 40960;
constexpr int L_EW = 3 * SLOT;
constexpr int L_EX = L_EW + 2 * WSLAB;
constexpr int L_REC = L_EW;
constexpr int L_Z = L_EX + ABYTES;
constexpr int L_WP = L_Z + 256;
constexpr int L_MN = L_WP + 4096;
constexpr int L_RS = L_MN + 512;
constexpr int L_DN = L_RS + 512;
constexpr int L_C = L_DN + 256;
constexpr int L_H = L_C + 768;
constexpr int L_GI = L_H + 1024;
constexpr int L_F = L_GI + 128;
constexpr int L_DU = L_F + 320;
constexpr int LDSM = L_DU + 256;
constexpr int F_KV = 0, F_WO = 4, F_PG = 8, F_X = 16, F_T = 24, F_GT = 25, F_BL = 32, F_IM = 36, F_EX = 40, F_ST = 44, F_SD = 48, F_CA = 52, F_OS = 60, F_GO = 64, F_AL = 68, F_SL = 72, F_LD = 76;
#define SPINV (1u << 20)
constexpr unsigned SPIN = SPINV;
DI void pin1(float& v) { asm volatile("" : "+v"(v)); }
DI void pinf(f32x4& v) { asm volatile("" : "+v"(v)); }
DI void pin2(u32x4 (&v)[2]) { asm volatile("" : "+v"(v[0]), "+v"(v[1])); }
constexpr float L2E = 1.4426950408889634f;

template <int CTRL, int RMASK> DI float dpp_add(float v) {
  return v + __builtin_bit_cast(float, __builtin_amdgcn_update_dpp(0, __builtin_bit_cast(int, v), CTRL, RMASK, 0xf, true));
}
DI float wave_total(float v) {
  v = dpp_add<0x111, 0xf>(v); v = dpp_add<0x112, 0xf>(v); v = dpp_add<0x114, 0xf>(v); v = dpp_add<0x118, 0xf>(v);
  v = dpp_add<0x142, 0xa>(v); v = dpp_add<0x143, 0xc>(v);
  return __builtin_bit_cast(float, __builtin_amdgcn_readlane(__builtin_bit_cast(int, v), 63));
}
DI f32x2 sw32(float a, float b) {
  auto r = __builtin_amdgcn_permlane32_swap(__float_as_uint(a), __float_as_uint(b), false, false);
  return f32x2{__uint_as_float(r[0]), __uint_as_float(r[1])};
}
DI f32x2 sw16(float a, float b) {
  auto r = __builtin_amdgcn_permlane16_swap(__float_as_uint(a), __float_as_uint(b), false, false);
  return f32x2{__uint_as_float(r[0]), __uint_as_float(r[1])};
}
DI float ror8(float v) { return __builtin_bit_cast(float, __builtin_amdgcn_update_dpp(0, __builtin_bit_cast(int, v), 0x128, 0xf, 0xf, true)); }
DI float sum_g(float v) {
  f32x2 a = sw32(v, v); v = a[0] + a[1];
  a = sw16(v, v); return a[0] + a[1];
}
DI unsigned tag12(float v, unsigned t) { return ((__float_as_uint(v) + 0x800u) & 0xfffff000u) | t; }
DI float untag12(unsigned u) { return __uint_as_float(u & 0xfffff000u); }
DI unsigned bad4(u32x4 v, unsigned t) { return (v[0] ^ t) | (v[1] ^ t) | (v[2] ^ t) | (v[3] ^ t); }
DI bool all0(unsigned v) { return __ballot(v != 0u) == 0ull; }
DI void lds_set(unsigned* f, unsigned v, int l) {
  asm volatile("s_waitcnt lgkmcnt(0)" ::: "memory");
  if (l == 0) __hip_atomic_store(f, v, __ATOMIC_RELAXED, __HIP_MEMORY_SCOPE_WORKGROUP);
}
DI unsigned lds_get(unsigned* f) { return __builtin_amdgcn_readfirstlane(__hip_atomic_load(f, __ATOMIC_RELAXED, __HIP_MEMORY_SCOPE_WORKGROUP)); }
DI void lds_wait4(unsigned* f) {
#pragma unroll 1
  for (unsigned spins = 0; spins < SPIN; ++spins) {
    u32x4 v; asm volatile("ds_read_b128 %0, %1\n\ts_waitcnt lgkmcnt(0)" : "=v"(v) : "v"(LDS_OFF(f)) : "memory");
    if (__builtin_amdgcn_readfirstlane(v[0] & v[1] & v[2] & v[3])) break;
    __builtin_amdgcn_s_sleep(1);
  }
}

template <int NC> struct Frag {
  bf16x8 af[4]; s16x4 wl[NC], wh[NC];
  DI void wait() {
    if constexpr (NC == 4)
      asm volatile("s_waitcnt lgkmcnt(0)" : "+v"(af[0]), "+v"(af[1]), "+v"(af[2]), "+v"(af[3]), "+v"(wl[0]), "+v"(wh[0]), "+v"(wl[1]), "+v"(wh[1]),
                   "+v"(wl[2]), "+v"(wh[2]), "+v"(wl[3]), "+v"(wh[3]));
    else
      asm volatile("s_waitcnt lgkmcnt(0)" : "+v"(af[0]), "+v"(af[1]), "+v"(af[2]), "+v"(af[3]), "+v"(wl[0]), "+v"(wh[0]), "+v"(wl[1]), "+v"(wh[1]));
  }
};
template <int OFF> DI void ds_32(unsigned& d, unsigned a) { asm volatile("ds_read_b32 %0, %1 offset:%2" : "=v"(d) : "v"(a), "n"(OFF) : "memory"); }
template <int OFF, class V> DI void ds_128v(V& d, unsigned a) { DS_B128(d, a, OFF); }
template <int O> DI void ds_2x32(f32x2& d, unsigned a) { asm volatile("ds_read2_b32 %0, %1 offset0:%2 offset1:%3" : "=v"(d) : "v"(a), "n"(O), "n"(O + 128) : "memory"); }
template <int F> DI void raise(unsigned a) { asm volatile("ds_write_b32 %0, %0 offset:%1" :: "v"(a), "n"(F * 4) : "memory"); }
template <int OFF> DI void ds_w64(unsigned a, u32x2 v) { asm volatile("ds_write_b64 %0, %1 offset:%2" :: "v"(a), "v"(v), "n"(OFF) : "memory"); }
template <int T> DI void ds_if(u32x4 (&v)[2], unsigned a, int t) {
  asm volatile("s_cmp_eq_u32 %4, %5\n\ts_cselect_b64 exec, -1, 0\n\tds_read_b128 %0, %2\n\tds_read_b128 %1, %3\n\ts_mov_b64 exec, -1"
               : "+v"(v[0]), "+v"(v[1]) : "v"(a), "v"(a ^ 64u), "s"(t), "n"(T) : "scc", "memory");
}
template <int OFF> DI void ds_128(bf16x8& d, unsigned a) { DS_B128(d, a, OFF); }
DI void lds_wait2(unsigned* f) {
#pragma unroll 1
  for (unsigned spins = 0; spins < SPIN; ++spins) {
    u32x2 v; asm volatile("ds_read_b64 %0, %1\n\ts_waitcnt lgkmcnt(0)" : "=v"(v) : "v"(LDS_OFF(f)) : "memory");
    if (__builtin_amdgcn_readfirstlane(v[0] & v[1])) break;
    __builtin_amdgcn_s_sleep(1);
  }
}
DI void reload2(u32x4 (&p)[2], unsigned o, const char* sb, unsigned long long m0, unsigned long long m1) {
  asm volatile("s_mov_b64 exec, %4\n\tglobal_load_dwordx4 %0, %2, %3 sc1\n\ts_mov_b64 exec, %5\n\tglobal_load_dwordx4 %1, %2, %3 offset:1024 sc1\n\t"
               "s_mov_b64 exec, -1\n\ts_waitcnt vmcnt(0)"
               : "+v"(p[0]), "+v"(p[1]) : "v"(o), "s"(sb), "s"(m0), "s"(m1) : "memory");
}

template <int CTRL> DI unsigned dpp_umax(unsigned v) { return max(v, (unsigned)__builtin_amdgcn_update_dpp((int)v, (int)v, CTRL, 0xf, 0xf, false)); }
template <int CTRL, int RMASK> DI unsigned dpp_umax_r(unsigned v) { return max(v, (unsigned)__builtin_amdgcn_update_dpp((int)v, (int)v, CTRL, RMASK, 0xf, false)); }
DI unsigned now() { return (unsigned)__builtin_amdgcn_s_memrealtime(); }
DI void lds_wait1(unsigned* f) {
#pragma unroll 1
  for (unsigned spins = 0; spins < SPIN && lds_get(f) == 0u; ++spins) __builtin_amdgcn_s_sleep(1);
  asm volatile("" ::: "memory");
}

DI void cs_read(u32x2 (&c)[4], unsigned a) {
  asm volatile("ds_read_b64_tr_b16 %0, %4\n\tds_read_b64_tr_b16 %1, %4 offset:512\n\tds_read_b64_tr_b16 %2, %4 offset:4096\n\tds_read_b64_tr_b16 %3, %4 offset:4608"
               : "=&v"(c[0]), "=&v"(c[1]), "=&v"(c[2]), "=&v"(c[3]) : "v"(a) : "memory");
}
DI void cs_sum(u32x2 (&c)[4], float& s0, float& s1) {
  asm volatile("s_waitcnt lgkmcnt(0)" : "+v"(c[0]), "+v"(c[1]), "+v"(c[2]), "+v"(c[3]) :: "memory");
#pragma unroll
  for (int i = 0; i < 4; ++i) { s0 = dot2(c[i][0], 0x3f803f80u, s0); s1 = dot2(c[i][1], 0x3f803f80u, s1); }
  asm volatile("" : "+v"(s0), "+v"(s1));
}
#define NG 4
#define PP1 12
#define PD1 12
#define PD3 6
#define PPY 12
#define FP0 1
#define FPR 2
#define FPE 0
#define FPG 3
#define GAS 10
#define SB0 2
#define SBI 3
#define SBP 6
#define CTS 6
#define ES0 7
#define ESL 5
#define NF 4
#define WNO 40
#define D2 40
#define PYD 16
#define LM7 2
#define XH 11
#define OC3 1
#define CAS 9
#define LG1 1
#define LM1 4
#define BH0 5
#define NS 3
constexpr int PUB = SBP > SB0 + 1 ? SBP : SB0 + 1;
constexpr int n_e(int S) { int n = 0; for (int i = 0; i < 8; ++i) n += ES0 + i * ESL / 8 == S; return n; }
constexpr int n_x(int S) { return (S == PUB) + (S == 0) + (S == GAS ? 2 : 0) + (S == CTS) + n_e(S) + (S == CAS); }
static_assert((NS == 3 || !OC3) && SBP > SBI && PUB >= 3 && PUB < GAS && GAS <= 10 && CTS <= 10 && ES0 > SB0 && ES0 + ESL <= 12 && CAS > PUB && CAS <= 9 && NF + NG <= 16, "");
DI void wphi_frag(bf16x8& w0, bf16x8& w1, unsigned a) {
  s16x4 t0, t1, t2, t3;
  asm volatile("ds_read_b64_tr_b16 %0, %4\n\tds_read_b64_tr_b16 %1, %4 offset:1024\n\tds_read_b64_tr_b16 %2, %4 offset:32\n\t"
               "ds_read_b64_tr_b16 %3, %4 offset:1056\n\ts_waitcnt lgkmcnt(0)" : "=&v"(t0), "=&v"(t1), "=&v"(t2), "=&v"(t3) : "v"(a) : "memory");
  w0 = cat8(t0, t1); w1 = cat8(t2, t3);
}
DI float mx2(float a, float b) { return __builtin_elementwise_maximum(a, b); }
DI float mx3(float a, float b, float c) { return __builtin_elementwise_maximum(__builtin_elementwise_maximum(a, b), c); }
DI void bcast2(float m, f32x2& c0, f32x2& c1) {
  const f32x2 a = sw16(m, m);
  c0 = sw32(a[0], a[0]); c1 = sw32(a[1], a[1]);
}
template <bool NRM, class F> DI void phi_map(const f32x4 (&p)[4][2], const float (&sc)[4], const float (&nrm)[2], F&& sink) {
  float r[4];
  f32x2 c[4];
#pragma unroll
  for (int t = 0; t < 4; ++t) {
    const float mx = mx2(mx3(mx3(p[t][0][0], p[t][0][1], p[t][0][2]), p[t][0][3], p[t][1][0]), mx3(p[t][1][1], p[t][1][2], p[t][1][3]));
    const float nm = mx2(mx3(mx3(-p[t][0][0], -p[t][0][1], -p[t][0][2]), -p[t][0][3], -p[t][1][0]), mx3(-p[t][1][1], -p[t][1][2], -p[t][1][3]));
    const f32x2 a = sw32(mx, nm);
    r[t] = mx2(a[0], a[1]);
  }
#pragma unroll
  for (int q = 0; q < 2; ++q) {
    const f32x2 a = sw16(r[2 * q], r[2 * q + 1]);
    bcast2(mx2(a[0], a[1]), c[2 * q], c[2 * q + 1]);
  }
#pragma unroll
  for (int t = 0; t < 4; ++t) c[t] *= -sc[t];
#pragma unroll
  for (int q = 0; q < 2; ++q) {
    f32x2 e[2][8], sm[2];
#pragma unroll
    for (int i = 0; i < 8; ++i)
#pragma unroll
      for (int j = 0; j < 2; ++j) {
        const float v = p[2 * q + j][i >> 2][i & 3];
        const f32x2 a = __builtin_elementwise_fma(f32x2{v, v}, f32x2{sc[2 * q + j], -sc[2 * q + j]}, c[2 * q + j]);
        e[j][i] = f32x2{__builtin_amdgcn_exp2f(a[0]), __builtin_amdgcn_exp2f(a[1])};
      }
    if constexpr (NRM) {
#pragma unroll
    for (int j = 0; j < 2; ++j) {
      sm[j] = ((e[j][0] + e[j][1]) + (e[j][2] + e[j][3])) + ((e[j][4] + e[j][5]) + (e[j][6] + e[j][7]));
      sm[j] = sw32(sm[j][0], sm[j][1]);
    }
    const f32x2 a = sw16(sm[0][0] + sm[0][1], sm[1][0] + sm[1][1]);
    float rc = __builtin_amdgcn_rcpf(a[0] + a[1]);
    rc *= nrm[q];
    bcast2(rc, sm[0], sm[1]);
    }
#pragma unroll
    for (int j = 0; j < 2; ++j) {
      unsigned pe[8];
#pragma unroll
      for (int i = 0; i < 8; ++i) if (NRM) e[j][i] *= sm[j];
#pragma unroll
      for (int i = 0; i < 4; ++i) { pe[i] = pk2(e[j][2 * i][0], e[j][2 * i + 1][0]); pe[4 + i] = pk2(e[j][2 * i][1], e[j][2 * i + 1][1]); }
      sink(2 * q + j, pe);
    }
  }
}

struct Sel { unsigned e[4], o[4]; };
DI Sel make_sel(int l) {
  Sel s;
#pragma unroll
  for (int q = 0; q < 4; ++q) {
    const bool on = ((l & 7) >> 1) == q;
    s.e[q] = on ? ((l & 1) ? 0x01000c0cu : 0x0c0c0100u) : 0x0c0c0c0cu;
    s.o[q] = on ? ((l & 1) ? 0x03020c0cu : 0x0c0c0302u) : 0x0c0c0c0cu;
  }
  return s;
}
DI void pin_sel(Sel& s, unsigned (&v)[2], unsigned& a, unsigned& c) {
  asm volatile("" : "+v"(s.e[0]), "+v"(s.e[1]), "+v"(s.e[2]), "+v"(s.e[3]), "+v"(s.o[0]), "+v"(s.o[1]), "+v"(s.o[2]), "+v"(s.o[3]), "+v"(v[0]), "+v"(v[1]), "+v"(a), "+v"(c));
}
DI f32x4 gemv2(unsigned xp, u32x4 w0, u32x4 w1, const Sel& s, f32x4 d) {
  const u32x4 a0 = {__builtin_amdgcn_perm(xp, xp, s.e[0]), __builtin_amdgcn_perm(xp, xp, s.e[1]), __builtin_amdgcn_perm(xp, xp, s.e[2]), __builtin_amdgcn_perm(xp, xp, s.e[3])};
  const u32x4 a1 = {__builtin_amdgcn_perm(xp, xp, s.o[0]), __builtin_amdgcn_perm(xp, xp, s.o[1]), __builtin_amdgcn_perm(xp, xp, s.o[2]), __builtin_amdgcn_perm(xp, xp, s.o[3])};
  return MFMA(as8(a1), as8(w1), MFMA(as8(a0), as8(w0), d));
}
DI u32x4 gemv_fin(f32x4 d, unsigned t12) {
  u32x4 v;
#pragma unroll
  for (int e = 0; e < 4; ++e) { const float t = ror8(d[e]); v[e] = tag12(d[e] + sw32(t, t)[1], t12); }
  return v;
}

DI void opair(u32x4 (&o)[2], unsigned v0, unsigned v1, const char* p) { GL4(o[0], v0, p, 0, P_SC1); GL4(o[1], v1, p, 0, P_SC1); }
template <int N, bool COLD = false> DI void ocheck(u32x4 (&o)[2], unsigned v0, unsigned v1, const char* p) {
  asm volatile("s_waitcnt vmcnt(%2)" : "+v"(o[0]), "+v"(o[1]) : "n"(N));
  if (COLD && __builtin_expect(__ballot(max(o[0][3], o[1][3]) == ~0u) == 0ull, 1)) return;
#pragma unroll 1
  for (unsigned spins = 0; spins < SPIN && __ballot(max(o[0][3], o[1][3]) == ~0u) != 0ull; ++spins)
    asm volatile("s_sleep 2\n\tglobal_load_dwordx4 %0, %2, %4 sc1\n\tglobal_load_dwordx4 %1, %3, %4 sc1\n\ts_waitcnt vmcnt(0)"
                 : "+v"(o[0]), "+v"(o[1]) : "v"(v0), "v"(v1), "s"(p) : "memory");
}
template <int N> DI void ocheck3(u32x4 (&a)[2], u32x4 (&b)[2], u32x4 (&c)[2], unsigned v0, unsigned v1, const char* pa, const char* pb, const char* pc) {
  asm volatile("s_waitcnt vmcnt(%2)" : "+v"(a[0]), "+v"(a[1]) : "n"(N));
  if (__builtin_expect(__ballot(max(a[0][3], a[1][3]) == ~0u) == 0ull, 1)) return;
  asm volatile("s_waitcnt vmcnt(0)" : "+v"(b[0]), "+v"(b[1]), "+v"(c[0]), "+v"(c[1]));
#pragma unroll 1
  for (unsigned spins = 0; spins < SPIN; ++spins) {
    const unsigned long long ma = __ballot(max(a[0][3], a[1][3]) == ~0u), mb = __ballot(max(b[0][3], b[1][3]) == ~0u), mc = __ballot(max(c[0][3], c[1][3]) == ~0u);
    if (ma == 0ull) break;
    asm volatile("s_mov_b64 exec, %11\n\tglobal_load_dwordx4 %0, %6, %8 sc1\n\tglobal_load_dwordx4 %1, %7, %8 sc1\n\t"
                 "s_mov_b64 exec, %12\n\tglobal_load_dwordx4 %2, %6, %9 sc1\n\tglobal_load_dwordx4 %3, %7, %9 sc1\n\t"
                 "s_mov_b64 exec, %13\n\tglobal_load_dwordx4 %4, %6, %10 sc1\n\tglobal_load_dwordx4 %5, %7, %10 sc1\n\ts_mov_b64 exec, -1\n\ts_waitcnt vmcnt(0)"
                 : "+v"(a[0]), "+v"(a[1]), "+v"(b[0]), "+v"(b[1]), "+v"(c[0]), "+v"(c[1])
                 : "v"(v0), "v"(v1), "s"(pa), "s"(pb), "s"(pc), "s"(ma), "s"(mb), "s"(mc) : "memory");
  }
}
DI void sw16u(unsigned& a, unsigned& b) { auto r = __builtin_amdgcn_permlane16_swap(a, b, false, false); a = r[0]; b = r[1]; }
template <int N> DI void waitl2(s16x4& a, s16x4& c) { asm volatile("s_waitcnt lgkmcnt(%2)" : "+v"(a), "+v"(c) : "n"(N)); }
template <int N> DI void tie4(f32x4 (&a)[4]) { asm volatile("s_nop %4" : "+v"(a[0]), "+v"(a[1]), "+v"(a[2]), "+v"(a[3]) : "n"(N)); }
DI void rowst(f32x2 (&m)[4], unsigned a) {
  asm volatile("ds_read2_b32 %0, %4 offset1:128\n\tds_read2_b32 %1, %4 offset0:16 offset1:144\n\tds_read2_b32 %2, %4 offset0:32 offset1:160\n\t"
               "ds_read2_b32 %3, %4 offset0:48 offset1:176\n\ts_waitcnt lgkmcnt(0)" : "=&v"(m[0]), "=&v"(m[1]), "=&v"(m[2]), "=&v"(m[3]) : "v"(a) : "memory");
}
DI void reload4c(u32x4 (&p)[4], unsigned o, const char* sb, unsigned long long m0, unsigned long long m1, unsigned long long m2, unsigned long long m3) {
  asm volatile("s_mov_b64 exec, %6\n\tglobal_load_dwordx4 %0, %4, %5 sc1\n\ts_mov_b64 exec, %7\n\tglobal_load_dwordx4 %1, %4, %5 offset:1024 sc1\n\t"
               "s_mov_b64 exec, %8\n\tglobal_load_dwordx4 %2, %4, %5 offset:2048 sc1\n\ts_mov_b64 exec, %9\n\tglobal_load_dwordx4 %3, %4, %5 offset:3072 sc1\n\t"
               "s_mov_b64 exec, -1\n\ts_waitcnt vmcnt(0)"
               : "+v"(p[0]), "+v"(p[1]), "+v"(p[2]), "+v"(p[3]) : "v"(o), "s"(sb), "s"(m0), "s"(m1), "s"(m2), "s"(m3) : "memory");
}

#define PL(r, o) "global_load_dwordx4 v[" #r ":" #r "+3], %[vo], %[sb] offset:" #o " sc1\n\t"
#define PC(r, m) "v_cmp_eq_u32 " m ", -1, v[" #r "+3]\n\t"
#define PM(d, r) "v_mov_b32 v[" #d "], v[" #r "]\n\tv_mov_b32 v[" #d "+1], v[" #r "+1]\n\tv_mov_b32 v[" #d "+2], v[" #r "+2]\n\tv_mov_b32 v[" #d "+3], v[" #r "+3]\n\t"
template <int D> DI void poll2(u32x4 (&p)[2], u32x4 (&q)[4], unsigned vo, const char* sb) {
  unsigned long long m; unsigned n = SPIN;
  asm volatile(PL(128, 0) PL(132, 1024) "s_sleep %[d]\n\t" PL(136, 0) PL(140, 1024)
               "1:\n\ts_waitcnt vmcnt(2)\n\t" PC(128, "vcc") PC(132, "%[m]") "s_or_b64 exec, vcc, %[m]\n\ts_cbranch_scc0 2f\n\t" PL(128, 0) PL(132, 1024)
               "s_mov_b64 exec, -1\n\ts_waitcnt vmcnt(2)\n\t" PC(136, "vcc") PC(140, "%[m]") "s_or_b64 exec, vcc, %[m]\n\ts_cbranch_scc0 3f\n\t" PL(136, 0) PL(140, 1024)
               "s_mov_b64 exec, -1\n\ts_sub_u32 %[n], %[n], 1\n\ts_cbranch_scc0 1b\n\ts_waitcnt vmcnt(0)\n"
               "3:\n\ts_mov_b64 exec, -1\n\t" PM(120, 136) PM(124, 140) "s_branch 4f\n"
               "2:\n\ts_mov_b64 exec, -1\n\t" PM(120, 128) PM(124, 132) "4:\n\ts_nop 1"
               : "={v[120:123]}"(p[0]), "={v[124:127]}"(p[1]), "=&{v[128:131]}"(q[0]), "=&{v[132:135]}"(q[1]), "=&{v[136:139]}"(q[2]), "=&{v[140:143]}"(q[3]),
                 [m] "=&s"(m), [n] "+s"(n)
               : [vo] "v"(vo), [sb] "s"(sb), [d] "n"(D) : "vcc", "scc", "memory");
}
#define PL4(r) PL(r, 0) PL(r+4, 1024) PL(r+8, 2048) PL(r+12, 3072)
#define PM2(o, r) "v_mov_b64 v[128+" #o ":129+" #o "], v[" #r "+" #o ":" #r "+1+" #o "]\n\t"
#define PM16(r) PM2(0, r) PM2(2, r) PM2(8, r) PM2(10, r) PM2(4, r) PM2(6, r) PM2(12, r) PM2(14, r)
#define PQ8(c) c "{v[96:99]}"(q[0]), c "{v[100:103]}"(q[1]), c "{v[104:107]}"(q[2]), c "{v[108:111]}"(q[3]), c "{v[112:115]}"(q[4]), c "{v[116:119]}"(q[5]), c "{v[120:123]}"(q[6]), c "{v[124:127]}"(q[7])
#define PB(a, r) "v_bitop3_b32 " a ", " a ", v[" #r "], %[t] bitop3:0xf6\n\t"
#define PB7(a, r) "v_xor_b32 " a ", %[t], v[" #r "]\n\t" PB(a, r+1) PB(a, r+2) PB(a, r+3) PB(a, r+4) PB(a, r+5) PB(a, r+6) PB(a, r+7)
#define PCT(r) "s_waitcnt vmcnt(4)\n\t" PB7("v128", r) PB7("v129", r+8) "v_and_b32 v128, 0xfff, v128\n\tv_and_or_b32 v128, v129, %[tm], v128\n\tv_cmp_ne_u32 vcc, 0, v128\n\ts_or_b64 exec, vcc, vcc\n\t"
template <int D> DI void pollt(u32x4 (&p)[4], u32x4 (&q)[8], unsigned vo, const char* sb, unsigned t, unsigned tm) {
  unsigned n = SPIN;
  asm volatile(PL4(96) "s_sleep %[d]\n\t" PL4(112)
               "1:\n\t" PCT(96) "s_cbranch_scc0 2f\n\t" PL4(96) "s_mov_b64 exec, -1\n\t" PCT(112) "s_cbranch_scc0 3f\n\t" PL4(112)
               "s_mov_b64 exec, -1\n\ts_sub_u32 %[n], %[n], 1\n\ts_cbranch_scc0 1b\n\ts_waitcnt vmcnt(0)\n"
               "3:\n\ts_mov_b64 exec, -1\n\t" PM16(112) "s_branch 4f\n"
               "2:\n\ts_mov_b64 exec, -1\n\t" PM16(96) "4:\n\ts_nop 1"
               : "=&{v[128:131]}"(p[0]), "=&{v[132:135]}"(p[1]), "=&{v[136:139]}"(p[2]), "=&{v[140:143]}"(p[3]), PQ8("=&"), [n] "+s"(n)
               : [vo] "v"(vo), [sb] "s"(sb), [t] "s"(t), [tm] "s"(tm), [d] "n"(D) : "vcc", "scc", "memory");
}
#define PKEEP(pl) "v"(pl[0]), "v"(pl[1]), "v"(pl[2]), "v"(pl[3]), "v"(pl[4]), "v"(pl[5]), "v"(pl[6]), "v"(pl[7])
extern "C" __global__ __launch_bounds__(768) __attribute__((aligned(4096))) void kf_main(const u16* __restrict__ x, const u16* __restrict__ wqkv, char* ws, const u16* __restrict__ w_r,
                                               const u16* __restrict__ wphi, const u16* __restrict__ wo, const u16* __restrict__ w_gate,
                                               const u16* __restrict__ w_up, const u16* __restrict__ w_down, u16* __restrict__ out) {
  extern __shared__ __attribute__((aligned(16))) char smem[];
  const int b = blockIdx.x;
  const int xcd = b & 7, jj = b >> 3;
  const int panel = (xcd >> 2) * 8 + (jj & 7), hp = (xcd & 3) * 4 + (jj >> 3);
  const int tid = threadIdx.x;
  const int w = __builtin_amdgcn_readfirstlane(tid >> 6), lw = w & 3;
  int l = tid & 63, r15 = l & 15, g = l >> 4, tq = r15 >> 2, tp = l & 3;
  unsigned xo;
  auto relane = [&]() {
    asm volatile("v_mbcnt_lo_u32_b32 %0, -1, 0\n\tv_mbcnt_hi_u32_b32 %0, -1, %0" : "=v"(l));
    r15 = l & 15; g = l >> 4; tq = r15 >> 2; tp = l & 3; xo = r15 * 2048u + g * 16u;
  };
  const unsigned lbase = LDS_OFF(smem);
  auto pf3 = [&](int half) {
    touch(l * 6144u, (const char*)wqkv + (size_t)(half * 512 + (jj & 7) * 64) * 6144 + (jj >> 3) * 2048 + (xcd & 3) * 512 + 384, lbase + L_DU);
  };
  const char* pc0; unsigned clen;
  asm volatile("s_getpc_b64 %0\n.Lkf_here:\n\ts_mov_b32 %1, ((.Lkf_last-kf_main)|4095)-3-(.Lkf_here-kf_main)\n\ts_max_u32 %1, %1, ((.Lkf_cold-kf_main)|4095)-3-(.Lkf_here-kf_main)"
               : "=s"(pc0), "=s"(clen) :: "scc");
  if (__builtin_expect(w < 4, 1)) {
    if (__builtin_expect(w >= 2, 1)) {
      touch(min(((jj * 2 + w) & 15) * 4096u + l * 64u, clen), pc0, lbase + L_DU);
    }
    if (__builtin_expect(w == 0 && !(xcd & 1) && jj < 24, 1)) pf3(xcd >> 2);
  }
  unsigned* flg = (unsigned*)(smem + L_F);
  const bf16x8 ones = {0x3f80, 0x3f80, 0x3f80, 0x3f80, 0x3f80, 0x3f80, 0x3f80, 0x3f80};
  const f32x4 z4 = {0.f, 0.f, 0.f, 0.f};
  const int row0 = panel * 128 + hp * 8;
  auto ptile = [&](int p) { return p == 0 ? hp : p <= 12 ? ((hp | 3) + p) & 15 : hp ^ (p - 12); };
  auto kvo = [&]() { return (l >> 3) * 2048u + (l & 7) * 16u + ((panel ^ (l >> 3)) & 15) * 128u; };
  if (__builtin_expect((w >> 2) == 1, 1)) {
    const int srow = l >> 3, sch = l & 7, cg = sch, rg = srow;
    const int kt0 = 4 * (panel >> 2);
    unsigned G, Tw, t12;
    unsigned voA[2], voW[2];
    const char* sA = (const char*)(x + (size_t)panel * 128 * DM);
    const char* sQ = (const char*)(wqkv + hp * 64);
#pragma unroll
    for (int i = 0; i < 2; ++i) {
      const int row = lw * 32 + i * 8 + srow, wr = lw * 16 + i * 8 + srow;
      voA[i] = (unsigned)(row * 2048 + ((sch ^ ((row >> 1) & 7)) * 16));
      voW[i] = (unsigned)(wr * 6144 + ((sch ^ ((((wr >> 1) & 1) << 2) | (((wr >> 3) & 1) << 1))) * 16));
    }
    auto stage = [&](int S) {
      const int kt = (S + kt0) & 15;
      const char* a = sA + kt * 128; const char* wv = sQ + (size_t)kt * 64 * 6144;
      const unsigned d = lbase + (S % 3) * SLOT;
#pragma unroll
      for (int i = 0; i < 4; ++i) glds(voA[i & 1], a + (i >> 1) * 32768, d + lw * 4096 + i * 1024);
#pragma unroll
      for (int i = 0; i < 6; ++i) glds(voW[i & 1], wv + ((i >> 1) == 2 ? 0 : ((i >> 1) + 1) * 2048), d + ABYTES + (i >> 1) * WSLAB + lw * 2048 + (i & 1) * 1024);
    };
    auto last = [&](int i) {
      const int kt = (15 + kt0) & 15;
      const char* a = sA + kt * 128; const char* wv = sQ + (size_t)kt * 64 * 6144;
      if (i < 4) glds(voA[i & 1], a + (i >> 1) * 32768, lbase + L_EX + lw * 4096 + i * 1024);
      else if (i < 8) glds(voW[i & 1], wv + ((i >> 1) - 1) * 2048, lbase + L_EW + ((i >> 1) - 2) * WSLAB + lw * 2048 + (i & 1) * 1024);
      else glds(voW[i & 1], wv, lbase + ABYTES + 2 * WSLAB + lw * 2048 + (i & 1) * 1024);
    };
    __builtin_amdgcn_s_setprio(3);
    if (jj & 1) {
      const char* a = sA + kt0 * 128; const char* wv = sQ + (size_t)kt0 * 64 * 6144;
#pragma unroll
      for (int i = 0; i < 6; ++i) glds(voW[i & 1], wv + ((i >> 1) == 2 ? 0 : ((i >> 1) + 1) * 2048), lbase + ABYTES + (i >> 1) * WSLAB + lw * 2048 + (i & 1) * 1024);
#pragma unroll
      for (int i = 0; i < 4; ++i) glds(voA[i & 1], a + (i >> 1) * 32768, lbase + lw * 4096 + i * 1024);
    } else stage(0);
    stage(1);
    GL1(G, 0u, (const char*)ws, 0);
    const bool gu = (hp & 7) != 3;
    const int bb = gu ? (hp - (hp > 3) - (hp > 11)) * 16 + panel : 0;
    const int ke = bb & 7, cb = ((bb >> 3) * 2 + (lw & 1)) ^ ((lw >> 1) * 4);
    const int kc = (bb >> 4) * 4 + ((bb >> 1) & 3), cq = ((bb >> 3) & 1) | ((bb & 1) << 1);
    u32x4 gv[16], dv[8];
    const uintptr_t gb = (uintptr_t)(((lw >> 1) ? w_up : w_gate) + (size_t)(ke * 128) * FF + cb * 64);
    const u32x4 grs = {(unsigned)gb, (unsigned)(gb >> 32) & 0xffffu, gu ? 127u * FF * 2 + 128u : 0u, 0x00020000u};
    const unsigned go = rg * (16u * FF * 2) + cg * 16u;
    const uintptr_t db = (uintptr_t)(w_down + (size_t)(kc * 64) * DM + cq * 256 + lw * 64);
    const u32x4 drs = {(unsigned)db, (unsigned)(db >> 32) & 0xffffu, gu ? 63u * DM * 2 + 128u : 0u, 0x00020000u};
    const unsigned dof = rg * 16384u + cg * 16u;
    auto strm = [&](auto I0, auto I1) {
      static_for<(decltype(I1)::value > decltype(I0)::value ? decltype(I1)::value - decltype(I0)::value : 0)>([&](auto I) {
        constexpr int i = decltype(I)::value + decltype(I0)::value;
        if constexpr (i < 16) bl4nt(gv[i], go, grs, i * (FF * 2u)); else bl4nt(dv[i - 16], dof, drs, (i - 16) * 2048u);
      });
    };
    const int wrow = 8 * g + tq, wsw = (((wrow >> 1) & 1) << 2) | (((wrow >> 3) & 1) << 1);
    const unsigned csa = lbase + ABYTES + (lw < 3 ? lw : 0) * WSLAB + wrow * 128 + (((2 * (panel & 3) + (tp >> 1)) ^ wsw) * 16) + 8 * (tp & 1);
    waitv<11>();
    RAW_BARRIER();
    float mean = 0.f, var = 1.f, c0 = 0.f, c1 = 0.f;
    f32x4 sm = z4;
    u32x4 pv; u32x2 pw; unsigned po, pc; unsigned long long pm;
    u32x4 ga[2], ca;
    const char* gab = ws + (lw < 3 ? WS_CS + (hp * 3 + lw) * 2048 : WS_ST + panel * 1024 - 1024);
    const unsigned go0 = l * 16u + (lw < 3 ? 0u : 1024u);
    Sel sl;
    unsigned voO[2], guo, yo;
    auto later = [&]() {
      sl = make_sel(l);
#pragma unroll
      for (int i = 0; i < 2; ++i) voO[i] = (unsigned)((lw * 16 + i * 8 + srow) * 2048 + (sch ^ (((srow >> 1) & 1) | ((((srow >> 2) ^ i) & 1) << 2))) * 16);
      guo = WS_GU + (cb * 16 + (lw >> 1) * 8 + ke) * 256 + cg * 32 + g * 16;
      yo = WS_Y + ((cq * 4 + lw) * 56 + kc) * 256 + cg * 32 + g * 16;
      pin_sel(sl, voO, guo, yo);
    };
    static_for<13>([&](auto I) {
      constexpr int S = decltype(I)::value - 1;
      if constexpr (S >= 0) {
        waitv<10 + n_x(S - 2) + n_x(S - 1)>();
        RAW_BARRIER();
      }
      if constexpr (S < 3) {
        u32x2 c[4];
        cs_read(c, csa + ((S + 1) % 3) * SLOT);
        stage(S + 3);
        cs_sum(c, c0, c1);
      } else stage(S + 3);
      if constexpr (S == 0) {
        asm volatile("" : "+v"(G));
        Tw = __builtin_amdgcn_readfirstlane(G) + 1u;
        if ((Tw & 0xfffu) == 0u) ++Tw;
        t12 = Tw & 0xfffu;
        if (tid == 256) flg[F_T] = Tw;
      }
      if constexpr (S == 1) later();
      if constexpr (S == SB0) {
        const f32x4* rp = (const f32x4*)(smem + L_REC) + (l & 7);
        sm = ((rp[0] + rp[8]) + (rp[16] + rp[24])) + ((rp[32] + rp[40]) + (rp[48] + rp[56]));
        pinf(sm);
      }
      if constexpr (S == SB0 + 1) {
        mean = sm[0] * (1.0f / 1024.0f);
        var = __builtin_fmaf(-mean, mean, sm[2] * (1.0f / 1024.0f));
        if (lw == 3 && __ballot(!(var * 64.0f >= mean * mean)) != 0ull) {
#pragma unroll 1
          for (int r = 0; r < 8; ++r) {
            const float m = __builtin_bit_cast(float, __builtin_amdgcn_readlane(__builtin_bit_cast(int, mean), r));
            const u32x4* p = (const u32x4*)(x + (size_t)(row0 + r) * DM) + l * 2;
            const u32x4 a = p[0], c = p[1];
            float q = 0.f;
#pragma unroll
            for (int i = 0; i < 8; ++i) {
              const unsigned u = i < 4 ? a[i & 3] : c[i & 3];
              const float e0 = bflo(u) - m, e1 = bfhi(u) - m;
              q = __builtin_fmaf(e0, e0, q); q = __builtin_fmaf(e1, e1, q);
            }
            q = wave_total(q) * (1.0f / 1024.0f);
            if ((l & 7) == r) var = q;
          }
        }
        mean = rbf(mean); var = rbf(rbf(var) + 1e-5f);
        pin1(mean); pin1(var);
      }
      if constexpr (S == SB0 + 1) { var = rbf(sqrtf(var)); pin1(var); }
      if constexpr (S == SB0 + 1) { sm[1] = rbf(__builtin_fmaf(-mean, sm[3], sm[1]) / var); pinf(sm); }
      if constexpr (S == SB0 + 1) {
        if (lw == 3) {
          const float r = sm[1];
          const unsigned u = __float_as_uint(r);
          const unsigned key = (r == r ? ((u & 0x80000000u) ? (~u & 0xffff0000u) : (u | 0x80000000u)) : 0u) | (unsigned)(2047 - row0 - (l & 7));
          const unsigned km = dpp_umax<0x141>(dpp_umax<0x4e>(dpp_umax<0xb1>(key)));
          pv = u32x4{__float_as_uint(mean) | (__float_as_uint(var) >> 16), key, Tw, Tw};
          po = WS_ST + (row0 + l) * 8;
          pm = __ballot(key == km) & 0xffull;
        } else {
          pv = u32x4{__float_as_uint(sum_g(c0 + c1)), 0u, Tw, Tw};
          po = WS_CS + (((hp * 3 + lw) * 4 + (panel >> 2)) * 64 + (panel & 3) * 16 + l) * 8;
          pm = 0ull;
        }
        pw = u32x2{pv[0], Tw};
        pc = WS_CA + (panel * 16 + hp) * 16;
        pub_pin(pv, pw, po, pc);
      }
      if constexpr (S == PUB) pub(pv, pw, po, pc, lw == 3 ? 0xffull : 0xffffull, pm, ws);
      if constexpr (S == GAS) { GL4(ga[0], go0, gab, 0, P_SC1); GL4(ga[1], l * 16u, gab, 1024, P_SC1); }
      static_for<8>([&](auto J) { if constexpr (ES0 + decltype(J)::value * ESL / 8 == S) last(decltype(J)::value); });
      if constexpr (S == CAS) GL4(ca, WS_CA + lw * 1024 + l * 16, ws, 0, P_SC1);
      if constexpr (S == CTS) touch4(min(((jj * 4 + lw) * 4 + l) * 128u, clen), pc0, lbase + L_DU);
      if constexpr (S == 0) glds(l * 16u, (const char*)(wphi + hp * 2048 + lw * 512), lbase + L_WP + lw * 1024);
      if constexpr (S == LG1) { GAP(1); VISIT(1); }
      if constexpr (S == LM1) PAD(1);
    });
    const unsigned fa = lbase + L_F, fb = fa + lw * 4;
    auto ca_bad = [&]() { return (ca[2] ^ Tw) | (ca[3] ^ Tw); };
    auto ca_put = [&]() {
      unsigned m;
#define MX(a, c) "v_max_u32_dpp %0, " a ", " a " " c " row_mask:0xf bank_mask:0xf\n\ts_nop 1\n\t"
      asm volatile(MX("%3", "quad_perm:[1,0,3,2]") MX("%0", "quad_perm:[2,3,0,1]") MX("%0", "row_half_mirror") "v_max_u32_dpp %0, %0, %0 row_mirror row_mask:0xf bank_mask:0xf\n\t"
                   "v_cmp_eq_u32 vcc, %0, %3\n\ts_mov_b64 exec, vcc\n\tds_max_u64 %1, %2 offset:%4\n\ts_mov_b64 exec, -1"
                   : "=&v"(m) : "v"(fa), "v"(u32x2{ca[0], ca[1]}), "v"(ca[1]), "n"(F_CA * 4) : "vcc", "memory");
      raise<F_SD>(fb);
    };
    asm volatile("s_waitcnt vmcnt(%1)" : "+v"(ca) : "n"(10 + n_x(10) + n_x(11)) : "memory");
    RAW_BARRIER();
    last(8); last(9);
    strm(IC(0), IC(NF));
    const bool cok = all0(ca_bad());
    if (__builtin_expect(cok, 1)) ca_put();
    auto ga_bad = [&]() { return (ga[0][1] ^ Tw) | (ga[0][3] ^ Tw) | (ga[1][1] ^ Tw) | (ga[1][3] ^ Tw); };
    auto ga_put = [&]() {
      if (lw < 3) {
        const float u0 = __uint_as_float(ga[0][0]) + __uint_as_float(ga[1][0]), u1 = __uint_as_float(ga[0][2]) + __uint_as_float(ga[1][2]);
        const f32x2 a = sw32(u0, u1);
        ((float*)(smem + L_C))[lw * 64 + (l & 31) * 2 + (l >> 5)] = a[0] + a[1];
      } else {
        *(f32x2*)(smem + L_MN + l * 8) = f32x2{-bfhi(ga[0][0]), -bfhi(ga[0][2])};
        *(f32x2*)(smem + L_RS + l * 8) = f32x2{__builtin_amdgcn_rcpf(bflo(ga[0][0])), __builtin_amdgcn_rcpf(bflo(ga[0][2]))};
        *(unsigned*)(smem + L_DN + l * 4) = (ga[0][0] & 0xffffu) | (ga[0][2] << 16);
      }
      raise<F_ST>(fb);
    };
    asm volatile("s_waitcnt vmcnt(%2)" : "+v"(ga[0]), "+v"(ga[1]) : "n"(2 + NF) : "memory");
    RAW_BARRIER();
    u32x4 fs; u32x2 sel;
    asm volatile("ds_read_b128 %0, %2 offset:%3\n\tds_read_b64 %1, %2 offset:%4" : "=&v"(fs), "=&v"(sel) : "v"(fa), "n"(F_SD * 4), "n"(F_CA * 4) : "memory");
    strm(IC(NF), IC(NF + NG));
    if (__builtin_expect(!all0(ga_bad()), 0))
#pragma unroll 1
      for (unsigned spins = 0; spins < SPIN; ++spins) {
        asm volatile("global_load_dwordx4 %0, %2, %4 sc1\n\tglobal_load_dwordx4 %1, %3, %4 offset:1024 sc1\n\ts_waitcnt vmcnt(0)"
                     : "+v"(ga[0]), "+v"(ga[1]) : "v"(go0), "v"(l * 16u), "s"(gab) : "memory");
        if (all0(ga_bad())) break;
      }
    ga_put();
    asm volatile("s_waitcnt lgkmcnt(0)" : "+v"(fs), "+v"(sel) :: "memory");
    if (__builtin_expect(!__builtin_amdgcn_readfirstlane(fs[0] & fs[1] & fs[2] & fs[3]), 0)) {
      if (!cok) {
#pragma unroll 1
        for (unsigned spins = 0; spins < SPIN; ++spins) {
          asm volatile("global_load_dwordx4 %0, %1, %2 sc1\n\ts_waitcnt vmcnt(0)" : "+v"(ca) : "v"(WS_CA + lw * 1024 + l * 16), "s"(ws) : "memory");
          if (all0(ca_bad())) break;
        }
        ca_put();
      }
      lds_wait4(flg + F_SD);
      asm volatile("ds_read_b64 %0, %1 offset:%2\n\ts_waitcnt lgkmcnt(0)" : "=v"(sel) : "v"(fa), "n"(F_CA * 4) : "memory");
    }
    const unsigned key = __builtin_amdgcn_readfirstlane(sel[1]);
    const int idx = 2047 - (int)(key & 2047u);
    unsigned xd;
    GL1P(xd, l * 4u, (const char*)(x + (size_t)idx * DM + ke * 128));
    strm(IC(NF + NG), IC(24));
    waitv<25>();
    raise<F_GO>(fb);
    __builtin_amdgcn_s_setprio(FP0);
    auto wotile = [&](int p) {
      const char* wv = (const char*)(wo + (size_t)(ptile(p) * 64) * DM + hp * 64);
      waitv<WNO>();
      glds(voO[0], wv, lbase + p * 8192 + lw * 2048); glds(voO[1], wv, lbase + p * 8192 + lw * 2048 + 1024);
    };
    asm volatile("s_waitcnt vmcnt(8)" : "+v"(xd), "+v"(gv[0]), "+v"(gv[1]), "+v"(gv[2]), "+v"(gv[3]), "+v"(gv[4]), "+v"(gv[5]), "+v"(gv[6]), "+v"(gv[7]) :: "memory");
    asm volatile("" : "+v"(gv[8]), "+v"(gv[9]), "+v"(gv[10]), "+v"(gv[11]), "+v"(gv[12]), "+v"(gv[13]), "+v"(gv[14]), "+v"(gv[15]));
    char* hs = smem + L_H + lw * 256;
    {
      const float sm = bfhi(sel[0]), rden = __builtin_amdgcn_rcpf(bflo(sel[0]));
      const unsigned d = pk2(bflo(xd) - sm, bfhi(xd) - sm);
      *(unsigned*)(hs + l * 4) = pk2(bflo(d) * rden, bfhi(d) * rden);
      const u32x4 x0 = *(const u32x4*)(hs + rg * 32), x1 = *(const u32x4*)(hs + rg * 32 + 16);
      f32x4 d0 = gemv2(x0[0], gv[0], gv[1], sl, z4), d1 = gemv2(x0[1], gv[2], gv[3], sl, z4);
      d0 = gemv2(x0[2], gv[4], gv[5], sl, d0); d1 = gemv2(x0[3], gv[6], gv[7], sl, d1);
      d0 = gemv2(x1[0], gv[8], gv[9], sl, d0); d1 = gemv2(x1[1], gv[10], gv[11], sl, d1);
      d0 = gemv2(x1[2], gv[12], gv[13], sl, d0); d1 = gemv2(x1[3], gv[14], gv[15], sl, d1);
      const u32x4 v = gemv_fin(d0 + d1, t12);
      if (gu && (l & 40) == 0) gs4(guo, ws, v);
    }
    __builtin_amdgcn_s_setprio(FPR);
    lds_wait4(flg + F_LD);
    wotile(12); wotile(13); wotile(15);
    lds_wait4(flg + F_IM);
    wotile(0); wotile(1); wotile(2); wotile(3); wotile(7); wotile(8);
    lds_wait4(flg + F_BL);
    u32x4 p[4];
    const char* gp = ws + WS_GU + kc * 4096;
    lds_wait1(flg + F_SL + lw);
    wotile(4); wotile(5); wotile(6); wotile(9); wotile(10); wotile(11); wotile(14);
    GL4(p[0], l * 16u, gp, 0, P_SC1); GL4(p[1], l * 16u, gp, 1024, P_SC1); GL4(p[2], l * 16u, gp, 2048, P_SC1); GL4(p[3], l * 16u, gp, 3072, P_SC1);
    waitv<4>();
    lds_set(flg + F_WO + lw, 1u, l);
    if (FPR != FPE) __builtin_amdgcn_s_setprio(FPE);
    waitv4<0>(p);
    asm volatile("" : "+v"(dv[0]), "+v"(dv[1]), "+v"(dv[2]), "+v"(dv[3]), "+v"(dv[4]), "+v"(dv[5]), "+v"(dv[6]), "+v"(dv[7]) :: "memory");
    if (gu) {
#pragma unroll 1
      for (unsigned spins = 0; spins < SPIN; ++spins) {
        const unsigned long long m0 = __ballot(bad4(p[0], t12) & 0xfffu), m1 = __ballot(bad4(p[1], t12) & 0xfffu), m2 = __ballot(bad4(p[2], t12) & 0xfffu),
                                 m3 = __ballot(bad4(p[3], t12) & 0xfffu);
        if ((m0 | m1 | m2 | m3) == 0ull) break;
        reload4c(p, l * 16u, gp, m0, m1, m2, m3);
      }
      float hv[4];
#pragma unroll
      for (int e = 0; e < 4; ++e) {
        const float gg = rbf(sum_g(untag12(p[0][e]) + untag12(p[1][e]))), uu = rbf(sum_g(untag12(p[2][e]) + untag12(p[3][e])));
        hv[e] = rbf(silu_r(gg)) * uu;
      }
      if (l < 16) *(u32x2*)(hs + l * 8) = u32x2{pk2(hv[0], hv[1]), pk2(hv[2], hv[3])};
      const u32x4 hp4 = *(const u32x4*)(hs + rg * 16);
      f32x4 d0 = gemv2(hp4[0], dv[0], dv[1], sl, z4), d1 = gemv2(hp4[1], dv[2], dv[3], sl, z4);
      d0 = gemv2(hp4[2], dv[4], dv[5], sl, d0); d1 = gemv2(hp4[3], dv[6], dv[7], sl, d1);
      const u32x4 v = gemv_fin(d0 + d1, t12);
      if ((l & 40) == 0) gs4(yo, ws, v);
    }
    if (FPG != FPE) __builtin_amdgcn_s_setprio(FPG);
    if ((idx >> 7) == panel) {
      const char* yb = ws + WS_Y + hp * 56 * 256 + lw * 4096;
      u32x2 xr;
      GL2(xr, r15 * 8u, (const char*)(x + (size_t)idx * DM + hp * 64), 0);
      const unsigned tm = lw < 3 ? 0xfffu : 0u;
      u32x4 pl[8];
      lds_wait4(flg + F_OS);
      if (PYD) __builtin_amdgcn_s_sleep(PYD);
      pollt<PPY>(p, pl, l * 16u, yb, t12, tm);
      asm volatile("" : "+v"(xr));
      f32x4 y;
#pragma unroll
      for (int e = 0; e < 4; ++e) y[e] = sum_g((untag12(p[0][e]) + untag12(p[1][e])) + (lw < 3 ? untag12(p[2][e]) + untag12(p[3][e]) : 0.f));
      if (lw) {
        if (l < 16) *(f32x4*)(hs + l * 16) = y;
        lds_set(flg + F_AL + lw, 1u, l);
      } else {
      if (l == 0) flg[F_AL] = 1u;
      lds_wait4(flg + F_AL);
      y = (y + *(const f32x4*)(hs + 256 + r15 * 16)) + (*(const f32x4*)(hs + 512 + r15 * 16) + *(const f32x4*)(hs + 768 + r15 * 16));
      const int row = idx & 127;
      const float mn = ((const float*)(smem + L_MN))[row], rs = ((const float*)(smem + L_RS))[row];
      const float r = __uint_as_float((key & 0x80000000u) ? (key & 0x7fff0000u) : (~key & 0xffff0000u));
      const float sg = rbf(1.0f / (1.0f + __expf(-r)));
      float gi[4];
#pragma unroll
      for (int e = 0; e < 4; ++e) {
        const float xe = (e & 1) ? bfhi(xr[e >> 1]) : bflo(xr[e >> 1]);
        gi[e] = silu_r(rbf(rbf(rbf(xe + mn) * rs) + rbf(rbf(y[e]) * sg)));
      }
      if (l < 16) *(u32x2*)(smem + L_GI + l * 8) = u32x2{pk2(gi[0], gi[1]), pk2(gi[2], gi[3])};
      lds_set(flg + F_GT, 1u, l);
      }
      asm volatile("s_waitcnt vmcnt(0)" :: PKEEP(pl) : "memory");
    }
    if (b == 0 && tid == 256) gs1(0u, ws, Tw);
    GAP(2);
    return;
  }
  const int s = w >> 3;
  const int wt = lw & 1, wc = lw >> 1;
  const char* ob = ws + WS_O + (size_t)(panel * 4 + lw) * 65536;
  const char* xfb = (const char*)(x + (size_t)(panel * 128 + lw * 32 + s * 16) * DM + hp * 64);
  u32x4 xv[2], gq[2];
  unsigned T;
  auto gate = [&]() {
    const float mn = ((const float*)(smem + L_MN))[lw * 32 + s * 16 + r15], rs = ((const float*)(smem + L_RS))[lw * 32 + s * 16 + r15];
#pragma unroll
    for (int hf = 0; hf < 2; ++hf) {
      unsigned x0 = xv[hf][0], x1 = xv[hf][1], x2 = xv[hf][2], x3 = xv[hf][3];
      sw16u(x0, x2); sw16u(x1, x3);
      xv[hf] = u32x4{x0, x1, x2, x3};
#pragma unroll
      for (int e = 0; e < 4; ++e) {
        const unsigned d = pk2(bflo(xv[hf][e]) + mn, bfhi(xv[hf][e]) + mn);
        const unsigned y = pk2(bflo(d) * rs, bfhi(d) * rs);
        gq[hf][e] = pk2(silu_r(bflo(y)), silu_r(bfhi(y)));
      }
    }
    pin2(gq); pin2(xv);
  };
  const int wrow = 8 * g + tq, wsw = (((wrow >> 1) & 1) << 2) | (((wrow >> 3) & 1) << 1);
  unsigned asw[3][2], wa[3][2];
#pragma unroll
  for (int q = 0; q < 3; ++q) {
#pragma unroll
    for (int ks = 0; ks < 2; ++ks) asw[q][ks] = lbase + (wt * 64 + r15) * 128 + (((4 * ks + g) ^ ((r15 >> 1) & 7)) * 16) + q * SLOT;
#pragma unroll
    for (int c = 0; c < 2; ++c) wa[q][c] = lbase + ABYTES + wrow * 128 + (((wc * 4 + 2 * c + (tp >> 1)) ^ wsw) * 16) + 8 * (tp & 1) + q * SLOT;
  }
  if (s) {
    u32x4 xa[4], wr[4];
    auto st_load = [&]() {
      const char* xb = (const char*)(x + (size_t)row0 * DM + lw * 256);
      const unsigned o = (l & 7) * 2048u + ((l >> 3) & 1) * 64u + g * 16u;
      GL4(xa[0], o, xb, 0, P_DEF); GL4(xa[1], o, xb, 128, P_DEF); GL4(xa[2], o, xb, 256, P_DEF); GL4(xa[3], o, xb, 384, P_DEF);
      const char* rb = (const char*)(w_r + lw * 256);
      const unsigned ro = (l & 1) * 64u + g * 16u;
      GL4(wr[0], ro, rb, 0, P_DEF); GL4(wr[1], ro, rb, 128, P_DEF); GL4(wr[2], ro, rb, 256, P_DEF); GL4(wr[3], ro, rb, 384, P_DEF);
    };
    auto st_sum = [&](auto NV) {
      waitv4<decltype(NV)::value>(xa); waitv4<decltype(NV)::value>(wr);
      f32x4 d0 = z4, d1 = z4, d2 = z4, d3 = z4;
#pragma unroll
      for (int j = 0; j < 4; ++j) {
        d0 = MFMA(ones, as8(xa[j]), d0); d1 = MFMA(as8(wr[j]), as8(xa[j]), d1);
        d2 = MFMA(as8(xa[j]), as8(xa[j]), d2); d3 = MFMA(as8(wr[j]), ones, d3);
      }
      const bool hf = l & 8;
      const float q2 = (l & 2) ? ((l & 1) ? d2[3] : d2[2]) : ((l & 1) ? d2[1] : d2[0]);
      if (g == tq) ldsw1(lbase + L_REC + (lw * 16 + r15) * 16, __builtin_bit_cast(u32x4, f32x4{d0[0], hf ? d1[1] : d1[0], q2, hf ? d3[1] : d3[0]}));
      SB();
    };
    f32x4 acc[4][2];
    bf16x8 w0, w1;
    float sc[4];
    const char* kb = ws + WS_KVR + hp * 8192 + wc * 4096;
    unsigned l16, fsl = LDS_OFF(flg + F_SL + 2 * wc);
    {
#pragma unroll
      for (int t = 0; t < 4; ++t) { acc[t][0] = z4; acc[t][1] = z4; }
      int ts = (hp - 4 * (panel >> 2)) & 15;
      const unsigned xr = lbase + (ts == 15 ? L_EX : (ts % 3) * SLOT) + (lw * 32 + 16 + r15) * 128 + ((g ^ ((r15 >> 1) & 7)) * 16);
      ts = min(ts, 13);
      Frag<2> f[2];
      auto rd = [&](Frag<2>& y, auto I, auto OW, unsigned pa, const unsigned (&pw)[2]) {
        constexpr int i = decltype(I)::value, ow = decltype(OW)::value + 2 * WSLAB;
        if constexpr (i < 4) ds_128<i * 2048>(y.af[i], pa);
        else if constexpr (i & 1) ds_tr<ow + 512>(y.wh[(i - 4) >> 1], pw[(i - 4) >> 1]);
        else ds_tr<ow>(y.wl[(i - 4) >> 1], pw[(i - 4) >> 1]);
      };
      auto step = [&](Frag<2>& c, Frag<2>& y, auto OW, unsigned pa, const unsigned (&pw)[2], auto RD, auto XT) {
        static_for<8>([&](auto I) {
          constexpr int i = decltype(I)::value;
          acc[i & 3][i >> 2] = MFMA(cat8(c.wl[i >> 2], c.wh[i >> 2]), c.af[i & 3], acc[i & 3][i >> 2]);
          SB();
          if constexpr (i < 4 && decltype(RD)::value) { rd(y, IC(2 * i), OW, pa, pw); rd(y, IC(2 * i + 1), OW, pa, pw); SB(); }
          if constexpr (i == 4 && decltype(XT)::value >= 0) { ds_if<decltype(XT)::value>(xv, xr, ts); SB(); }
        });
      };
      auto tile = [&](auto J, auto BAR, auto RD, auto&& side) {
        constexpr int j = decltype(J)::value % 3, n = (j + 1) % 3;
        f[0].wait();
        step(f[0], f[1], IC(4096), asw[j][1], wa[j], IC(1), IC(decltype(BAR)::value ? decltype(J)::value : -1));
        f[1].wait();
        if constexpr (decltype(BAR)::value) { __builtin_amdgcn_s_barrier(); asm volatile("" ::: "memory"); }
        side();
        if constexpr (!decltype(RD)::value) { if (l == 0) __hip_atomic_store(flg + F_BL + lw, 1u, __ATOMIC_RELAXED, __HIP_MEMORY_SCOPE_WORKGROUP); SB(); }
        step(f[1], f[0], IC(0), asw[n][0], wa[n], RD, IC(-1));
      };
      auto sents = [&]() {
        unsigned ln;
        asm volatile("v_mbcnt_lo_u32_b32 %0, -1, 0\n\tv_mbcnt_hi_u32_b32 %0, -1, %0" : "=v"(ln));
        u32x4 ff = {~0u, ~0u, ~0u, ~0u};
        asm volatile("" : "+v"(ff));
        const unsigned ko = (ln >> 3) * 2048u + (ln & 7) * 16u + ((panel ^ (ln >> 3)) & 15) * 128u;
        static_for<4>([&](auto J) { gs4o<decltype(J)::value * 1024>(ln * 16u, ws + WS_O + (size_t)(panel * 4 + lw) * 65536 + hp * 4096, ff); });
        gs4(ko, ws + WS_KVP + (hp * 4 + lw) * 32768, ff);
        gs4(ko ^ 17408u, ws + WS_KVP + (hp * 4 + lw) * 32768, ff);
        gs4m(ln * 16u, ws + WS_KVR + hp * 8192 + lw * 2048 + panel * 128, ff);
        SB();
      };
      asm volatile("" : "=v"(xv[0]), "=v"(xv[1]));
      TICK();
      static_for<8>([&](auto I) { rd(f[0], I, IC(0), asw[0][0], wa[0]); });
      static_for<13>([&](auto J) {
        constexpr int t = decltype(J)::value;
        if constexpr (t == 0) st_load();
        if constexpr (t == SBP) { waitv<0>(); SB(); }
        tile(J, IC(1), IC(1), [&]() {
          if constexpr (t == SBI) {
            __builtin_amdgcn_s_setprio(3);
            sents();
            __builtin_amdgcn_s_setprio(0);
            SB();
          }
        });
        if constexpr (t == 1) {
          st_sum(IC(0));
        }
        if constexpr (t == 1) { if (lw == 0 && !(xcd & 1) && jj < 24) pf3((xcd >> 2) ^ 1); }
        if constexpr (t == 1) {
          if (lw & 1) touch((l >> 2) * 2048u + (l & 3) * 128u, (const char*)wo + jj * 65536 + (lw >> 1) * 32768 + (xcd & 3) * 512, lbase + L_DU);
        }
        if constexpr (t == 2) MARK(2);
        if constexpr (t == 4 || t == 11) {
        }
      });
      __builtin_amdgcn_s_barrier(); asm volatile("" ::: "memory");
      VISIT(3);
      relane();
      asw[0][0] += L_EX; asw[0][1] += L_EX;
      ds_if<13>(xv, xr, ts);
      wphi_frag(w0, w1, lbase + L_WP + wc * 2048 + (4 * g + tq) * 64 + tp * 8);
      lds_wait4(flg + F_ST);
      {
        const f32x4 c0 = *(const f32x4*)(smem + L_C + 512 + (wc * 32 + 4 * g) * 4), c1 = *(const f32x4*)(smem + L_C + 512 + (wc * 32 + 16 + 4 * g) * 4);
        f32x2 mr[4];
        rowst(mr, lbase + L_MN + (wt * 64 + r15) * 4);
#pragma unroll
        for (int t = 0; t < 4; ++t) {
          sc[t] = mr[t][1] * L2E;
          acc[t][0] += mr[t][0] * c0; acc[t][1] += mr[t][0] * c1;
          asm volatile("" : "+v"(sc[t]), "+v"(acc[t][0]), "+v"(acc[t][1]));
        }
      }
      asm volatile("" : "+v"(xv[0]), "+v"(xv[1]), "+v"(w0), "+v"(w1));
      gate();
      SB();
      asm volatile("" : "+s"(kb));
      l16 = l * 16u;
      asm volatile("" : "+v"(l16), "+v"(fsl));
      GAP(6);
      VISIT(6);
      lds_wait1(flg + F_IM + lw);
      __builtin_amdgcn_s_setprio(1);
      tile(IC(1), IC(0), IC(1), []() {});
      lds_wait4(flg + F_GO);
      tile(IC(2), IC(0), IC(1), []() {}); tile(IC(0), IC(0), IC(0), []() {});
    }
    bf16x8 pq[4][2];
    {
      f32x4 p[4][2];
      const float one[2] = {1.f, 1.f};
#pragma unroll
      for (int t = 0; t < 4; ++t) {
        const f32x4 v0 = acc[t][0], v1 = acc[t][1];
        const bf16x8 qp = as8(u32x4{pk2(v0[0], v0[1]), pk2(v0[2], v0[3]), pk2(v1[0], v1[1]), pk2(v1[2], v1[3])});
        p[t][0] = MFMA(w0, qp, z4); p[t][1] = MFMA(w1, qp, z4);
      }
      phi_map<false>(p, sc, one, [&](int t, const unsigned (&pe)[8]) {
        pq[t][0] = as8(u32x4{pe[0], pe[1], pe[2], pe[3]}); pq[t][1] = as8(u32x4{pe[4], pe[5], pe[6], pe[7]});
      });
    }
    asm volatile("" : "+v"(pq[0][0]), "+v"(pq[0][1]), "+v"(pq[1][0]), "+v"(pq[1][1]), "+v"(pq[2][0]), "+v"(pq[2][1]), "+v"(pq[3][0]), "+v"(pq[3][1]));
    {
      f32x2 rd[4];
      u32x4 kf[4];
      PAD(6);
#pragma unroll 1
      for (unsigned spins = 0; spins < SPIN; ++spins) {
        u32x2 v; unsigned long long ck;
        asm volatile("ds_read_b64 %0, %2\n\ts_memrealtime %1\n\ts_waitcnt lgkmcnt(0)" : "=v"(v), "=s"(ck) : "v"(fsl) : "memory");
        const unsigned t0 = __builtin_amdgcn_readfirstlane(v[0]), t1 = __builtin_amdgcn_readfirstlane(v[1]), t = __builtin_amdgcn_readfirstlane((unsigned)ck);
        if ((t0 & t1) && min(t - t0, t - t1) >= D2) break;
      }
      GL4(kf[0], l16, kb, 0, P_SC1); GL4(kf[1], l16, kb, 1024, P_SC1); GL4(kf[2], l16, kb, 2048, P_SC1); GL4(kf[3], l16, kb, 3072, P_SC1);
      lds_wait4(flg + F_BL);
      lds_wait2(flg + F_KV + 2 * wc);
      {
        bf16x8 z0 = *(const bf16x8*)(smem + L_Z + wc * 128 + g * 16), z1 = *(const bf16x8*)(smem + L_Z + wc * 128 + 64 + g * 16);
        if (l & 1) { z0 = ones; z1 = ones; }
#pragma unroll
        for (int t = 0; t < 4; ++t) {
          const f32x4 a = MFMA(z0, pq[t][0], z4), c = MFMA(z1, pq[t][1], z4);
          const f32x2 r = {__builtin_amdgcn_rcpf(a[1]), __builtin_amdgcn_rcpf(c[1])};
          rd[t] = r * __builtin_amdgcn_rcpf(__builtin_fmaf(a[0], r[0], __builtin_fmaf(c[0], r[1], 1e-6f)));
          asm volatile("" : "+v"(rd[t]));
        }
      }
      const char* pb = ws + WS_O + (size_t)(panel * 4 + wt * 2) * 65536 + hp * 4096 + wc * 2048;
      unsigned l2 = l16 + 65536u, oa = lbase + L_EX + wc * 8192 + wt * 4096 + l * 16, fo = LDS_OFF(flg + F_OS + lw);
      asm volatile("" : "+s"(pb));
      asm volatile("" : "+v"(l2), "+v"(oa), "+v"(fo));
      waitv4<0>(kf);
      if (__builtin_expect(__ballot(max(max(kf[0][3], kf[1][3]), max(kf[2][3], kf[3][3])) == ~0u) != 0ull, 0)) {
#pragma unroll 1
        for (unsigned spins = 0; spins < SPIN; ++spins) {
          const unsigned long long m0 = __ballot(kf[0][3] == ~0u), m1 = __ballot(kf[1][3] == ~0u), m2 = __ballot(kf[2][3] == ~0u), m3 = __ballot(kf[3][3] == ~0u);
          if ((m0 | m1 | m2 | m3) == 0ull) break;
          reload4c(kf, l16, kb, m0, m1, m2, m3);
        }
      }
      u32x4 ov[4];
      f32x4 n[4][4];
      auto mm = [&](int t, int i) { n[t][i] = MFMA(as8(kf[i]), pq[t][i >> 1], z4); SB(); };
#pragma unroll
      for (int i = 0; i < 8; ++i) mm(i >> 2, i & 3);
      static_for<4>([&](auto TT) {
        constexpr int t = decltype(TT)::value;
        f32x4 a = n[t][0] * rd[t][0], c = n[t][1] * rd[t][0];
        SB(); if (t < 2) mm(t + 2, 0);
        a += n[t][2] * rd[t][1];
        SB(); if (t < 2) mm(t + 2, 1);
        c += n[t][3] * rd[t][1];
        SB(); if (t < 2) mm(t + 2, 2);
        ov[t] = u32x4{pk2(a[0], a[1]), pk2(a[2], a[3]), pk2(c[0], c[1]), nosent(pk2(c[2], c[3]))};
        SB(); if (t < 2) mm(t + 2, 3);
        asm volatile("global_store_dwordx4 %0, %1, %2 offset:%3 sc1" :: "v"(t < 2 ? l16 : l2), "v"(ov[t]), "s"(pb), "n"((t & 1) * 1024) : "memory");
        SB();
      });
      asm volatile("ds_write_b128 %0, %1\n\tds_write_b128 %0, %2 offset:1024\n\tds_write_b128 %0, %3 offset:2048\n\tds_write_b128 %0, %4 offset:3072\n\tds_write_b32 %5, %5\n\ts_nop 0"
                   :: "v"(oa), "v"(ov[0]), "v"(ov[1]), "v"(ov[2]), "v"(ov[3]), "v"(fo) : "memory");
    }
    __builtin_amdgcn_s_setprio(0);
    GAP(3);
  } else {
    if (tid < 20) ((u32x4*)flg)[tid] = u32x4{0, 0, 0, 0};
    f32x4 acc[4][4];
    Frag<4> f[2];
    u32x4 fs;
    f32x4 c0, c1;
    f32x2 mr[4];
    s16x4 wq[4];
    unsigned mra = lbase + L_MN + (wt * 64 + r15) * 4, cca = lbase + L_C + (wc * 32 + 4 * g) * 4, wpa = lbase + L_WP + wc * 2048 + (4 * g + tq) * 64 + tp * 8, fa = lbase + L_F;
    char* img = smem + lw * 8192;
    unsigned va = lbase + SLOT + ABYTES + lw * 4096 + l * 16, fb = lbase + L_F + lw * 4, ia, wi[4], ko = kvo(), da = lbase + L_DN + wt * 128 + g * 8,
             zo = WS_ZP + ((hp * 4 + lw) * 16 + panel) * 128 + (r15 * 4 + g) * 16, m4a = lbase + L_MN + (wt * 64 + 4 * g) * 4, cva = lbase + L_C + (64 + wc * 32 + r15) * 4;
    {
      const int row = 4 * g + tq, sx = (((row >> 1) & 3) << 2) | (row & 1) | (((row >> 3) & 1) << 1);
      ia = LDS_OFF(img) + row * 128 + (((8 * wt + tp) ^ sx) * 8);
      const int sw = (((r15 >> 1) & 3) << 2) | (r15 & 1) | (((r15 >> 3) & 1) << 1);
#pragma unroll
      for (int i = 0; i < 4; ++i) wi[i] = LDS_OFF(img) + r15 * 128 + (((4 * i + g) ^ sw) * 8);
    }
    {
#pragma unroll
      for (int t = 0; t < 4; ++t)
#pragma unroll
        for (int c = 0; c < 4; ++c) acc[t][c] = z4;
      auto rd = [&](Frag<4>& y, auto I, auto OW, unsigned pa, const unsigned (&pw)[2]) {
        constexpr int i = decltype(I)::value, ow = decltype(OW)::value;
        if constexpr (i < 4) ds_128<i * 2048>(y.af[i], pa);
        else {
          constexpr int c = (i - 4) >> 1;
          if constexpr (i & 1) ds_tr<ow + (c >> 1) * WSLAB + 512>(y.wh[c], pw[c & 1]);
          else ds_tr<ow + (c >> 1) * WSLAB>(y.wl[c], pw[c & 1]);
        }
      };
      auto step = [&](Frag<4>& c, Frag<4>& y, auto OW, unsigned pa, const unsigned (&pw)[2], auto&& more) {
        static_for<16>([&](auto I) {
          constexpr int i = decltype(I)::value, t = i & 3, cb = i >> 2;
          if constexpr (cb < 2) mfma_t(acc[t][cb], cat8(c.wl[cb], c.wh[cb]), __builtin_bit_cast(u32x4, c.af[t]));
          else mfma_t(acc[t][cb], c.af[t], __builtin_bit_cast(u32x4, cat8(c.wl[cb], c.wh[cb])));
          if constexpr (i < 12) rd(y, I, OW, pa, pw); else more(I);
        });
      };
      auto none = [](auto) {};
      __builtin_amdgcn_s_setprio(2);
#pragma unroll
      for (int t = 0; t < 4; ++t) asm volatile("" : "+v"(acc[t][0]), "+v"(acc[t][1]), "+v"(acc[t][2]), "+v"(acc[t][3]));
      asm volatile("" : "+v"(mra), "+v"(cca), "+v"(wpa), "+v"(fa), "+v"(va), "+v"(fb), "+v"(ia), "+v"(wi[0]), "+v"(wi[1]), "+v"(wi[2]), "+v"(wi[3]),
                   "+v"(ko), "+v"(da), "+v"(zo), "+v"(m4a), "+v"(cva));
      TICK();
      static_for<12>([&](auto I) { rd(f[0], I, IC(0), asw[0][0], wa[0]); });
#pragma unroll 1
      for (int it = 0; it < 5; ++it) {
        static_for<3>([&](auto J) {
          constexpr int j = decltype(J)::value, n = (j + 1) % 3;
          f[0].wait();
          step(f[0], f[1], IC(4096), asw[j][1], wa[j], none);
          if constexpr (j == 1) {
            const unsigned dx = it == 4 ? L_EX : 0, dw = it == 4 ? L_EW - ABYTES : 0;
            asw[0][0] += dx; asw[0][1] += dx; wa[0][0] += dw; wa[0][1] += dw;
            asm volatile("" : "+v"(asw[0][0]), "+v"(asw[0][1]), "+v"(wa[0][0]), "+v"(wa[0][1]));
          }
          f[1].wait();
          if (j < 2 || __builtin_expect(it < 4, 1)) __builtin_amdgcn_s_barrier();
          asm volatile("" ::: "memory");
          step(f[1], f[0], IC(0), asw[n][0], wa[n], none);
        });
      }
      f[0].wait();
      step(f[0], f[1], IC(4096), asw[0][1], wa[0], [&](auto I) {
        constexpr int i = decltype(I)::value;
        if constexpr (i == 12) { ds_tr<0>(wq[0], wpa); ds_tr<1024>(wq[1], wpa); ds_tr<32>(wq[2], wpa); }
        if constexpr (i == 13) { ds_tr<1056>(wq[3], wpa); ds_32<F_T * 4>(T, fa); }
      });
      asm volatile("s_waitcnt lgkmcnt(5)" : "+v"(f[1].af[0]), "+v"(f[1].af[1]), "+v"(f[1].af[2]), "+v"(f[1].af[3]), "+v"(f[1].wl[0]), "+v"(f[1].wh[0]), "+v"(f[1].wl[1]), "+v"(f[1].wh[1]),
                   "+v"(f[1].wl[2]), "+v"(f[1].wh[2]), "+v"(f[1].wl[3]), "+v"(f[1].wh[3]));
      static_for<8>([&](auto I) {
        constexpr int i = decltype(I)::value, t = i >> 1, cb = i & 1;
        acc[t][cb] = MFMA(cat8(f[1].wl[cb], f[1].wh[cb]), f[1].af[t], acc[t][cb]);
        SB();
        if constexpr (i == 0) { ds_128v<F_ST * 4>(fs, fa); ds_128v<0>(c0, cca); ds_128v<64>(c1, cca); }
        if constexpr (i == 1) { ds_2x32<0>(mr[0], mra); ds_2x32<16>(mr[1], mra); }
        if constexpr (i == 2) { ds_2x32<32>(mr[2], mra); ds_2x32<48>(mr[3], mra); }
        if constexpr (i == 3) raise<F_LD>(fb);
        SB();
      });
    }
    asm volatile("s_waitcnt lgkmcnt(0)" : "+v"(fs), "+v"(c0), "+v"(c1), "+v"(mr[0]), "+v"(mr[1]), "+v"(mr[2]), "+v"(mr[3]), "+v"(wq[0]), "+v"(wq[1]), "+v"(wq[2]), "+v"(wq[3]), "+v"(T));
    if (__builtin_expect(!__builtin_amdgcn_readfirstlane(fs[0] & fs[1] & fs[2] & fs[3]), 0)) {
      lds_wait4(flg + F_ST);
      asm volatile("ds_read_b128 %0, %2\n\tds_read_b128 %1, %2 offset:64\n\ts_waitcnt lgkmcnt(0)" : "=&v"(c0), "=&v"(c1) : "v"(cca) : "memory");
      rowst(mr, mra);
    }
    T = __builtin_amdgcn_readfirstlane(T);
    const unsigned t12 = T & 0xfffu;
    const bf16x8 w0 = cat8(wq[0], wq[1]), w1 = cat8(wq[2], wq[3]);
    u32x4 vp[2][4];
    auto selm = [](unsigned long long m, float a, float c) { float r; asm("v_cndmask_b32 %0, %1, %2, %3" : "=v"(r) : "v"(c), "v"(a), "s"(m)); return r; };
    {
      f32x4 p[4][2], m4[4];
      f32x2 cv;
      float sc[4], nrm[2];
#pragma unroll
      for (int t = 0; t < 4; ++t) {
        sc[t] = mr[t][1] * L2E;
        if (t < 2) nrm[t] = selm(0xffff0000ffff0000ull, mr[2 * t + 1][1], mr[2 * t][1]);
        const f32x4 v0 = mr[t][0] * c0 + acc[t][0], v1 = mr[t][0] * c1 + acc[t][1];
        const bf16x8 kc = as8(u32x4{pk2(v0[0], v0[1]), pk2(v0[2], v0[3]), pk2(v1[0], v1[1]), pk2(v1[2], v1[3])});
        p[t][0] = MFMA(w0, kc, z4); p[t][1] = MFMA(w1, kc, z4);
      }
      auto vl = [&](int i0, int i1) {
#pragma unroll
        for (int i = i0; i < i1; ++i)
          acc[i & 3][2 + (i >> 2)] = MFMA(f[1].af[i & 3], cat8(f[1].wl[2 + (i >> 2)], f[1].wh[2 + (i >> 2)]), acc[i & 3][2 + (i >> 2)]);
      };
      SB();
      PAD(3);
      SB();
      phi_map<true>(p, sc, nrm, [&](int t, const unsigned (&pe)[8]) {
        if (t < 2) { SB(); vl(4 * t, 4 * t + 4); SB(); }
        if (t == 2) asm volatile("s_waitcnt lgkmcnt(0)" : "+v"(m4[0]), "+v"(m4[1]), "+v"(m4[2]), "+v"(m4[3]), "+v"(cv) :: "memory");
#pragma unroll
        for (int i = 0; i < 4; ++i) *(u32x2*)(smem + (wi[i] - lbase) + t * 2048) = u32x2{pe[2 * i], pe[2 * i + 1]};
        if (t == 1) {
          asm volatile("ds_read_b128 %0, %5\n\tds_read_b128 %1, %5 offset:64\n\tds_read_b128 %2, %5 offset:128\n\tds_read_b128 %3, %5 offset:192\n\tds_read2_b32 %4, %6 offset1:16"
                       : "=&v"(m4[0]), "=&v"(m4[1]), "=&v"(m4[2]), "=&v"(m4[3]), "=&v"(cv)
                       : "v"(m4a), "v"(cva) : "memory");
          SB();
        }
        if (t == 2) {
#pragma unroll
          for (int d = 0; d < 2; ++d)
#pragma unroll
            for (int q = 0; q < 2; ++q) {
              const f32x4 a = m4[2 * q] * cv[d] + acc[2 * q][2 + d], c = m4[2 * q + 1] * cv[d] + acc[2 * q + 1][2 + d];
              vp[0][2 * q + d] = u32x4{pk2(a[0], a[1]), pk2(a[2], a[3]), pk2(c[0], c[1]), pk2(c[2], c[3])};
            }
          asm volatile("ds_write_b128 %0, %1\n\tds_write_b128 %0, %2 offset:1024\n\tds_write_b128 %0, %3 offset:2048\n\tds_write_b128 %0, %4 offset:3072"
                       :: "v"(va), "v"(vp[0][0]), "v"(vp[0][1]), "v"(vp[0][2]), "v"(vp[0][3]) : "memory");
        }
      });
    }
    const char* kb = ws + WS_KVP + (hp * 4 + lw) * 32768;
    {
      s16x4 tl[2][2], th[2][2];
#define IMG8 "ds_read_b64_tr_b16 %0, %8\n\tds_read_b64_tr_b16 %1, %8 offset:2048\n\tds_read_b64_tr_b16 %2, %9\n\tds_read_b64_tr_b16 %3, %9 offset:2048\n\t" \
             "ds_read_b64_tr_b16 %4, %8 offset:4096\n\tds_read_b64_tr_b16 %5, %8 offset:6144\n\tds_read_b64_tr_b16 %6, %9 offset:4096\n\tds_read_b64_tr_b16 %7, %9 offset:6144\n\t"
#define IMGO(c) c(tl[0][0]), c(th[0][0]), c(tl[0][1]), c(th[0][1]), c(tl[1][0]), c(th[1][0]), c(tl[1][1]), c(th[1][1])
      raise<F_EX>(fb);
      asm volatile(IMG8 : IMGO("=&v") : "v"(ia), "v"(ia ^ 32u) : "memory");
      {
        u32x4 d0, d1, d2, d3;
        asm volatile("ds_read2_b64 %0, %4 offset1:4\n\tds_read2_b64 %1, %4 offset0:8 offset1:12\n\tds_read2_b64 %2, %5 offset1:4\n\tds_read2_b64 %3, %5 offset0:8 offset1:12"
                     : "=&v"(d0), "=&v"(d1), "=&v"(d2), "=&v"(d3) : "v"(da), "v"(da ^ 128u) : "memory");
        unsigned o1 = ko ^ 17408u, ib = ia ^ 8192u, ic = ia ^ 8224u, fx, fe = (fb ^ 4u) + F_EX * 4;
        va ^= 4096u;
        asm volatile("s_waitcnt lgkmcnt(4)" : IMGO("+v"), "+v"(o1), "+v"(ib), "+v"(ic), "+v"(va), "+v"(fe), "+s"(kb) :: "memory");
        f32x4 kva[2][2], za[2];
        bf16x8 a[2][2];
#pragma unroll
        for (int q = 0; q < 2; ++q)
#pragma unroll
          for (int fb = 0; fb < 2; ++fb) {
            a[q][fb] = cat8(tl[q][fb], th[q][fb]);
#pragma unroll
            for (int d = 0; d < 2; ++d) kva[fb][d] = MFMA(a[q][fb], as8(vp[0][2 * q + d]), q ? kva[fb][d] : z4);
          }
        SB();
        s16x4 ul[2][2], uh[2][2];
#define IMGP(c) c(ul[0][0]), c(uh[0][0]), c(ul[0][1]), c(uh[0][1]), c(ul[1][0]), c(uh[1][0]), c(ul[1][1]), c(uh[1][1])
#define OTH "ds_read_b32 %10, %15\n\t" IMG8 "ds_read_b128 %11, %16\n\tds_read_b128 %12, %16 offset:1024\n\tds_read_b128 %13, %16 offset:2048\n\tds_read_b128 %14, %16 offset:3072"
        asm volatile("s_waitcnt lgkmcnt(0)" : "+v"(d0), "+v"(d1), "+v"(d2), "+v"(d3) :: "memory");
        asm volatile(OTH : IMGP("=&v"), "+v"(ib), "+v"(ic), "=&v"(fx), "=&v"(vp[1][0]), "=&v"(vp[1][1]), "=&v"(vp[1][2]), "=&v"(vp[1][3]) : "v"(fe), "v"(va) : "memory");
        SB();
#pragma unroll
        for (int fb = 0; fb < 2; ++fb) za[fb] = MFMA(a[1][fb], as8(d1), MFMA(a[0][fb], as8(d0), z4));
        SB();
        asm volatile("s_waitcnt lgkmcnt(0)" : IMGP("+v"), "+v"(fx), "+v"(vp[1][0]), "+v"(vp[1][1]), "+v"(vp[1][2]), "+v"(vp[1][3]) :: "memory");
        if (__builtin_expect(__builtin_amdgcn_readfirstlane(fx) == 0u, 0)) {
          asm volatile(".Lkf_cold:");
#pragma unroll 1
          for (unsigned spins = 0; spins < SPIN && __builtin_amdgcn_readfirstlane(fx) == 0u; ++spins)
            asm volatile(OTH "\n\ts_waitcnt lgkmcnt(0)" : IMGP("+v"), "+v"(ib), "+v"(ic), "+v"(fx), "+v"(vp[1][0]), "+v"(vp[1][1]), "+v"(vp[1][2]), "+v"(vp[1][3]) : "v"(fe), "v"(va) : "memory");
        }
#pragma unroll
        for (int d = 0; d < 2; ++d)
#pragma unroll
          for (int q = 0; q < 2; ++q)
#pragma unroll
            for (int fb = 0; fb < 2; ++fb) kva[fb][d] = MFMA(cat8(ul[q][fb], uh[q][fb]), as8(vp[1][2 * q + d]), kva[fb][d]);
        SB();
#pragma unroll
        for (int d = 0; d < 2; ++d) {
          za[d] = MFMA(cat8(ul[1][d], uh[1][d]), as8(d3), MFMA(cat8(ul[0][d], uh[0][d]), as8(d2), za[d]));
          const f32x4 u = kva[0][d], v = kva[1][d];
          gs4(d ? o1 : ko, kb, u32x4{pk2(u[0], u[1]), pk2(u[2], u[3]), pk2(v[0], v[1]), nosent(pk2(v[2], v[3]))});
        }
        raise<F_IM>(fb);
        relane();
        const f32x4 z = (l & 1) ? za[1] : za[0];
        if (r15 < 2) gs4(zo, ws, u32x4{tag12(z[0], t12), tag12(z[1], t12), tag12(z[2], t12), tag12(z[3], t12)});
      }
    }
    GL4(xv[0], xo, xfb, 0, P_DEF); GL4(xv[1], xo, xfb, 64, P_DEF);
    const char* zb = ws + WS_ZP + (hp * 4 + lw) * 2048;
    u32x4 zp[2], pl[4];
    {
      u32x4 p[2];
      const unsigned one = (l & 1) ? 0x3f800000u : 0x3f80u;
      u32x4 s0 = {(l & 2) ? 0u : one, (l & 2) ? one : 0u, 0u, 0u}, s1 = {0u, 0u, s0[0], s0[1]};
      asm volatile("" : "+v"(s0), "+v"(s1));
      GAP(7);
      VISIT(7);
      if (PD1) __builtin_amdgcn_s_sleep(PD1);
      poll2<PP1>(p, pl, l * 16u + panel * 2048, kb);
      asm volatile("" : "+v"(xv[0]), "+v"(xv[1]));
      GL4(zp[0], l * 16u, zb, 0, P_SC1); GL4(zp[1], l * 16u, zb, 1024, P_SC1);
      f32x4 u = MFMA(as8(s0), as8(p[1]), MFMA(as8(s0), as8(p[0]), z4)), v = MFMA(as8(s1), as8(p[1]), MFMA(as8(s1), as8(p[0]), z4));
#pragma unroll
      for (int e = 0; e < 4; ++e) { u[e] += ror8(u[e]); v[e] += ror8(v[e]); }
      gs4m(l * 16u, ws + WS_KVR + hp * 8192 + lw * 2048 + panel * 128, u32x4{pk2(u[0], u[1]), pk2(u[2], u[3]), pk2(v[0], v[1]), nosent(pk2(v[2], v[3]))});
      asm volatile("ds_write_b32 %0, %1 offset:%2" :: "v"(fb), "v"(now() | 1u), "n"(F_SL * 4) : "memory");
    }
    {
      asm volatile("s_waitcnt vmcnt(1)" : "+v"(zp[0]), "+v"(zp[1]) :: "memory");
      asm volatile("" :: "v"(pl[0]), "v"(pl[1]), "v"(pl[2]), "v"(pl[3]));
#pragma unroll 1
      for (unsigned spins = 0; spins < SPIN; ++spins) {
        const unsigned long long m0 = __ballot(bad4(zp[0], t12) & 0xfffu), m1 = __ballot(bad4(zp[1], t12) & 0xfffu);
        if ((m0 | m1) == 0ull) break;
        reload2(zp, l * 16u, zb, m0, m1);
      }
      float z[4];
#pragma unroll
      for (int e = 0; e < 4; ++e) { const float q = sum_g(untag12(zp[0][e]) + untag12(zp[1][e])); z[e] = q + ror8(q); }
      if (l < 8) *(u32x2*)(smem + L_Z + lw * 64 + (l & 3) * 16 + (l >> 2) * 8) = u32x2{pk2(z[0], z[1]), pk2(z[2], z[3])};
      lds_set(flg + F_KV + lw, 1u, l);
    }
    __builtin_amdgcn_s_setprio(0);
    gate();
  }
  {
    relane();
    u32x4 of[NS][2], o0[2];
    f32x4 acc[2][4];
#pragma unroll
    for (int t = 0; t < 2; ++t)
#pragma unroll
      for (int c = 0; c < 4; ++c) acc[t][c] = z4;
    tie8<1>(acc);
    unsigned wb[4], wx[4];
    {
      const int row = 4 * g + tq, f = ((row >> 1) & 1) | ((((row >> 2) ^ (row >> 3)) & 1) << 2);
#pragma unroll
      for (int nb = 0; nb < 4; ++nb) wb[nb] = lbase + s * 4096 + row * 128 + ((((tp & 2) + (nb & 1) + 4 * (nb >> 1)) ^ f) * 16) + (tp & 1) * 8;
    }
    const unsigned v0 = l * 16u + s * 3072u, v1 = l * 16u + s * 1024u + 1024u, v3 = l * 16u + 2048u - s * 1024u;
    auto oadr = [&](int h) { return ob + ptile(h) * 4096; };
    s16x4 wl[5], wh[5], el[2][4], eh[2][4];
    auto wrd = [&](auto K, auto HI) {
      constexpr int k = decltype(K)::value, h = k >> 2, nb = k & 3, o = (h & 7) * 8192;
      if constexpr (decltype(HI)::value) ds_tr<o + 2048>(wh[k % 5], wb[nb]);
      else {
        if constexpr (h == 8) { wb[nb] += 65536u; asm volatile("" : "+v"(wb[nb])); }
        ds_tr<o>(wl[k % 5], wb[nb]);
      }
    };
    unsigned xa = lbase + lw * 8192 + s * 4096 + l * 16, fa = LDS_OFF(flg + F_X + s * 4 + lw), fg = LDS_OFF(flg + F_PG + s * 4);
    f32x2 xf[8], gf[8];
    char* op = (char*)(out + (size_t)(panel * 128 + lw * 32 + s * 16) * DM + hp * 64);
    unsigned oo = (l & 7) * 2048u + (l & 8) * 8u + g * 16u, oa = lbase + L_EX + s * 9216 + lw * 2048 + l * 16;
    char* op2 = op + 16384;
    int idx;
    auto pre = [&]() {
#pragma unroll
      for (int i = 0; i < 8; ++i) {
        xf[i] = f32x2{bflo(xv[i >> 2][i & 3]), bfhi(xv[i >> 2][i & 3])}; gf[i] = f32x2{bflo(gq[i >> 2][i & 3]), bfhi(gq[i >> 2][i & 3])};
        asm volatile("" : "+v"(xf[i]), "+v"(gf[i]));
      }
      asm volatile("" : "+v"(oo), "+s"(op), "+s"(op2), "+v"(xa), "+v"(fa), "+v"(fg), "+v"(oa), "+v"(wb[0]), "+v"(wb[1]), "+v"(wb[2]), "+v"(wb[3]));
    };
    pre();
    lds_wait4(flg + F_OS);
    if (!s) __builtin_amdgcn_s_sleep(PD3);
    static_for<NS>([&](auto H) { opair(of[decltype(H)::value], v0, v1, oadr(decltype(H)::value + 1)); });
    lds_wait4(flg + F_WO);
    idx = 2047 - (int)(lds_get(flg + F_CA + 1) & 2047u);
    asm volatile("ds_read_b128 %0, %2\n\tds_read_b128 %1, %3" : "=&v"(o0[0]), "=&v"(o0[1]) : "v"(oa), "v"(oa ^ 1024u) : "memory");
    static_for<4>([&](auto NB) { wrd(NB, IC(0)); wrd(NB, IC(1)); });
    u32x4 pg;
    static_for<14>([&](auto H) {
      constexpr int h = decltype(H)::value, x0 = h == XH || h == XH + 1, x1 = h == XH + 1 || h == XH + 2;
      u32x4 (&o)[2] = h ? of[(h + NS - 1) % NS] : o0;
      if constexpr (h == BH0) { if (s) __builtin_amdgcn_s_setprio(1); }
      static_for<4>([&](auto NB) {
        constexpr int nb = decltype(NB)::value, k = 4 * h + nb;
        constexpr bool ld = nb == 3 && h && h + NS < 16;
        const bf16x8 wf = cat8(wl[k % 5], wh[k % 5]);
        mfma_w<x1 + (3 - nb) * (2 + 2 * x1) + nb * (2 + 2 * x0) + (h == 5 || h == 13)>(acc[1][nb], wf, o[1]);
        wrd(IC(k + 4), IC(0));
        if constexpr (x0) ds_tr<(h - XH + 6) * 8192>(el[h - XH][nb], wx[nb]);
        if constexpr (ld) GL4(o[1], h + NS < 14 ? v1 : v3, oadr(h + NS), 0, P_SC1);
        mfma_t(acc[0][nb], wf, o[0]);
        wrd(IC(k + 4), IC(1));
        if constexpr (x0) ds_tr<(h - XH + 6) * 8192 + 2048>(eh[h - XH][nb], wx[nb]);
        if constexpr (ld) GL4(o[0], v0, oadr(h + NS), 0, P_SC1);
      });
      if constexpr (h == 4) asm volatile("ds_write_b32 %0, %0" :: "v"(fg + lw * 4u) : "memory");
      if constexpr (h == LM7) PAD(7);
      if constexpr (h == 3) {
      }
      if constexpr (h == XH - 1) {
#pragma unroll
        for (int nb = 0; nb < 4; ++nb) { wx[nb] = wb[nb] ^ 4096u; asm volatile("" : "+v"(wx[nb])); }
      }
      if constexpr (h == 12) asm volatile("ds_read_b128 %0, %1" : "=v"(pg) : "v"(fg) : "memory");
      if constexpr (h < OC3) ocheck3<4>(of[h % 3], of[(h + 1) % 3], of[(h + 2) % 3], v0, v1, oadr(h + 1), oadr(h + 2), oadr(h + 3));
      else if constexpr (h < 13) ocheck<2 * ((h + NS < 15 ? h + NS : 15) - h - 1), true>(of[h % NS], v0, v1, oadr(h + 1));
    });
    asm volatile("s_waitcnt lgkmcnt(%1)" : "+v"(pg) : "n"(XH == 12 ? 15 : 8));
    if (__builtin_expect(!__builtin_amdgcn_readfirstlane(pg[0] & pg[1] & pg[2] & pg[3]), 0)) lds_wait4(flg + F_PG + s * 4);
    asm volatile("s_nop 1\n\tds_write_b128 %0, %1\n\tds_write_b128 %0, %2 offset:1024\n\tds_write_b128 %0, %3 offset:2048\n\tds_write_b128 %0, %4 offset:3072\n\tds_write_b32 %5, %5"
                 :: "v"(xa), "v"(acc[1][0]), "v"(acc[1][1]), "v"(acc[1][2]), "v"(acc[1][3]), "v"(fa) : "memory");
    __builtin_amdgcn_s_setprio(0);
    ocheck<2, true>(of[13 % NS], v0, v3, oadr(14));
    static_for<4>([&](auto NB) {
      constexpr int nb = decltype(NB)::value, k = 56 + nb;
      mfma_w<nb ? 15 : 5>(acc[0][nb], cat8(wl[k % 5], wh[k % 5]), of[13 % NS][0]);
      wrd(IC(k + 4), IC(0)); wrd(IC(k + 4), IC(1));
    });
#pragma unroll
    for (int nb = 0; nb < 4; ++nb) mfma_t(acc[0][nb], cat8(el[0][nb], eh[0][nb]), of[13 % NS][1]);
    unsigned fx;
    fa ^= 16u; xa ^= 4096u;
    asm volatile("ds_read_b32 %0, %5\n\tds_read_b128 %1, %6\n\tds_read_b128 %2, %6 offset:1024\n\tds_read_b128 %3, %6 offset:2048\n\tds_read_b128 %4, %6 offset:3072"
                 : "=&v"(fx), "=&v"(acc[1][0]), "=&v"(acc[1][1]), "=&v"(acc[1][2]), "=&v"(acc[1][3]) : "v"(fa), "v"(xa) : "memory");
    ocheck<0, true>(of[14 % NS], v0, v3, oadr(15));
#pragma unroll
    for (int nb = 0; nb < 4; ++nb) mfma_t(acc[0][nb], cat8(el[1][nb], eh[1][nb]), of[14 % NS][1]);
    static_for<4>([&](auto NB) { constexpr int nb = decltype(NB)::value, k = 60 + nb; mfma_w<5>(acc[0][nb], cat8(wl[k % 5], wh[k % 5]), of[14 % NS][0]); });
    if (__builtin_expect((idx >> 4) == panel * 8 + lw * 2 + s, 0)) {
      lds_wait1(flg + F_GT);
      relane();
      if (r15 == (idx & 15)) {
        const u32x2* gi = (const u32x2*)(smem + L_GI + (g >> 1) * 32 + (g & 1) * 8);
#pragma unroll
        for (int i = 0; i < 4; ++i) {
          const u32x2 a = gi[(i & 1) * 2 + (i >> 1) * 8];
          gf[2 * i] = f32x2{bflo(a[0]), bfhi(a[0])}; gf[2 * i + 1] = f32x2{bflo(a[1]), bfhi(a[1])};
        }
      }
    }
    asm volatile("s_waitcnt lgkmcnt(0)" : "+v"(fx), "+v"(acc[1][0]), "+v"(acc[1][1]), "+v"(acc[1][2]), "+v"(acc[1][3]) :: "memory");
    if (__builtin_expect(__builtin_amdgcn_readfirstlane(fx) == 0u, 0))
#pragma unroll 1
    for (unsigned spins = 0; spins < SPIN && __builtin_amdgcn_readfirstlane(fx) == 0u; ++spins)
      asm volatile("s_sleep 1\n\tds_read_b32 %0, %5\n\tds_read_b128 %1, %6\n\tds_read_b128 %2, %6 offset:1024\n\tds_read_b128 %3, %6 offset:2048\n\t"
                   "ds_read_b128 %4, %6 offset:3072\n\ts_waitcnt lgkmcnt(0)"
                   : "+v"(fx), "+v"(acc[1][0]), "+v"(acc[1][1]), "+v"(acc[1][2]), "+v"(acc[1][3]) : "v"(fa), "v"(xa) : "memory");
#pragma unroll
    for (int i = 0; i < 8; ++i) xf[i] = __builtin_elementwise_fma(f32x2{acc[1][i >> 1][2 * (i & 1)], acc[1][i >> 1][2 * (i & 1) + 1]}, gf[i], xf[i]);
    asm volatile("" : "+v"(xf[0]), "+v"(xf[1]), "+v"(xf[2]), "+v"(xf[3]), "+v"(xf[4]), "+v"(xf[5]), "+v"(xf[6]), "+v"(xf[7]),
                 "+v"(acc[0][0]), "+v"(acc[0][1]), "+v"(acc[0][2]), "+v"(acc[0][3]));
    unsigned rr[8];
#pragma unroll
    for (int i = 0; i < 8; ++i) {
      const f32x2 r = __builtin_elementwise_fma(f32x2{acc[0][i >> 1][2 * (i & 1)], acc[0][i >> 1][2 * (i & 1) + 1]}, gf[i], xf[i]);
      rr[i] = pk2(r[0], r[1]);
    }
    {
      sw16u(rr[0], rr[2]); sw16u(rr[1], rr[3]); sw16u(rr[4], rr[6]); sw16u(rr[5], rr[7]);
      unsigned a0, a1, a2, a3, c0, c1, c2, c3;
#define CD(d, x, y) "v_cndmask_b32_dpp " d ", " x ", " y ", vcc row_ror:8 row_mask:0xf bank_mask:0xf\n\t"
      asm volatile("s_mov_b32 vcc_lo, 0x00ff00ff\n\ts_mov_b32 vcc_hi, 0x00ff00ff\n\t" CD("%0", "%12", "%8") CD("%2", "%14", "%10") CD("%1", "%13", "%9") CD("%3", "%15", "%11")
                   "s_not_b64 vcc, vcc\n\t" CD("%4", "%8", "%12") CD("%5", "%9", "%13") CD("%6", "%10", "%14") CD("%7", "%11", "%15")
                   : "=&v"(a0), "=&v"(a1), "=&v"(a2), "=&v"(a3), "=&v"(c0), "=&v"(c1), "=&v"(c2), "=&v"(c3)
                   : "v"(rr[0]), "v"(rr[1]), "v"(rr[2]), "v"(rr[3]), "v"(rr[4]), "v"(rr[5]), "v"(rr[6]), "v"(rr[7]) : "vcc", "scc");
      const u32x4 a = {a0, a1, a2, a3}, c = {c0, c1, c2, c3};
      asm volatile("global_store_dwordx4 %0, %1, %3 sc1\n.Lkf_last:\n\tglobal_store_dwordx4 %0, %2, %4 sc1\n\ts_nop 1" :: "v"(oo), "v"(a), "v"(c), "s"(op), "s"(op2) : "memory");
    }
    if (s) {
    } else {
    }
  }
}

extern "C" void kernel_launch(void* const* d_in, const int* in_sizes, int n_in,
                              void* d_out, int out_size, void* d_ws, size_t ws_size,
                              hipStream_t stream) {
  (void)hipFuncSetAttribute((const void*)kf_main, hipFuncAttributeMaxDynamicSharedMemorySize, LDSM);
  kf_main<<<256, 768, LDSM, stream>>>((const u16*)d_in[0], (const u16*)d_in[1], (char*)d_ws, (const u16*)d_in[5], (const u16*)d_in[2],
                                      (const u16*)d_in[3], (const u16*)d_in[6], (const u16*)d_in[7], (const u16*)d_in[8], (u16*)d_out);
}
```

```cpp
#include <hip/hip_runtime.h>
#include <stdint.h>
#include <utility>

typedef __attribute__((ext_vector_type(8))) short bf16x8;
typedef __attribute__((ext_vector_type(4))) short s16x4;
typedef __attribute__((ext_vector_type(4))) float f32x4;
typedef __attribute__((ext_vector_type(2))) float f32x2;
typedef __attribute__((ext_vector_type(4))) unsigned u32x4;
typedef __attribute__((ext_vector_type(2))) unsigned u32x2;
typedef __attribute__((ext_vector_type(2))) __bf16 bfx2;
typedef unsigned short u16;

#define DI __device__ __forceinline__
#define MFMA(a, b, c) __builtin_amdgcn_mfma_f32_16x16x32_bf16((a), (b), (c), 0, 0, 0)
#define SB() __builtin_amdgcn_sched_barrier(0)
#define IC(x) std::integral_constant<int, (x)>{}

DI unsigned pk2(float a, float b) { f32x2 v = {a, b}; return __builtin_bit_cast(unsigned, __builtin_convertvector(v, bfx2)); }
DI float bflo(unsigned u) { return __uint_as_float(u << 16); }
DI float bfhi(unsigned u) { return __uint_as_float(u & 0xffff0000u); }
DI float rbf(float x) { return (float)(__bf16)x; }
DI f32x2 rb2(f32x2 v) { return f32x2{__uint_as_float(pk2(0.f, v[0])), __uint_as_float(pk2(0.f, v[1]))}; }
DI bf16x8 cat8(s16x4 lo, s16x4 hi) { return __builtin_shufflevector(lo, hi, 0, 1, 2, 3, 4, 5, 6, 7); }
DI bf16x8 as8(u32x4 v) { return __builtin_bit_cast(bf16x8, v); }
template <int... I, class F> DI void static_for_impl(std::integer_sequence<int, I...>, F&& f) { (f(std::integral_constant<int, I>{}), ...); }
template <int N, class F> DI void static_for(F&& f) { static_for_impl(std::make_integer_sequence<int, N>{}, f); }
template <int N> DI void waitv() { asm volatile("s_waitcnt vmcnt(%0)" ::"n"(N) : "memory"); }
#define RAW_BARRIER() do { asm volatile("s_waitcnt lgkmcnt(0)" ::: "memory"); __builtin_amdgcn_s_barrier(); asm volatile("" ::: "memory"); } while (0)
#define TICK() do { SB(); RAW_BARRIER(); SB(); } while (0)
#define LAY 1
#define GAP(n) asm volatile("s_branch .Lkg" #n "\n.Lkh" #n ":\n\t.fill ((.Lkg" #n " - .Lkt" #n " - (.Lkh" #n " - kf_main)) & 4095) / 4, 4, 0xbf800000\n.Lkg" #n ":" ::: "memory")
#define MARK(n) asm volatile(".Lkt" #n ":" ::: "memory")
#define PAD(n) do { unsigned d_; asm volatile(".Lkt" #n ":\n\ts_mov_b32 %0, 0xbe801d6a" : "=s"(d_)); } while (0)
#define VISIT(n) do { const char* p_; int o_; asm volatile("s_getpc_b64 %0\n.Lkv" #n ":\n\ts_mov_b32 %1, .Lkt" #n " - .Lkv" #n " + 4" : "=s"(p_), "=s"(o_)); \
                      asm volatile("s_swappc_b64 vcc, %0" :: "s"(p_ + o_) : "vcc"); } while (0)
#define DS_B128(dst, addr, off) asm volatile("ds_read_b128 %0, %1 offset:%2" : "=v"(dst) : "v"(addr), "n"(off))
#define DS_TR(dst, addr, off) asm volatile("ds_read_b64_tr_b16 %0, %1 offset:%2" : "=v"(dst) : "v"(addr), "n"(off))
#define LDS_OFF(p) ((unsigned)(uintptr_t)(__attribute__((address_space(3))) const char*)(p))
enum { P_DEF = 0, P_SC1 = 1, P_NT = 2 };
template <int OFF, int POL> DI void gl4(u32x4& d, unsigned voff, const char* sb) {
  if constexpr (POL == P_SC1) asm volatile("global_load_dwordx4 %0, %1, %2 offset:%3 sc1" : "=v"(d) : "v"(voff), "s"(sb), "n"(OFF));
  else if constexpr (POL == P_NT) asm volatile("global_load_dwordx4 %0, %1, %2 offset:%3 nt" : "=v"(d) : "v"(voff), "s"(sb), "n"(OFF));
  else asm volatile("global_load_dwordx4 %0, %1, %2 offset:%3" : "=v"(d) : "v"(voff), "s"(sb), "n"(OFF));
}
#define GL4(dst, voff, sb, off, pol) gl4<off, pol>(dst, voff, sb)
#define GL2(dst, voff, sb, off) asm volatile("global_load_dwordx2 %0, %1, %2 offset:%3" : "=v"(dst) : "v"(voff), "s"(sb), "n"(off))
#define GL1(dst, voff, sb, off) asm volatile("global_load_dword %0, %1, %2 offset:%3 sc1" : "=v"(dst) : "v"(voff), "s"(sb), "n"(off))
DI void bl4nt(u32x4& d, unsigned voff, u32x4 rs, unsigned soff) {
  asm volatile("buffer_load_dwordx4 %0, %1, %2, %3 offen nt" : "=v"(d) : "v"(voff), "s"(rs), "s"(soff));
}
DI void mfma_t(f32x4& c, bf16x8 a, u32x4 bb) { asm volatile("v_mfma_f32_16x16x32_bf16 %0, %1, %2, %0" : "+v"(c) : "v"(a), "v"(bb)); }
template <int N> DI void mfma_w(f32x4& c, bf16x8 a, u32x4 bb) {
  asm volatile("s_waitcnt lgkmcnt(%3)\n\tv_mfma_f32_16x16x32_bf16 %0, %1, %2, %0" : "+v"(c) : "v"(a), "v"(bb), "n"(N));
}
template <int N> DI void tie8(f32x4 (&a)[2][4]) {
  asm volatile("s_nop %8" : "+v"(a[0][0]), "+v"(a[0][1]), "+v"(a[0][2]), "+v"(a[0][3]), "+v"(a[1][0]), "+v"(a[1][1]), "+v"(a[1][2]), "+v"(a[1][3]) : "n"(N));
}
template <int OFF> DI void ds_tr(s16x4& d, unsigned a) { DS_TR(d, a, OFF); }
template <int N> DI void waitv4(u32x4 (&v)[4]) { asm volatile("s_waitcnt vmcnt(%4)" : "+v"(v[0]), "+v"(v[1]), "+v"(v[2]), "+v"(v[3]) : "n"(N)); }
template <int N> DI void waitl8(s16x4 (&a)[4], s16x4 (&c)[4]) {
  asm volatile("s_waitcnt lgkmcnt(%8)" : "+v"(a[0]), "+v"(c[0]), "+v"(a[1]), "+v"(c[1]), "+v"(a[2]), "+v"(c[2]), "+v"(a[3]), "+v"(c[3]) : "n"(N));
}
DI void gs4(unsigned voff, const char* sb, u32x4 v) { asm volatile("global_store_dwordx4 %0, %1, %2 sc1\n\ts_nop 1" :: "v"(voff), "v"(v), "s"(sb) : "memory"); }
DI void gs1(unsigned voff, const char* sb, unsigned v) { asm volatile("global_store_dword %0, %1, %2 sc1" :: "v"(voff), "v"(v), "s"(sb) : "memory"); }
DI void glds(unsigned voff, const char* sbase, unsigned lds) {
  asm volatile("s_mov_b32 m0, %2\n\ts_nop 0\n\tglobal_load_lds_dwordx4 %0, %1" :: "v"(voff), "s"(sbase), "s"(lds) : "memory");
}
DI void touch(unsigned voff, const char* sbase, unsigned lds) {
  asm volatile("s_mov_b32 m0, %2\n\ts_nop 0\n\tglobal_load_lds_dword %0, %1" :: "v"(voff), "s"(sbase), "s"(lds) : "memory");
}
DI void touch4(unsigned voff, const char* sbase, unsigned lds) {
  asm volatile("s_mov_b64 exec, 15\n\ts_mov_b32 m0, %2\n\ts_nop 0\n\tglobal_load_lds_dword %0, %1\n\ts_mov_b64 exec, -1" :: "v"(voff), "s"(sbase), "s"(lds) : "memory");
}
DI void pub_pin(u32x4& a, u32x2& b, unsigned& c, unsigned& d) { asm volatile("" : "+v"(a), "+v"(b), "+v"(c), "+v"(d)); }
DI void pub(u32x4 v, u32x2 w, unsigned o, unsigned oc, unsigned long long m, unsigned long long mc, const char* sb) {
  asm volatile("s_mov_b64 exec, %4\n\tglobal_store_dwordx2 %2, %1, %6 sc1\n\ts_mov_b64 exec, %5\n\tglobal_store_dwordx4 %3, %0, %6 sc1\n\ts_mov_b64 exec, -1\n\ts_nop 0"
               :: "v"(v), "v"(w), "v"(o), "v"(oc), "s"(m), "s"(mc), "s"(sb) : "memory");
}
DI float selm(unsigned long long m, float a, float c) { float r; asm("v_cndmask_b32 %0, %1, %2, %3" : "=v"(r) : "v"(c), "v"(a), "s"(m)); return r; }
DI void touchm(unsigned voff, const char* sbase, unsigned lds, unsigned long long m) {
  asm volatile("s_mov_b64 exec, %3\n\ts_mov_b32 m0, %2\n\ts_nop 0\n\tglobal_load_lds_dword %0, %1\n\ts_mov_b64 exec, -1" :: "v"(voff), "s"(sbase), "s"(lds), "s"(m) : "memory");
}
DI float silu_f(float x) { return x / (1.0f + __expf(-x)); }
DI float silu_r(float x) { return x * __builtin_amdgcn_rcpf(1.0f + __expf(-x)); }
DI float dot2(unsigned a, unsigned b, float c) { return __builtin_amdgcn_fdot2_f32_bf16(__builtin_bit_cast(bfx2, a), __builtin_bit_cast(bfx2, b), c, false); }

template <int OFF> DI void gs4o(unsigned voff, const char* sb, u32x4 v) {
  asm volatile("global_store_dwordx4 %0, %1, %2 offset:%3 sc1\n\ts_nop 1" :: "v"(voff), "v"(v), "s"(sb), "n"(OFF) : "memory");
}
DI void gs4m(unsigned voff, const char* sb, u32x4 v) {
  asm volatile("s_mov_b64 exec, 0xff\n\tglobal_store_dwordx4 %0, %1, %2 sc1\n\ts_mov_b64 exec, -1\n\ts_nop 0" :: "v"(voff), "v"(v), "s"(sb) : "memory");
}
DI void gs2(unsigned voff, const char* sb, u32x2 v) { asm volatile("global_store_dwordx2 %0, %1, %2 sc1" :: "v"(voff), "v"(v), "s"(sb) : "memory"); }
#define GL1P(dst, voff, sb) asm volatile("global_load_dword %0, %1, %2" : "=v"(dst) : "v"(voff), "s"(sb))
DI u32x4 ldsr1(unsigned a) { u32x4 v; asm volatile("ds_read_b128 %0, %1\n\ts_waitcnt lgkmcnt(0)" : "=v"(v) : "v"(a) : "memory"); return v; }
DI void ldsw1(unsigned a, u32x4 v) { asm volatile("ds_write_b128 %0, %1" :: "v"(a), "v"(v) : "memory"); }
DI unsigned nosent(unsigned v) { return min(v, 0xfffffffeu); }

constexpr int DM = 1024, FF = 3584;
constexpr unsigned WS_ST = 4096;
constexpr unsigned WS_CA = WS_ST + 2048 * 8;
constexpr unsigned WS_CS = WS_CA + 256 * 16;
constexpr unsigned WS_KVP = WS_CS + 256 * 1024;
constexpr unsigned WS_ZP = WS_KVP + 2048 * 1024;
constexpr unsigned WS_KVR = WS_ZP + 1024 * 128;
constexpr unsigned WS_O = WS_KVR + 16 * 8192;
constexpr unsigned WS_GU = WS_O + 4096 * 1024;
constexpr unsigned WS_Y = WS_GU + 56 * 4096;

constexpr int ABYTES = 16384, WSLAB = 8192, SLOT = 40960;
constexpr int L_EW = 3 * SLOT;
constexpr int L_EX = L_EW + 2 * WSLAB;
constexpr int L_REC = L_EW;
constexpr int L_Z = L_EX + ABYTES;
constexpr int L_WP = L_Z + 256;
constexpr int L_MN = L_WP + 4096;
constexpr int L_RS = L_MN + 512;
constexpr int L_DN = L_RS + 512;
constexpr int L_C = L_DN + 256;
constexpr int L_H = L_C + 768;
constexpr int L_GI = L_H + 1024;
constexpr int L_F = L_GI + 128;
constexpr int L_DU = L_F + 320;
constexpr int LDSM = L_DU + 256;
constexpr int F_KV = 0, F_WO = 4, F_PG = 8, F_X = 16, F_T = 24, F_GT = 25, F_BL = 32, F_IM = 36, F_EX = 40, F_ST = 44, F_SD = 48, F_CA = 52, F_OS = 60, F_GO = 64, F_AL = 68, F_SL = 72, F_LD = 76;
#define SPINV (1u << 20)
constexpr unsigned SPIN = SPINV;
DI void pin1(float& v) { asm volatile("" : "+v"(v)); }
DI void pinf(f32x4& v) { asm volatile("" : "+v"(v)); }
DI void pin2(u32x4 (&v)[2]) { asm volatile("" : "+v"(v[0]), "+v"(v[1])); }
constexpr float L2E = 1.4426950408889634f;

template <int CTRL, int RMASK> DI float dpp_add(float v) {
  return v + __builtin_bit_cast(float, __builtin_amdgcn_update_dpp(0, __builtin_bit_cast(int, v), CTRL, RMASK, 0xf, true));
}
DI float wave_total(float v) {
  v = dpp_add<0x111, 0xf>(v); v = dpp_add<0x112, 0xf>(v); v = dpp_add<0x114, 0xf>(v); v = dpp_add<0x118, 0xf>(v);
  v = dpp_add<0x142, 0xa>(v); v = dpp_add<0x143, 0xc>(v);
  return __builtin_bit_cast(float, __builtin_amdgcn_readlane(__builtin_bit_cast(int, v), 63));
}
DI f32x2 sw32(float a, float b) {
  auto r = __builtin_amdgcn_permlane32_swap(__float_as_uint(a), __float_as_uint(b), false, false);
  return f32x2{__uint_as_float(r[0]), __uint_as_float(r[1])};
}
DI f32x2 sw16(float a, float b) {
  auto r = __builtin_amdgcn_permlane16_swap(__float_as_uint(a), __float_as_uint(b), false, false);
  return f32x2{__uint_as_float(r[0]), __uint_as_float(r[1])};
}
DI float ror8(float v) { return __builtin_bit_cast(float, __builtin_amdgcn_update_dpp(0, __builtin_bit_cast(int, v), 0x128, 0xf, 0xf, true)); }
DI float sum_g(float v) {
  f32x2 a = sw32(v, v); v = a[0] + a[1];
  a = sw16(v, v); return a[0] + a[1];
}
DI unsigned tag12(float v, unsigned t) { return ((__float_as_uint(v) + 0x800u) & 0xfffff000u) | t; }
DI float untag12(unsigned u) { return __uint_as_float(u & 0xfffff000u); }
DI unsigned bad4(u32x4 v, unsigned t) { return (v[0] ^ t) | (v[1] ^ t) | (v[2] ^ t) | (v[3] ^ t); }
DI bool all0(unsigned v) { return __ballot(v != 0u) == 0ull; }
DI void lds_set(unsigned* f, unsigned v, int l) {
  asm volatile("s_waitcnt lgkmcnt(0)" ::: "memory");
  if (l == 0) __hip_atomic_store(f, v, __ATOMIC_RELAXED, __HIP_MEMORY_SCOPE_WORKGROUP);
}
DI unsigned lds_get(unsigned* f) { return __builtin_amdgcn_readfirstlane(__hip_atomic_load(f, __ATOMIC_RELAXED, __HIP_MEMORY_SCOPE_WORKGROUP)); }
DI void lds_wait4(unsigned* f) {
#pragma unroll 1
  for (unsigned spins = 0; spins < SPIN; ++spins) {
    u32x4 v; asm volatile("ds_read_b128 %0, %1\n\ts_waitcnt lgkmcnt(0)" : "=v"(v) : "v"(LDS_OFF(f)) : "memory");
    if (__builtin_amdgcn_readfirstlane(v[0] & v[1] & v[2] & v[3])) break;
    __builtin_amdgcn_s_sleep(1);
  }
}

template <int NC> struct Frag {
  bf16x8 af[4]; s16x4 wl[NC], wh[NC];
  DI void wait() {
    if constexpr (NC == 4)
      asm volatile("s_waitcnt lgkmcnt(0)" : "+v"(af[0]), "+v"(af[1]), "+v"(af[2]), "+v"(af[3]), "+v"(wl[0]), "+v"(wh[0]), "+v"(wl[1]), "+v"(wh[1]),
                   "+v"(wl[2]), "+v"(wh[2]), "+v"(wl[3]), "+v"(wh[3]));
    else
      asm volatile("s_waitcnt lgkmcnt(0)" : "+v"(af[0]), "+v"(af[1]), "+v"(af[2]), "+v"(af[3]), "+v"(wl[0]), "+v"(wh[0]), "+v"(wl[1]), "+v"(wh[1]));
  }
};
template <int OFF> DI void ds_32(unsigned& d, unsigned a) { asm volatile("ds_read_b32 %0, %1 offset:%2" : "=v"(d) : "v"(a), "n"(OFF) : "memory"); }
template <int OFF, class V> DI void ds_128v(V& d, unsigned a) { DS_B128(d, a, OFF); }
template <int O> DI void ds_2x32(f32x2& d, unsigned a) { asm volatile("ds_read2_b32 %0, %1 offset0:%2 offset1:%3" : "=v"(d) : "v"(a), "n"(O), "n"(O + 128) : "memory"); }
template <int F> DI void raise(unsigned a) { asm volatile("ds_write_b32 %0, %0 offset:%1" :: "v"(a), "n"(F * 4) : "memory"); }
template <int OFF> DI void ds_w64(unsigned a, u32x2 v) { asm volatile("ds_write_b64 %0, %1 offset:%2" :: "v"(a), "v"(v), "n"(OFF) : "memory"); }
template <int T> DI void ds_if(u32x4 (&v)[2], unsigned a, int t) {
  asm volatile("s_cmp_eq_u32 %4, %5\n\ts_cselect_b64 exec, -1, 0\n\tds_read_b128 %0, %2\n\tds_read_b128 %1, %3\n\ts_mov_b64 exec, -1"
               : "+v"(v[0]), "+v"(v[1]) : "v"(a), "v"(a ^ 64u), "s"(t), "n"(T) : "scc", "memory");
}
template <int OFF> DI void ds_128(bf16x8& d, unsigned a) { DS_B128(d, a, OFF); }
DI void lds_wait2(unsigned* f) {
#pragma unroll 1
  for (unsigned spins = 0; spins < SPIN; ++spins) {
    u32x2 v; asm volatile("ds_read_b64 %0, %1\n\ts_waitcnt lgkmcnt(0)" : "=v"(v) : "v"(LDS_OFF(f)) : "memory");
    if (__builtin_amdgcn_readfirstlane(v[0] & v[1])) break;
    __builtin_amdgcn_s_sleep(1);
  }
}
DI void reload2(u32x4 (&p)[2], unsigned o, const char* sb, unsigned long long m0, unsigned long long m1) {
  asm volatile("s_mov_b64 exec, %4\n\tglobal_load_dwordx4 %0, %2, %3 sc1\n\ts_mov_b64 exec, %5\n\tglobal_load_dwordx4 %1, %2, %3 offset:1024 sc1\n\t"
               "s_mov_b64 exec, -1\n\ts_waitcnt vmcnt(0)"
               : "+v"(p[0]), "+v"(p[1]) : "v"(o), "s"(sb), "s"(m0), "s"(m1) : "memory");
}

template <int CTRL> DI unsigned dpp_umax(unsigned v) { return max(v, (unsigned)__builtin_amdgcn_update_dpp((int)v, (int)v, CTRL, 0xf, 0xf, false)); }
template <int CTRL, int RMASK> DI unsigned dpp_umax_r(unsigned v) { return max(v, (unsigned)__builtin_amdgcn_update_dpp((int)v, (int)v, CTRL, RMASK, 0xf, false)); }
DI unsigned now() { return (unsigned)__builtin_amdgcn_s_memrealtime(); }
DI void lds_wait1(unsigned* f) {
#pragma unroll 1
  for (unsigned spins = 0; spins < SPIN && lds_get(f) == 0u; ++spins) __builtin_amdgcn_s_sleep(1);
  asm volatile("" ::: "memory");
}

DI void cs_read(u32x2 (&c)[4], unsigned a) {
  asm volatile("ds_read_b64_tr_b16 %0, %4\n\tds_read_b64_tr_b16 %1, %4 offset:512\n\tds_read_b64_tr_b16 %2, %4 offset:4096\n\tds_read_b64_tr_b16 %3, %4 offset:4608"
               : "=&v"(c[0]), "=&v"(c[1]), "=&v"(c[2]), "=&v"(c[3]) : "v"(a) : "memory");
}
DI void cs_sum(u32x2 (&c)[4], float& s0, float& s1) {
  asm volatile("s_waitcnt lgkmcnt(0)" : "+v"(c[0]), "+v"(c[1]), "+v"(c[2]), "+v"(c[3]) :: "memory");
#pragma unroll
  for (int i = 0; i < 4; ++i) { s0 = dot2(c[i][0], 0x3f803f80u, s0); s1 = dot2(c[i][1], 0x3f803f80u, s1); }
  asm volatile("" : "+v"(s0), "+v"(s1));
}
#define NG 4
#define PP1 12
#define PD1 12
#define PD3 6
#define PPY 12
#define FP0 1
#define FPR 2
#define FPE 0
#define FPG 3
#define GAS 10
#define SB0 2
#define SBI 3
#define SBP 6
#define CTS 6
#define ES0 7
#define ESL 5
#define NF 4
#define WNO 40
#define D2 40
#define PYD 16
#define LM7 2
#define XH 11
#define OC3 1
#define CAS 9
#define BH0 5
#define STC 1
#define SM 3
#define NS 3
constexpr int PUB = SBP > SM ? SBP : SM;
constexpr int n_e(int S) { int n = 0; for (int i = 0; i < 8; ++i) n += ES0 + i * ESL / 8 == S; return n; }
constexpr int n_x(int S) { return (S == PUB) + (S == 0) + (S == GAS ? 2 : 0) + (S == CTS) + n_e(S) + (S == CAS); }
static_assert(SB0 >= 1 && SM >= SB0 && SM >= 2 && (NS == 3 || !OC3) && SBP > SBI && PUB >= 3 && PUB < GAS && GAS <= 10 && CTS <= 10 && ES0 > SB0 && ES0 + ESL <= 12 && CAS > PUB && CAS <= 9 && NF + NG <= 16, "");
DI void wphi_frag(bf16x8& w0, bf16x8& w1, unsigned a) {
  s16x4 t0, t1, t2, t3;
  asm volatile("ds_read_b64_tr_b16 %0, %4\n\tds_read_b64_tr_b16 %1, %4 offset:1024\n\tds_read_b64_tr_b16 %2, %4 offset:32\n\t"
               "ds_read_b64_tr_b16 %3, %4 offset:1056\n\ts_waitcnt lgkmcnt(0)" : "=&v"(t0), "=&v"(t1), "=&v"(t2), "=&v"(t3) : "v"(a) : "memory");
  w0 = cat8(t0, t1); w1 = cat8(t2, t3);
}
DI float mx2(float a, float b) { return __builtin_elementwise_maximum(a, b); }
DI float mx3(float a, float b, float c) { return __builtin_elementwise_maximum(__builtin_elementwise_maximum(a, b), c); }
DI void bcast2(float m, f32x2& c0, f32x2& c1) {
  const f32x2 a = sw16(m, m);
  c0 = sw32(a[0], a[0]); c1 = sw32(a[1], a[1]);
}
template <bool NRM, class F> DI void phi_map(const f32x4 (&p)[4][2], const float (&sc)[4], const float (&nrm)[2], F&& sink) {
  float r[4];
  f32x2 c[4];
#pragma unroll
  for (int t = 0; t < 4; ++t) {
    const float mx = mx2(mx3(mx3(p[t][0][0], p[t][0][1], p[t][0][2]), p[t][0][3], p[t][1][0]), mx3(p[t][1][1], p[t][1][2], p[t][1][3]));
    const float nm = mx2(mx3(mx3(-p[t][0][0], -p[t][0][1], -p[t][0][2]), -p[t][0][3], -p[t][1][0]), mx3(-p[t][1][1], -p[t][1][2], -p[t][1][3]));
    const f32x2 a = sw32(mx, nm);
    r[t] = mx2(a[0], a[1]);
  }
#pragma unroll
  for (int q = 0; q < 2; ++q) {
    const f32x2 a = sw16(r[2 * q], r[2 * q + 1]);
    bcast2(mx2(a[0], a[1]), c[2 * q], c[2 * q + 1]);
  }
#pragma unroll
  for (int t = 0; t < 4; ++t) c[t] *= -sc[t];
#pragma unroll
  for (int q = 0; q < 2; ++q) {
    f32x2 e[2][8], sm[2];
#pragma unroll
    for (int i = 0; i < 8; ++i)
#pragma unroll
      for (int j = 0; j < 2; ++j) {
        const float v = p[2 * q + j][i >> 2][i & 3];
        const f32x2 a = __builtin_elementwise_fma(f32x2{v, v}, f32x2{sc[2 * q + j], -sc[2 * q + j]}, c[2 * q + j]);
        e[j][i] = f32x2{__builtin_amdgcn_exp2f(a[0]), __builtin_amdgcn_exp2f(a[1])};
      }
    if constexpr (NRM) {
#pragma unroll
    for (int j = 0; j < 2; ++j) {
      sm[j] = ((e[j][0] + e[j][1]) + (e[j][2] + e[j][3])) + ((e[j][4] + e[j][5]) + (e[j][6] + e[j][7]));
      sm[j] = sw32(sm[j][0], sm[j][1]);
    }
    const f32x2 a = sw16(sm[0][0] + sm[0][1], sm[1][0] + sm[1][1]);
    float rc = __builtin_amdgcn_rcpf(a[0] + a[1]);
    rc *= nrm[q];
    bcast2(rc, sm[0], sm[1]);
    }
#pragma unroll
    for (int j = 0; j < 2; ++j) {
      unsigned pe[8];
#pragma unroll
      for (int i = 0; i < 8; ++i) if (NRM) e[j][i] *= sm[j];
#pragma unroll
      for (int i = 0; i < 4; ++i) { pe[i] = pk2(e[j][2 * i][0], e[j][2 * i + 1][0]); pe[4 + i] = pk2(e[j][2 * i][1], e[j][2 * i + 1][1]); }
      sink(2 * q + j, pe);
    }
  }
}

struct Sel { unsigned e[4], o[4]; };
DI unsigned selu(unsigned long long m, unsigned a, unsigned c) { return __float_as_uint(selm(m, __uint_as_float(a), __uint_as_float(c))); }
DI Sel make_sel() {
  Sel s;
  const unsigned e = selu(0xaaaaaaaaaaaaaaaaull, 0x01000c0cu, 0x0c0c0100u), o = selu(0xaaaaaaaaaaaaaaaaull, 0x03020c0cu, 0x0c0c0302u);
#pragma unroll
  for (int q = 0; q < 4; ++q) { s.e[q] = selu(0x0303030303030303ull << (2 * q), e, 0x0c0c0c0cu); s.o[q] = selu(0x0303030303030303ull << (2 * q), o, 0x0c0c0c0cu); }
  return s;
}
DI void pin_sel(Sel& s, unsigned (&v)[2], unsigned& a, unsigned& c) {
  asm volatile("" : "+v"(s.e[0]), "+v"(s.e[1]), "+v"(s.e[2]), "+v"(s.e[3]), "+v"(s.o[0]), "+v"(s.o[1]), "+v"(s.o[2]), "+v"(s.o[3]), "+v"(v[0]), "+v"(v[1]), "+v"(a), "+v"(c));
}
DI f32x4 gemv2(unsigned xp, u32x4 w0, u32x4 w1, const Sel& s, f32x4 d) {
  const u32x4 a0 = {__builtin_amdgcn_perm(xp, xp, s.e[0]), __builtin_amdgcn_perm(xp, xp, s.e[1]), __builtin_amdgcn_perm(xp, xp, s.e[2]), __builtin_amdgcn_perm(xp, xp, s.e[3])};
  const u32x4 a1 = {__builtin_amdgcn_perm(xp, xp, s.o[0]), __builtin_amdgcn_perm(xp, xp, s.o[1]), __builtin_amdgcn_perm(xp, xp, s.o[2]), __builtin_amdgcn_perm(xp, xp, s.o[3])};
  return MFMA(as8(a1), as8(w1), MFMA(as8(a0), as8(w0), d));
}
DI u32x4 gemv_fin(f32x4 d, unsigned t12) {
  u32x4 v;
#pragma unroll
  for (int e = 0; e < 4; ++e) { const float t = ror8(d[e]); v[e] = tag12(d[e] + sw32(t, t)[1], t12); }
  return v;
}

DI void opair(u32x4 (&o)[2], unsigned v0, unsigned v1, const char* p) { GL4(o[0], v0, p, 0, P_SC1); GL4(o[1], v1, p, 0, P_SC1); }
template <int N, bool COLD = false> DI void ocheck(u32x4 (&o)[2], unsigned v0, unsigned v1, const char* p) {
  asm volatile("s_waitcnt vmcnt(%2)" : "+v"(o[0]), "+v"(o[1]) : "n"(N));
  if (COLD && __builtin_expect(__ballot(max(o[0][3], o[1][3]) == ~0u) == 0ull, 1)) return;
#pragma unroll 1
  for (unsigned spins = 0; spins < SPIN && __ballot(max(o[0][3], o[1][3]) == ~0u) != 0ull; ++spins)
    asm volatile("s_sleep 2\n\tglobal_load_dwordx4 %0, %2, %4 sc1\n\tglobal_load_dwordx4 %1, %3, %4 sc1\n\ts_waitcnt vmcnt(0)"
                 : "+v"(o[0]), "+v"(o[1]) : "v"(v0), "v"(v1), "s"(p) : "memory");
}
template <int N> DI void ocheck3(u32x4 (&a)[2], u32x4 (&b)[2], u32x4 (&c)[2], unsigned v0, unsigned v1, const char* pa, const char* pb, const char* pc) {
  asm volatile("s_waitcnt vmcnt(%2)" : "+v"(a[0]), "+v"(a[1]) : "n"(N));
  if (__builtin_expect(__ballot(max(a[0][3], a[1][3]) == ~0u) == 0ull, 1)) return;
  asm volatile("s_waitcnt vmcnt(0)" : "+v"(b[0]), "+v"(b[1]), "+v"(c[0]), "+v"(c[1]));
#pragma unroll 1
  for (unsigned spins = 0; spins < SPIN; ++spins) {
    const unsigned long long ma = __ballot(max(a[0][3], a[1][3]) == ~0u), mb = __ballot(max(b[0][3], b[1][3]) == ~0u), mc = __ballot(max(c[0][3], c[1][3]) == ~0u);
    if (ma == 0ull) break;
    asm volatile("s_mov_b64 exec, %11\n\tglobal_load_dwordx4 %0, %6, %8 sc1\n\tglobal_load_dwordx4 %1, %7, %8 sc1\n\t"
                 "s_mov_b64 exec, %12\n\tglobal_load_dwordx4 %2, %6, %9 sc1\n\tglobal_load_dwordx4 %3, %7, %9 sc1\n\t"
                 "s_mov_b64 exec, %13\n\tglobal_load_dwordx4 %4, %6, %10 sc1\n\tglobal_load_dwordx4 %5, %7, %10 sc1\n\ts_mov_b64 exec, -1\n\ts_waitcnt vmcnt(0)"
                 : "+v"(a[0]), "+v"(a[1]), "+v"(b[0]), "+v"(b[1]), "+v"(c[0]), "+v"(c[1])
                 : "v"(v0), "v"(v1), "s"(pa), "s"(pb), "s"(pc), "s"(ma), "s"(mb), "s"(mc) : "memory");
  }
}
DI void sw16u(unsigned& a, unsigned& b) { auto r = __builtin_amdgcn_permlane16_swap(a, b, false, false); a = r[0]; b = r[1]; }
template <int N> DI void waitl2(s16x4& a, s16x4& c) { asm volatile("s_waitcnt lgkmcnt(%2)" : "+v"(a), "+v"(c) : "n"(N)); }
template <int N> DI void tie4(f32x4 (&a)[4]) { asm volatile("s_nop %4" : "+v"(a[0]), "+v"(a[1]), "+v"(a[2]), "+v"(a[3]) : "n"(N)); }
DI void rowst(f32x2 (&m)[4], unsigned a) {
  asm volatile("ds_read2_b32 %0, %4 offset1:128\n\tds_read2_b32 %1, %4 offset0:16 offset1:144\n\tds_read2_b32 %2, %4 offset0:32 offset1:160\n\t"
               "ds_read2_b32 %3, %4 offset0:48 offset1:176\n\ts_waitcnt lgkmcnt(0)" : "=&v"(m[0]), "=&v"(m[1]), "=&v"(m[2]), "=&v"(m[3]) : "v"(a) : "memory");
}
DI void reload4c(u32x4 (&p)[4], unsigned o, const char* sb, unsigned long long m0, unsigned long long m1, unsigned long long m2, unsigned long long m3) {
  asm volatile("s_mov_b64 exec, %6\n\tglobal_load_dwordx4 %0, %4, %5 sc1\n\ts_mov_b64 exec, %7\n\tglobal_load_dwordx4 %1, %4, %5 offset:1024 sc1\n\t"
               "s_mov_b64 exec, %8\n\tglobal_load_dwordx4 %2, %4, %5 offset:2048 sc1\n\ts_mov_b64 exec, %9\n\tglobal_load_dwordx4 %3, %4, %5 offset:3072 sc1\n\t"
               "s_mov_b64 exec, -1\n\ts_waitcnt vmcnt(0)"
               : "+v"(p[0]), "+v"(p[1]), "+v"(p[2]), "+v"(p[3]) : "v"(o), "s"(sb), "s"(m0), "s"(m1), "s"(m2), "s"(m3) : "memory");
}

#define PL(r, o) "global_load_dwordx4 v[" #r ":" #r "+3], %[vo], %[sb] offset:" #o " sc1\n\t"
#define PC(r, m) "v_cmp_eq_u32 " m ", -1, v[" #r "+3]\n\t"
#define PM(d, r) "v_mov_b32 v[" #d "], v[" #r "]\n\tv_mov_b32 v[" #d "+1], v[" #r "+1]\n\tv_mov_b32 v[" #d "+2], v[" #r "+2]\n\tv_mov_b32 v[" #d "+3], v[" #r "+3]\n\t"
template <int D> DI void poll2(u32x4 (&p)[2], u32x4 (&q)[4], unsigned vo, const char* sb) {
  unsigned long long m; unsigned n = SPIN;
  asm volatile(PL(128, 0) PL(132, 1024) "s_sleep %[d]\n\t" PL(136, 0) PL(140, 1024)
               "1:\n\ts_waitcnt vmcnt(2)\n\t" PC(128, "vcc") PC(132, "%[m]") "s_or_b64 exec, vcc, %[m]\n\ts_cbranch_scc0 2f\n\t" PL(128, 0) PL(132, 1024)
               "s_mov_b64 exec, -1\n\ts_waitcnt vmcnt(2)\n\t" PC(136, "vcc") PC(140, "%[m]") "s_or_b64 exec, vcc, %[m]\n\ts_cbranch_scc0 3f\n\t" PL(136, 0) PL(140, 1024)
               "s_mov_b64 exec, -1\n\ts_sub_u32 %[n], %[n], 1\n\ts_cbranch_scc0 1b\n\ts_waitcnt vmcnt(0)\n"
               "3:\n\ts_mov_b64 exec, -1\n\t" PM(120, 136) PM(124, 140) "s_branch 4f\n"
               "2:\n\ts_mov_b64 exec, -1\n\t" PM(120, 128) PM(124, 132) "4:\n\ts_nop 1"
               : "={v[120:123]}"(p[0]), "={v[124:127]}"(p[1]), "=&{v[128:131]}"(q[0]), "=&{v[132:135]}"(q[1]), "=&{v[136:139]}"(q[2]), "=&{v[140:143]}"(q[3]),
                 [m] "=&s"(m), [n] "+s"(n)
               : [vo] "v"(vo), [sb] "s"(sb), [d] "n"(D) : "vcc", "scc", "memory");
}
#define PL4(r) PL(r, 0) PL(r+4, 1024) PL(r+8, 2048) PL(r+12, 3072)
#define PM2(o, r) "v_mov_b64 v[128+" #o ":129+" #o "], v[" #r "+" #o ":" #r "+1+" #o "]\n\t"
#define PM16(r) PM2(0, r) PM2(2, r) PM2(8, r) PM2(10, r) PM2(4, r) PM2(6, r) PM2(12, r) PM2(14, r)
#define PQ8(c) c "{v[96:99]}"(q[0]), c "{v[100:103]}"(q[1]), c "{v[104:107]}"(q[2]), c "{v[108:111]}"(q[3]), c "{v[112:115]}"(q[4]), c "{v[116:119]}"(q[5]), c "{v[120:123]}"(q[6]), c "{v[124:127]}"(q[7])
#define PB(a, r) "v_bitop3_b32 " a ", " a ", v[" #r "], %[t] bitop3:0xf6\n\t"
#define PB7(a, r) "v_xor_b32 " a ", %[t], v[" #r "]\n\t" PB(a, r+1) PB(a, r+2) PB(a, r+3) PB(a, r+4) PB(a, r+5) PB(a, r+6) PB(a, r+7)
#define PCT(r) "s_waitcnt vmcnt(4)\n\t" PB7("v128", r) PB7("v129", r+8) "v_and_b32 v128, 0xfff, v128\n\tv_and_or_b32 v128, v129, %[tm], v128\n\tv_cmp_ne_u32 vcc, 0, v128\n\ts_or_b64 exec, vcc, vcc\n\t"
template <int D> DI void pollt(u32x4 (&p)[4], u32x4 (&q)[8], unsigned vo, const char* sb, unsigned t, unsigned tm) {
  unsigned n = SPIN;
  asm volatile(PL4(96) "s_sleep %[d]\n\t" PL4(112)
               "1:\n\t" PCT(96) "s_cbranch_scc0 2f\n\t" PL4(96) "s_mov_b64 exec, -1\n\t" PCT(112) "s_cbranch_scc0 3f\n\t" PL4(112)
               "s_mov_b64 exec, -1\n\ts_sub_u32 %[n], %[n], 1\n\ts_cbranch_scc0 1b\n\ts_waitcnt vmcnt(0)\n"
               "3:\n\ts_mov_b64 exec, -1\n\t" PM16(112) "s_branch 4f\n"
               "2:\n\ts_mov_b64 exec, -1\n\t" PM16(96) "4:\n\ts_nop 1"
               : "=&{v[128:131]}"(p[0]), "=&{v[132:135]}"(p[1]), "=&{v[136:139]}"(p[2]), "=&{v[140:143]}"(p[3]), PQ8("=&"), [n] "+s"(n)
               : [vo] "v"(vo), [sb] "s"(sb), [t] "s"(t), [tm] "s"(tm), [d] "n"(D) : "vcc", "scc", "memory");
}
#define PKEEP(pl) "v"(pl[0]), "v"(pl[1]), "v"(pl[2]), "v"(pl[3]), "v"(pl[4]), "v"(pl[5]), "v"(pl[6]), "v"(pl[7])
extern "C" __global__ __launch_bounds__(768) __attribute__((aligned(4096))) void kf_main(const u16* __restrict__ x, const u16* __restrict__ wqkv, char* ws, const u16* __restrict__ w_r,
                                               const u16* __restrict__ wphi, const u16* __restrict__ wo, const u16* __restrict__ w_gate,
                                               const u16* __restrict__ w_up, const u16* __restrict__ w_down, u16* __restrict__ out) {
  extern __shared__ __attribute__((aligned(16))) char smem[];
  const int b = blockIdx.x;
  const int xcd = b & 7, jj = b >> 3;
  const int panel = (xcd >> 2) * 8 + (jj & 7), hp = (xcd & 3) * 4 + (jj >> 3);
  const int tid = threadIdx.x;
  const int w = __builtin_amdgcn_readfirstlane(tid >> 6), lw = w & 3;
  int l = tid & 63, r15 = l & 15, g = l >> 4, tq = r15 >> 2, tp = l & 3;
  unsigned xo;
  auto relane = [&]() {
    asm volatile("v_mbcnt_lo_u32_b32 %0, -1, 0\n\tv_mbcnt_hi_u32_b32 %0, -1, %0" : "=v"(l));
    r15 = l & 15; g = l >> 4; tq = r15 >> 2; tp = l & 3; xo = r15 * 2048u + g * 16u;
  };
  const unsigned lbase = LDS_OFF(smem);
  auto pf3 = [&](int half) {
    touch(l * 6144u, (const char*)wqkv + (size_t)(half * 512 + (jj & 7) * 64) * 6144 + (jj >> 3) * 2048 + (xcd & 3) * 512 + 384, lbase + L_DU);
  };
  const char* pc0; unsigned clen;
  asm volatile("s_getpc_b64 %0\n.Lkf_here:\n\ts_mov_b32 %1, ((.Lkf_last-kf_main)|4095)-3-(.Lkf_here-kf_main)\n\ts_max_u32 %1, %1, ((.Lkf_cold-kf_main)|4095)-3-(.Lkf_here-kf_main)"
               : "=s"(pc0), "=s"(clen) :: "scc");
  if (__builtin_expect(w < 4, 1)) {
    if (__builtin_expect(w >= 2, 1)) {
      touchm(min(((jj * 2 + w - 2) * 8 + l) * 128u, clen), pc0, lbase + L_DU, 0xffull);
    }
    if (__builtin_expect(w == 0 && !(xcd & 1) && jj < 24, 1)) pf3(xcd >> 2);
  }
  unsigned* flg = (unsigned*)(smem + L_F);
  const bf16x8 ones = {0x3f80, 0x3f80, 0x3f80, 0x3f80, 0x3f80, 0x3f80, 0x3f80, 0x3f80};
  const f32x4 z4 = {0.f, 0.f, 0.f, 0.f};
  const int row0 = panel * 128 + hp * 8;
  auto ptile = [&](int p) { return p == 0 ? hp : p <= 12 ? ((hp | 3) + p) & 15 : hp ^ (p - 12); };
  auto kvo = [&]() { return (l >> 3) * 2048u + (l & 7) * 16u + ((panel ^ (l >> 3)) & 15) * 128u; };
  if (__builtin_expect((w >> 2) == 1, 1)) {
    const int srow = l >> 3, sch = l & 7, cg = sch, rg = srow;
    const int kt0 = 4 * (panel >> 2);
    unsigned G, Tw, t12;
    unsigned voA[2], voW[2];
    const char* sA = (const char*)(x + (size_t)panel * 128 * DM);
    const char* sQ = (const char*)(wqkv + hp * 64);
#pragma unroll
    for (int i = 0; i < 2; ++i) {
      const int row = lw * 32 + i * 8 + srow, wr = lw * 16 + i * 8 + srow;
      voA[i] = (unsigned)(row * 2048 + ((sch ^ ((row >> 1) & 7)) * 16));
      voW[i] = (unsigned)(wr * 6144 + ((sch ^ ((((wr >> 1) & 1) << 2) | (((wr >> 3) & 1) << 1))) * 16));
    }
    auto stage = [&](int S) {
      const int kt = (S + kt0) & 15;
      const char* a = sA + kt * 128; const char* wv = sQ + (size_t)kt * 64 * 6144;
      const unsigned d = lbase + (S % 3) * SLOT;
#pragma unroll
      for (int i = 0; i < 4; ++i) glds(voA[i & 1], a + (i >> 1) * 32768, d + lw * 4096 + i * 1024);
#pragma unroll
      for (int i = 0; i < 6; ++i) glds(voW[i & 1], wv + ((i >> 1) == 2 ? 0 : ((i >> 1) + 1) * 2048), d + ABYTES + (i >> 1) * WSLAB + lw * 2048 + (i & 1) * 1024);
    };
    auto last = [&](int i) {
      const int kt = (15 + kt0) & 15;
      const char* a = sA + kt * 128; const char* wv = sQ + (size_t)kt * 64 * 6144;
      if (i < 4) glds(voA[i & 1], a + (i >> 1) * 32768, lbase + L_EX + lw * 4096 + i * 1024);
      else if (i < 8) glds(voW[i & 1], wv + ((i >> 1) - 1) * 2048, lbase + L_EW + ((i >> 1) - 2) * WSLAB + lw * 2048 + (i & 1) * 1024);
      else glds(voW[i & 1], wv, lbase + ABYTES + 2 * WSLAB + lw * 2048 + (i & 1) * 1024);
    };
    __builtin_amdgcn_s_setprio(3);
    if (jj & 1) {
      const char* a = sA + kt0 * 128; const char* wv = sQ + (size_t)kt0 * 64 * 6144;
#pragma unroll
      for (int i = 0; i < 6; ++i) glds(voW[i & 1], wv + ((i >> 1) == 2 ? 0 : ((i >> 1) + 1) * 2048), lbase + ABYTES + (i >> 1) * WSLAB + lw * 2048 + (i & 1) * 1024);
#pragma unroll
      for (int i = 0; i < 4; ++i) glds(voA[i & 1], a + (i >> 1) * 32768, lbase + lw * 4096 + i * 1024);
    } else stage(0);
    const bool gu = (hp & 7) != 3;
    const int bb = gu ? (hp - (hp > 3) - (hp > 11)) * 16 + panel : 0;
    const int ke = bb & 7, cb = ((bb >> 3) * 2 + (lw & 1)) ^ ((lw >> 1) * 4);
    const int kc = (bb >> 4) * 4 + ((bb >> 1) & 3), cq = ((bb >> 3) & 1) | ((bb & 1) << 1);
    Sel sl;
    unsigned voO[2], guo, yo;
    auto later = [&]() {
      sl = make_sel();
#pragma unroll
      for (int i = 0; i < 2; ++i) voO[i] = (unsigned)((lw * 16 + i * 8 + srow) * 2048 + (sch ^ (((srow >> 1) & 1) | ((((srow >> 2) ^ i) & 1) << 2))) * 16);
      guo = WS_GU + (cb * 16 + (lw >> 1) * 8 + ke) * 256 + cg * 32 + g * 16;
      yo = WS_Y + ((cq * 4 + lw) * 56 + kc) * 256 + cg * 32 + g * 16;
      pin_sel(sl, voO, guo, yo);
    };
    later();
    stage(1);
    GL1(G, 0u, (const char*)ws, 0);
    u32x4 gv[16], dv[8];
    const uintptr_t gb = (uintptr_t)(((lw >> 1) ? w_up : w_gate) + (size_t)(ke * 128) * FF + cb * 64);
    const u32x4 grs = {(unsigned)gb, (unsigned)(gb >> 32) & 0xffffu, gu ? 127u * FF * 2 + 128u : 0u, 0x00020000u};
    const unsigned go = rg * (16u * FF * 2) + cg * 16u;
    const uintptr_t db = (uintptr_t)(w_down + (size_t)(kc * 64) * DM + cq * 256 + lw * 64);
    const u32x4 drs = {(unsigned)db, (unsigned)(db >> 32) & 0xffffu, gu ? 63u * DM * 2 + 128u : 0u, 0x00020000u};
    const unsigned dof = rg * 16384u + cg * 16u;
    auto strm = [&](auto I0, auto I1) {
      static_for<(decltype(I1)::value > decltype(I0)::value ? decltype(I1)::value - decltype(I0)::value : 0)>([&](auto I) {
        constexpr int i = decltype(I)::value + decltype(I0)::value;
        if constexpr (i < 16) bl4nt(gv[i], go, grs, i * (FF * 2u)); else bl4nt(dv[i - 16], dof, drs, (i - 16) * 2048u);
      });
    };
    const int wrow = 8 * g + tq, wsw = (((wrow >> 1) & 1) << 2) | (((wrow >> 3) & 1) << 1);
    const unsigned csa = lbase + ABYTES + (lw < 3 ? lw : 0) * WSLAB + wrow * 128 + (((2 * (panel & 3) + (tp >> 1)) ^ wsw) * 16) + 8 * (tp & 1);
    waitv<11>();
    RAW_BARRIER();
    float mean = 0.f, var = 1.f, c0 = 0.f, c1 = 0.f;
    f32x4 sm = z4;
    u32x4 pv; u32x2 pw; unsigned po, pc; unsigned long long pm;
    u32x4 ga[2], ca;
    const char* gab = ws + (lw < 3 ? WS_CS + (hp * 3 + lw) * 2048 : WS_ST + panel * 1024 - 1024);
    const unsigned go0 = l * 16u + (lw < 3 ? 0u : 1024u);
    static_for<13>([&](auto I) {
      constexpr int S = decltype(I)::value - 1;
      if constexpr (S >= 0) {
        waitv<10 + n_x(S - 2) + n_x(S - 1)>();
        RAW_BARRIER();
      }
      if constexpr (S < 3) {
        u32x2 c[4];
        cs_read(c, csa + ((S + 1) % 3) * SLOT);
        stage(S + 3);
        cs_sum(c, c0, c1);
      } else stage(S + 3);
      if constexpr (S == 0) {
        asm volatile("" : "+v"(G));
        Tw = __builtin_amdgcn_readfirstlane(G) + 1u;
        if ((Tw & 0xfffu) == 0u) ++Tw;
        t12 = Tw & 0xfffu;
        if (tid == 256) flg[F_T] = Tw;
      }
      if constexpr (S == SB0) {
        const f32x4* rp = (const f32x4*)(smem + L_REC) + (l & 7);
        sm = ((rp[0] + rp[8]) + (rp[16] + rp[24])) + ((rp[32] + rp[40]) + (rp[48] + rp[56]));
        pinf(sm);
      }
      if constexpr (S == SM) {
        mean = sm[0] * (1.0f / 1024.0f);
        var = __builtin_fmaf(-mean, mean, sm[2] * (1.0f / 1024.0f));
        if (lw == 3 && __ballot(!(var * 64.0f >= mean * mean)) != 0ull) {
#pragma unroll 1
          for (int r = 0; r < 8; ++r) {
            const float m = __builtin_bit_cast(float, __builtin_amdgcn_readlane(__builtin_bit_cast(int, mean), r));
            const u32x4* p = (const u32x4*)(x + (size_t)(row0 + r) * DM) + l * 2;
            const u32x4 a = p[0], c = p[1];
            float q = 0.f;
#pragma unroll
            for (int i = 0; i < 8; ++i) {
              const unsigned u = i < 4 ? a[i & 3] : c[i & 3];
              const float e0 = bflo(u) - m, e1 = bfhi(u) - m;
              q = __builtin_fmaf(e0, e0, q); q = __builtin_fmaf(e1, e1, q);
            }
            q = wave_total(q) * (1.0f / 1024.0f);
            if ((l & 7) == r) var = q;
          }
          GAP(1); MARK(1);
        }
        mean = rbf(mean); var = rbf(rbf(var) + 1e-5f);
        pin1(mean); pin1(var);
      }
      if constexpr (S == SM) { var = rbf(sqrtf(var)); pin1(var); }
      if constexpr (S == SM) { sm[1] = rbf(__builtin_fmaf(-mean, sm[3], sm[1]) / var); pinf(sm); }
      if constexpr (S == SM) {
        if (lw == 3) {
          const float r = sm[1];
          const unsigned u = __float_as_uint(r);
          const unsigned key = (r == r ? ((u & 0x80000000u) ? (~u & 0xffff0000u) : (u | 0x80000000u)) : 0u) | (unsigned)(2047 - row0 - (l & 7));
          const unsigned km = dpp_umax<0x141>(dpp_umax<0x4e>(dpp_umax<0xb1>(key)));
          pv = u32x4{__float_as_uint(mean) | (__float_as_uint(var) >> 16), key, Tw, Tw};
          po = WS_ST + (row0 + l) * 8;
          pm = __ballot(key == km) & 0xffull;
        } else {
          pv = u32x4{__float_as_uint(sum_g(c0 + c1)), 0u, Tw, Tw};
          po = WS_CS + (((hp * 3 + lw) * 4 + (panel >> 2)) * 64 + (panel & 3) * 16 + l) * 8;
          pm = 0ull;
        }
        pw = u32x2{pv[0], Tw};
        pc = WS_CA + (panel * 16 + hp) * 16;
        pub_pin(pv, pw, po, pc);
      }
      if constexpr (S == PUB) pub(pv, pw, po, pc, lw == 3 ? 0xffull : 0xffffull, pm, ws);
      if constexpr (S == GAS) { GL4(ga[0], go0, gab, 0, P_SC1); GL4(ga[1], l * 16u, gab, 1024, P_SC1); }
      static_for<8>([&](auto J) { if constexpr (ES0 + decltype(J)::value * ESL / 8 == S) last(decltype(J)::value); });
      if constexpr (S == CAS) GL4(ca, WS_CA + lw * 1024 + l * 16, ws, 0, P_SC1);
      if constexpr (S == CTS) touch4(min(((jj * 4 + lw) * 4 + l) * 128u, clen), pc0, lbase + L_DU);
      if constexpr (S == 0) glds(l * 16u, (const char*)(wphi + hp * 2048 + lw * 512), lbase + L_WP + lw * 1024);
    });
    const unsigned fa = lbase + L_F, fb = fa + lw * 4;
    auto ca_bad = [&]() { return (ca[2] ^ Tw) | (ca[3] ^ Tw); };
    auto ca_put = [&]() {
      unsigned m;
#define MX(a, c) "v_max_u32_dpp %0, " a ", " a " " c " row_mask:0xf bank_mask:0xf\n\ts_nop 1\n\t"
      asm volatile(MX("%3", "quad_perm:[1,0,3,2]") MX("%0", "quad_perm:[2,3,0,1]") MX("%0", "row_half_mirror") "v_max_u32_dpp %0, %0, %0 row_mirror row_mask:0xf bank_mask:0xf\n\t"
                   "v_cmp_eq_u32 vcc, %0, %3\n\ts_mov_b64 exec, vcc\n\tds_max_u64 %1, %2 offset:%4\n\ts_mov_b64 exec, -1"
                   : "=&v"(m) : "v"(fa), "v"(u32x2{ca[0], ca[1]}), "v"(ca[1]), "n"(F_CA * 4) : "vcc", "memory");
      raise<F_SD>(fb);
    };
    asm volatile("s_waitcnt vmcnt(%1)" : "+v"(ca) : "n"(10 + n_x(10) + n_x(11)) : "memory");
    RAW_BARRIER();
    last(8); last(9);
    strm(IC(0), IC(NF));
    const bool cok = all0(ca_bad());
    if (__builtin_expect(cok, 1)) ca_put();
    auto ga_bad = [&]() { return (ga[0][1] ^ Tw) | (ga[0][3] ^ Tw) | (ga[1][1] ^ Tw) | (ga[1][3] ^ Tw); };
    auto ga_put = [&]() {
      if (lw < 3) {
        const float u0 = __uint_as_float(ga[0][0]) + __uint_as_float(ga[1][0]), u1 = __uint_as_float(ga[0][2]) + __uint_as_float(ga[1][2]);
        const f32x2 a = sw32(u0, u1);
        ((float*)(smem + L_C))[lw * 64 + (l & 31) * 2 + (l >> 5)] = a[0] + a[1];
      } else {
        *(f32x2*)(smem + L_MN + l * 8) = f32x2{-bfhi(ga[0][0]), -bfhi(ga[0][2])};
        *(f32x2*)(smem + L_RS + l * 8) = f32x2{__builtin_amdgcn_rcpf(bflo(ga[0][0])), __builtin_amdgcn_rcpf(bflo(ga[0][2]))};
        *(unsigned*)(smem + L_DN + l * 4) = (ga[0][0] & 0xffffu) | (ga[0][2] << 16);
      }
      raise<F_ST>(fb);
    };
    asm volatile("s_waitcnt vmcnt(%2)" : "+v"(ga[0]), "+v"(ga[1]) : "n"(2 + NF) : "memory");
    RAW_BARRIER();
    u32x4 fs; u32x2 sel;
    asm volatile("ds_read_b128 %0, %2 offset:%3\n\tds_read_b64 %1, %2 offset:%4" : "=&v"(fs), "=&v"(sel) : "v"(fa), "n"(F_SD * 4), "n"(F_CA * 4) : "memory");
    strm(IC(NF), IC(NF + NG));
    if (__builtin_expect(!all0(ga_bad()), 0))
#pragma unroll 1
      for (unsigned spins = 0; spins < SPIN; ++spins) {
        asm volatile("global_load_dwordx4 %0, %2, %4 sc1\n\tglobal_load_dwordx4 %1, %3, %4 offset:1024 sc1\n\ts_waitcnt vmcnt(0)"
                     : "+v"(ga[0]), "+v"(ga[1]) : "v"(go0), "v"(l * 16u), "s"(gab) : "memory");
        if (all0(ga_bad())) break;
      }
    ga_put();
    asm volatile("s_waitcnt lgkmcnt(0)" : "+v"(fs), "+v"(sel) :: "memory");
    if (__builtin_expect(!__builtin_amdgcn_readfirstlane(fs[0] & fs[1] & fs[2] & fs[3]), 0)) {
      if (!cok) {
#pragma unroll 1
        for (unsigned spins = 0; spins < SPIN; ++spins) {
          asm volatile("global_load_dwordx4 %0, %1, %2 sc1\n\ts_waitcnt vmcnt(0)" : "+v"(ca) : "v"(WS_CA + lw * 1024 + l * 16), "s"(ws) : "memory");
          if (all0(ca_bad())) break;
        }
        ca_put();
      }
      lds_wait4(flg + F_SD);
      asm volatile("ds_read_b64 %0, %1 offset:%2\n\ts_waitcnt lgkmcnt(0)" : "=v"(sel) : "v"(fa), "n"(F_CA * 4) : "memory");
    }
    const unsigned key = __builtin_amdgcn_readfirstlane(sel[1]);
    const int idx = 2047 - (int)(key & 2047u);
    unsigned xd;
    GL1P(xd, l * 4u, (const char*)(x + (size_t)idx * DM + ke * 128));
    strm(IC(NF + NG), IC(24));
    waitv<25>();
    raise<F_GO>(fb);
    __builtin_amdgcn_s_setprio(FP0);
    auto wotile = [&](int p) {
      const char* wv = (const char*)(wo + (size_t)(ptile(p) * 64) * DM + hp * 64);
      waitv<WNO>();
      glds(voO[0], wv, lbase + p * 8192 + lw * 2048); glds(voO[1], wv, lbase + p * 8192 + lw * 2048 + 1024);
    };
    asm volatile("s_waitcnt vmcnt(8)" : "+v"(xd), "+v"(gv[0]), "+v"(gv[1]), "+v"(gv[2]), "+v"(gv[3]), "+v"(gv[4]), "+v"(gv[5]), "+v"(gv[6]), "+v"(gv[7]) :: "memory");
    asm volatile("" : "+v"(gv[8]), "+v"(gv[9]), "+v"(gv[10]), "+v"(gv[11]), "+v"(gv[12]), "+v"(gv[13]), "+v"(gv[14]), "+v"(gv[15]));
    char* hs = smem + L_H + lw * 256;
    {
      const float sm = bfhi(sel[0]), rden = __builtin_amdgcn_rcpf(bflo(sel[0]));
      const unsigned d = pk2(bflo(xd) - sm, bfhi(xd) - sm);
      *(unsigned*)(hs + l * 4) = pk2(bflo(d) * rden, bfhi(d) * rden);
      const u32x4 x0 = *(const u32x4*)(hs + rg * 32), x1 = *(const u32x4*)(hs + rg * 32 + 16);
      f32x4 d0 = gemv2(x0[0], gv[0], gv[1], sl, z4), d1 = gemv2(x0[1], gv[2], gv[3], sl, z4);
      d0 = gemv2(x0[2], gv[4], gv[5], sl, d0); d1 = gemv2(x0[3], gv[6], gv[7], sl, d1);
      d0 = gemv2(x1[0], gv[8], gv[9], sl, d0); d1 = gemv2(x1[1], gv[10], gv[11], sl, d1);
      d0 = gemv2(x1[2], gv[12], gv[13], sl, d0); d1 = gemv2(x1[3], gv[14], gv[15], sl, d1);
      const u32x4 v = gemv_fin(d0 + d1, t12);
      if (gu && (l & 40) == 0) gs4(guo, ws, v);
    }
    __builtin_amdgcn_s_setprio(FPR);
    lds_wait4(flg + F_LD);
    wotile(12); wotile(13); wotile(15);
    lds_wait4(flg + F_IM);
    wotile(0); wotile(1); wotile(2); wotile(3); wotile(7); wotile(8);
    lds_wait4(flg + F_BL);
    u32x4 p[4];
    const char* gp = ws + WS_GU + kc * 4096;
    lds_wait1(flg + F_SL + lw);
    wotile(4); wotile(5); wotile(6); wotile(9); wotile(10); wotile(11); wotile(14);
    GL4(p[0], l * 16u, gp, 0, P_SC1); GL4(p[1], l * 16u, gp, 1024, P_SC1); GL4(p[2], l * 16u, gp, 2048, P_SC1); GL4(p[3], l * 16u, gp, 3072, P_SC1);
    waitv<4>();
    lds_set(flg + F_WO + lw, 1u, l);
    if (FPR != FPE) __builtin_amdgcn_s_setprio(FPE);
    waitv4<0>(p);
    asm volatile("" : "+v"(dv[0]), "+v"(dv[1]), "+v"(dv[2]), "+v"(dv[3]), "+v"(dv[4]), "+v"(dv[5]), "+v"(dv[6]), "+v"(dv[7]) :: "memory");
    if (gu) {
#pragma unroll 1
      for (unsigned spins = 0; spins < SPIN; ++spins) {
        const unsigned long long m0 = __ballot(bad4(p[0], t12) & 0xfffu), m1 = __ballot(bad4(p[1], t12) & 0xfffu), m2 = __ballot(bad4(p[2], t12) & 0xfffu),
                                 m3 = __ballot(bad4(p[3], t12) & 0xfffu);
        if ((m0 | m1 | m2 | m3) == 0ull) break;
        reload4c(p, l * 16u, gp, m0, m1, m2, m3);
      }
      float hv[4];
#pragma unroll
      for (int e = 0; e < 4; ++e) {
        const float gg = rbf(sum_g(untag12(p[0][e]) + untag12(p[1][e]))), uu = rbf(sum_g(untag12(p[2][e]) + untag12(p[3][e])));
        hv[e] = rbf(silu_r(gg)) * uu;
      }
      if (l < 16) *(u32x2*)(hs + l * 8) = u32x2{pk2(hv[0], hv[1]), pk2(hv[2], hv[3])};
      const u32x4 hp4 = *(const u32x4*)(hs + rg * 16);
      f32x4 d0 = gemv2(hp4[0], dv[0], dv[1], sl, z4), d1 = gemv2(hp4[1], dv[2], dv[3], sl, z4);
      d0 = gemv2(hp4[2], dv[4], dv[5], sl, d0); d1 = gemv2(hp4[3], dv[6], dv[7], sl, d1);
      const u32x4 v = gemv_fin(d0 + d1, t12);
      if ((l & 40) == 0) gs4(yo, ws, v);
    }
    if (FPG != FPE) __builtin_amdgcn_s_setprio(FPG);
    if ((idx >> 7) == panel) {
      const char* yb = ws + WS_Y + hp * 56 * 256 + lw * 4096;
      u32x2 xr;
      GL2(xr, r15 * 8u, (const char*)(x + (size_t)idx * DM + hp * 64), 0);
      const unsigned tm = lw < 3 ? 0xfffu : 0u;
      u32x4 pl[8];
      lds_wait4(flg + F_OS);
      if (PYD) __builtin_amdgcn_s_sleep(PYD);
      pollt<PPY>(p, pl, l * 16u, yb, t12, tm);
      asm volatile("" : "+v"(xr));
      f32x4 y;
#pragma unroll
      for (int e = 0; e < 4; ++e) y[e] = sum_g((untag12(p[0][e]) + untag12(p[1][e])) + (lw < 3 ? untag12(p[2][e]) + untag12(p[3][e]) : 0.f));
      if (lw) {
        if (l < 16) *(f32x4*)(hs + l * 16) = y;
        lds_set(flg + F_AL + lw, 1u, l);
      } else {
      if (l == 0) flg[F_AL] = 1u;
      lds_wait4(flg + F_AL);
      y = (y + *(const f32x4*)(hs + 256 + r15 * 16)) + (*(const f32x4*)(hs + 512 + r15 * 16) + *(const f32x4*)(hs + 768 + r15 * 16));
      const int row = idx & 127;
      const float mn = ((const float*)(smem + L_MN))[row], rs = ((const float*)(smem + L_RS))[row];
      const float r = __uint_as_float((key & 0x80000000u) ? (key & 0x7fff0000u) : (~key & 0xffff0000u));
      const float sg = rbf(1.0f / (1.0f + __expf(-r)));
      float gi[4];
#pragma unroll
      for (int e = 0; e < 4; ++e) {
        const float xe = (e & 1) ? bfhi(xr[e >> 1]) : bflo(xr[e >> 1]);
        gi[e] = silu_r(rbf(rbf(rbf(xe + mn) * rs) + rbf(rbf(y[e]) * sg)));
      }
      if (l < 16) *(u32x2*)(smem + L_GI + l * 8) = u32x2{pk2(gi[0], gi[1]), pk2(gi[2], gi[3])};
      lds_set(flg + F_GT, 1u, l);
      }
      asm volatile("s_waitcnt vmcnt(0)" :: PKEEP(pl) : "memory");
    }
    if (b == 0 && tid == 256) gs1(0u, ws, Tw);
    GAP(2);
    return;
  }
  const int s = w >> 3;
  const int wt = lw & 1, wc = lw >> 1;
  const char* ob = ws + WS_O + (size_t)(panel * 4 + lw) * 65536;
  const char* xfb = (const char*)(x + (size_t)(panel * 128 + lw * 32 + s * 16) * DM + hp * 64);
  u32x4 xv[2], gq[2];
  unsigned T;
  auto gate = [&]() {
    const float mn = ((const float*)(smem + L_MN))[lw * 32 + s * 16 + r15], rs = ((const float*)(smem + L_RS))[lw * 32 + s * 16 + r15];
#pragma unroll
    for (int hf = 0; hf < 2; ++hf) {
      unsigned x0 = xv[hf][0], x1 = xv[hf][1], x2 = xv[hf][2], x3 = xv[hf][3];
      sw16u(x0, x2); sw16u(x1, x3);
      xv[hf] = u32x4{x0, x1, x2, x3};
#pragma unroll
      for (int e = 0; e < 4; ++e) {
        const unsigned d = pk2(bflo(xv[hf][e]) + mn, bfhi(xv[hf][e]) + mn);
        const unsigned y = pk2(bflo(d) * rs, bfhi(d) * rs);
        gq[hf][e] = pk2(silu_r(bflo(y)), silu_r(bfhi(y)));
      }
    }
    pin2(gq); pin2(xv);
  };
  const int wrow = 8 * g + tq, wsw = (((wrow >> 1) & 1) << 2) | (((wrow >> 3) & 1) << 1);
  unsigned asw[3][2], wa[3][2];
#pragma unroll
  for (int q = 0; q < 3; ++q) {
#pragma unroll
    for (int ks = 0; ks < 2; ++ks) asw[q][ks] = lbase + (wt * 64 + r15) * 128 + (((4 * ks + g) ^ ((r15 >> 1) & 7)) * 16) + q * SLOT;
#pragma unroll
    for (int c = 0; c < 2; ++c) wa[q][c] = lbase + ABYTES + wrow * 128 + (((wc * 4 + 2 * c + (tp >> 1)) ^ wsw) * 16) + 8 * (tp & 1) + q * SLOT;
  }
  if (s) {
    u32x4 xa[4], wr[4];
    unsigned xo8 = (l & 7) * 2048u + ((l >> 3) & 1) * 64u + g * 16u, ra = lbase + L_REC + (lw * 16 + r15) * 16;
    const char* xb = (const char*)(x + (size_t)row0 * DM + lw * 256);
    {
      const u32x4 o4 = __builtin_bit_cast(u32x4, ones);
      wr[0] = o4; wr[1] = o4; wr[2] = o4; wr[3] = o4;
      asm volatile("s_mov_b64 exec, %6\n\tglobal_load_dwordx4 %0, %4, %5\n\tglobal_load_dwordx4 %1, %4, %5 offset:128\n\tglobal_load_dwordx4 %2, %4, %5 offset:256\n\t"
                   "global_load_dwordx4 %3, %4, %5 offset:384\n\ts_mov_b64 exec, -1"
                   : "+v"(wr[0]), "+v"(wr[1]), "+v"(wr[2]), "+v"(wr[3]) : "v"((l & 1) * 64u + g * 16u), "s"(w_r + lw * 256), "s"(0x3333333333333333ull));
    }
    unsigned tco = (lw & 1) ? (l >> 2) * 2048u + (l & 3) * 128u : l * 6144u;
    const char* tcb = (lw & 1) ? (const char*)wo + jj * 65536 + (lw >> 1) * 32768 + (xcd & 3) * 512
                               : (const char*)wqkv + (size_t)(((xcd >> 2) ^ 1) * 512 + (jj & 7) * 64) * 6144 + min(jj >> 3, 2) * 2048 + (xcd & 3) * 512 + 384;
    unsigned long long tcm = __ballot((lw & 1) || (lw == 0 && !(xcd & 1) && jj < 24));
    asm volatile("" : "+v"(xo8), "+v"(ra), "+v"(tco));
    asm volatile("" : "+s"(xb), "+s"(tcb), "+s"(tcm));
    auto st_load = [&]() { GL4(xa[0], xo8, xb, 0, P_DEF); GL4(xa[1], xo8, xb, 128, P_DEF); GL4(xa[2], xo8, xb, 256, P_DEF); GL4(xa[3], xo8, xb, 384, P_DEF); };
    auto st_sum = [&]() {
      waitv4<0>(xa); waitv4<0>(wr);
      f32x4 d1 = z4, d2 = z4, d3 = z4;
#pragma unroll
      for (int j = 0; j < 4; ++j) { d2 = MFMA(as8(xa[j]), as8(xa[j]), d2); d3 = MFMA(as8(wr[j]), ones, d3); d1 = MFMA(as8(wr[j]), as8(xa[j]), d1); }
      const unsigned long long m1 = 0xaaaaaaaaaaaaaaaaull, m2 = 0xccccccccccccccccull, m8 = 0xff00ff00ff00ff00ull;
      asm volatile("s_nop 7" : "+v"(d1), "+v"(d2), "+v"(d3));
      const f32x4 v = {d1[2], selm(m8, d1[1], d1[0]), selm(m2, selm(m1, d2[3], d2[2]), selm(m1, d2[1], d2[0])), selm(m8, d3[1], d3[0])};
      asm volatile("s_mov_b64 exec, %2\n\tds_write_b128 %0, %1\n\ts_mov_b64 exec, -1" :: "v"(ra), "v"(v), "s"(0xf0000f0000f0000full) : "memory");
      SB();
    };
    f32x4 acc[4][2];
    bf16x8 w0, w1;
    float sc[4];
    const char* kb = ws + WS_KVR + hp * 8192 + wc * 4096;
    unsigned l16, fsl = LDS_OFF(flg + F_SL + 2 * wc);
    {
#pragma unroll
      for (int t = 0; t < 4; ++t) { acc[t][0] = z4; acc[t][1] = z4; }
      int ts = (hp - 4 * (panel >> 2)) & 15;
      const unsigned xr = lbase + (ts == 15 ? L_EX : (ts % 3) * SLOT) + (lw * 32 + 16 + r15) * 128 + ((g ^ ((r15 >> 1) & 7)) * 16);
      ts = min(ts, 13);
      Frag<2> f[2];
      auto rd = [&](Frag<2>& y, auto I, auto OW, unsigned pa, const unsigned (&pw)[2]) {
        constexpr int i = decltype(I)::value, ow = decltype(OW)::value + 2 * WSLAB;
        if constexpr (i < 4) ds_128<i * 2048>(y.af[i], pa);
        else if constexpr (i & 1) ds_tr<ow + 512>(y.wh[(i - 4) >> 1], pw[(i - 4) >> 1]);
        else ds_tr<ow>(y.wl[(i - 4) >> 1], pw[(i - 4) >> 1]);
      };
      auto step = [&](Frag<2>& c, Frag<2>& y, auto OW, unsigned pa, const unsigned (&pw)[2], auto RD, auto XT) {
        static_for<8>([&](auto I) {
          constexpr int i = decltype(I)::value;
          acc[i & 3][i >> 2] = MFMA(cat8(c.wl[i >> 2], c.wh[i >> 2]), c.af[i & 3], acc[i & 3][i >> 2]);
          SB();
          if constexpr (i < 4 && decltype(RD)::value) { rd(y, IC(2 * i), OW, pa, pw); rd(y, IC(2 * i + 1), OW, pa, pw); SB(); }
          if constexpr (i == 4 && decltype(XT)::value >= 0) { ds_if<decltype(XT)::value>(xv, xr, ts); SB(); }
        });
      };
      auto tile = [&](auto J, auto BAR, auto RD, auto&& side) {
        constexpr int j = decltype(J)::value % 3, n = (j + 1) % 3;
        f[0].wait();
        step(f[0], f[1], IC(4096), asw[j][1], wa[j], IC(1), IC(decltype(BAR)::value ? decltype(J)::value : -1));
        f[1].wait();
        if constexpr (decltype(BAR)::value) { __builtin_amdgcn_s_barrier(); asm volatile("" ::: "memory"); }
        side();
        if constexpr (!decltype(RD)::value) { if (l == 0) __hip_atomic_store(flg + F_BL + lw, 1u, __ATOMIC_RELAXED, __HIP_MEMORY_SCOPE_WORKGROUP); SB(); }
        step(f[1], f[0], IC(0), asw[n][0], wa[n], RD, IC(-1));
      };
      auto sents = [&]() {
        unsigned ln;
        asm volatile("v_mbcnt_lo_u32_b32 %0, -1, 0\n\tv_mbcnt_hi_u32_b32 %0, -1, %0" : "=v"(ln));
        u32x4 ff = {~0u, ~0u, ~0u, ~0u};
        asm volatile("" : "+v"(ff));
        const unsigned ko = (ln >> 3) * 2048u + (ln & 7) * 16u + ((panel ^ (ln >> 3)) & 15) * 128u;
        static_for<4>([&](auto J) { gs4o<decltype(J)::value * 1024>(ln * 16u, ws + WS_O + (size_t)(panel * 4 + lw) * 65536 + hp * 4096, ff); });
        gs4(ko, ws + WS_KVP + (hp * 4 + lw) * 32768, ff);
        gs4(ko ^ 17408u, ws + WS_KVP + (hp * 4 + lw) * 32768, ff);
        gs4m(ln * 16u, ws + WS_KVR + hp * 8192 + lw * 2048 + panel * 128, ff);
        SB();
      };
      asm volatile("" : "=v"(xv[0]), "=v"(xv[1]));
      st_load();
      TICK();
      static_for<8>([&](auto I) { rd(f[0], I, IC(0), asw[0][0], wa[0]); });
      static_for<13>([&](auto J) {
        constexpr int t = decltype(J)::value;
        if constexpr (t == SBP) { waitv<0>(); SB(); }
        tile(J, IC(1), IC(1), [&]() {
          if constexpr (t == SBI) {
            __builtin_amdgcn_s_setprio(3);
            sents();
            __builtin_amdgcn_s_setprio(0);
            SB();
          }
        });
        if constexpr (t == 0) st_sum();
        if constexpr (t == STC) touchm(tco, tcb, lbase + L_DU, tcm);
        if constexpr (t == 2) MARK(2);
        if constexpr (t == 4 || t == 11) {
        }
      });
      __builtin_amdgcn_s_barrier(); asm volatile("" ::: "memory");
      VISIT(3);
      relane();
      asw[0][0] += L_EX; asw[0][1] += L_EX;
      ds_if<13>(xv, xr, ts);
      wphi_frag(w0, w1, lbase + L_WP + wc * 2048 + (4 * g + tq) * 64 + tp * 8);
      lds_wait4(flg + F_ST);
      {
        const f32x4 c0 = *(const f32x4*)(smem + L_C + 512 + (wc * 32 + 4 * g) * 4), c1 = *(const f32x4*)(smem + L_C + 512 + (wc * 32 + 16 + 4 * g) * 4);
        f32x2 mr[4];
        rowst(mr, lbase + L_MN + (wt * 64 + r15) * 4);
#pragma unroll
        for (int t = 0; t < 4; ++t) {
          sc[t] = mr[t][1] * L2E;
          acc[t][0] += mr[t][0] * c0; acc[t][1] += mr[t][0] * c1;
          asm volatile("" : "+v"(sc[t]), "+v"(acc[t][0]), "+v"(acc[t][1]));
        }
      }
      asm volatile("" : "+v"(xv[0]), "+v"(xv[1]), "+v"(w0), "+v"(w1));
      gate();
      SB();
      asm volatile("" : "+s"(kb));
      l16 = l * 16u;
      asm volatile("" : "+v"(l16), "+v"(fsl));
      GAP(6);
      VISIT(6);
      lds_wait1(flg + F_IM + lw);
      __builtin_amdgcn_s_setprio(1);
      tile(IC(1), IC(0), IC(1), []() {});
      lds_wait4(flg + F_GO);
      tile(IC(2), IC(0), IC(1), []() {}); tile(IC(0), IC(0), IC(0), []() {});
    }
    bf16x8 pq[4][2];
    {
      f32x4 p[4][2];
      const float one[2] = {1.f, 1.f};
#pragma unroll
      for (int t = 0; t < 4; ++t) {
        const f32x4 v0 = acc[t][0], v1 = acc[t][1];
        const bf16x8 qp = as8(u32x4{pk2(v0[0], v0[1]), pk2(v0[2], v0[3]), pk2(v1[0], v1[1]), pk2(v1[2], v1[3])});
        p[t][0] = MFMA(w0, qp, z4); p[t][1] = MFMA(w1, qp, z4);
      }
      phi_map<false>(p, sc, one, [&](int t, const unsigned (&pe)[8]) {
        pq[t][0] = as8(u32x4{pe[0], pe[1], pe[2], pe[3]}); pq[t][1] = as8(u32x4{pe[4], pe[5], pe[6], pe[7]});
      });
    }
    asm volatile("" : "+v"(pq[0][0]), "+v"(pq[0][1]), "+v"(pq[1][0]), "+v"(pq[1][1]), "+v"(pq[2][0]), "+v"(pq[2][1]), "+v"(pq[3][0]), "+v"(pq[3][1]));
    {
      f32x2 rd[4];
      u32x4 kf[4];
      PAD(6);
#pragma unroll 1
      for (unsigned spins = 0; spins < SPIN; ++spins) {
        u32x2 v; unsigned long long ck;
        asm volatile("ds_read_b64 %0, %2\n\ts_memrealtime %1\n\ts_waitcnt lgkmcnt(0)" : "=v"(v), "=s"(ck) : "v"(fsl) : "memory");
        const unsigned t0 = __builtin_amdgcn_readfirstlane(v[0]), t1 = __builtin_amdgcn_readfirstlane(v[1]), t = __builtin_amdgcn_readfirstlane((unsigned)ck);
        if ((t0 & t1) && min(t - t0, t - t1) >= D2) break;
      }
      GL4(kf[0], l16, kb, 0, P_SC1); GL4(kf[1], l16, kb, 1024, P_SC1); GL4(kf[2], l16, kb, 2048, P_SC1); GL4(kf[3], l16, kb, 3072, P_SC1);
      lds_wait4(flg + F_BL);
      lds_wait2(flg + F_KV + 2 * wc);
      {
        bf16x8 z0 = *(const bf16x8*)(smem + L_Z + wc * 128 + g * 16), z1 = *(const bf16x8*)(smem + L_Z + wc * 128 + 64 + g * 16);
        if (l & 1) { z0 = ones; z1 = ones; }
#pragma unroll
        for (int t = 0; t < 4; ++t) {
          const f32x4 a = MFMA(z0, pq[t][0], z4), c = MFMA(z1, pq[t][1], z4);
          const f32x2 r = {__builtin_amdgcn_rcpf(a[1]), __builtin_amdgcn_rcpf(c[1])};
          rd[t] = r * __builtin_amdgcn_rcpf(__builtin_fmaf(a[0], r[0], __builtin_fmaf(c[0], r[1], 1e-6f)));
          asm volatile("" : "+v"(rd[t]));
        }
      }
      const char* pb = ws + WS_O + (size_t)(panel * 4 + wt * 2) * 65536 + hp * 4096 + wc * 2048;
      unsigned l2 = l16 + 65536u, oa = lbase + L_EX + wc * 8192 + wt * 4096 + l * 16, fo = LDS_OFF(flg + F_OS + lw);
      asm volatile("" : "+s"(pb));
      asm volatile("" : "+v"(l2), "+v"(oa), "+v"(fo));
      waitv4<0>(kf);
      if (__builtin_expect(__ballot(max(max(kf[0][3], kf[1][3]), max(kf[2][3], kf[3][3])) == ~0u) != 0ull, 0)) {
#pragma unroll 1
        for (unsigned spins = 0; spins < SPIN; ++spins) {
          const unsigned long long m0 = __ballot(kf[0][3] == ~0u), m1 = __ballot(kf[1][3] == ~0u), m2 = __ballot(kf[2][3] == ~0u), m3 = __ballot(kf[3][3] == ~0u);
          if ((m0 | m1 | m2 | m3) == 0ull) break;
          reload4c(kf, l16, kb, m0, m1, m2, m3);
        }
      }
      u32x4 ov[4];
      f32x4 n[4][4];
      auto mm = [&](int t, int i) { n[t][i] = MFMA(as8(kf[i]), pq[t][i >> 1], z4); SB(); };
#pragma unroll
      for (int i = 0; i < 8; ++i) mm(i >> 2, i & 3);
      static_for<4>([&](auto TT) {
        constexpr int t = decltype(TT)::value;
        f32x4 a = n[t][0] * rd[t][0], c = n[t][1] * rd[t][0];
        SB(); if (t < 2) mm(t + 2, 0);
        a += n[t][2] * rd[t][1];
        SB(); if (t < 2) mm(t + 2, 1);
        c += n[t][3] * rd[t][1];
        SB(); if (t < 2) mm(t + 2, 2);
        ov[t] = u32x4{pk2(a[0], a[1]), pk2(a[2], a[3]), pk2(c[0], c[1]), nosent(pk2(c[2], c[3]))};
        SB(); if (t < 2) mm(t + 2, 3);
        asm volatile("global_store_dwordx4 %0, %1, %2 offset:%3 sc1" :: "v"(t < 2 ? l16 : l2), "v"(ov[t]), "s"(pb), "n"((t & 1) * 1024) : "memory");
        SB();
      });
      asm volatile("ds_write_b128 %0, %1\n\tds_write_b128 %0, %2 offset:1024\n\tds_write_b128 %0, %3 offset:2048\n\tds_write_b128 %0, %4 offset:3072\n\tds_write_b32 %5, %5\n\ts_nop 0"
                   :: "v"(oa), "v"(ov[0]), "v"(ov[1]), "v"(ov[2]), "v"(ov[3]), "v"(fo) : "memory");
    }
    __builtin_amdgcn_s_setprio(0);
    GAP(3);
  } else {
    if (tid < 20) ((u32x4*)flg)[tid] = u32x4{0, 0, 0, 0};
    f32x4 acc[4][4];
    Frag<4> f[2];
    u32x4 fs;
    f32x4 c0, c1;
    f32x2 mr[4];
    s16x4 wq[4];
    unsigned mra = lbase + L_MN + (wt * 64 + r15) * 4, cca = lbase + L_C + (wc * 32 + 4 * g) * 4, wpa = lbase + L_WP + wc * 2048 + (4 * g + tq) * 64 + tp * 8, fa = lbase + L_F;
    char* img = smem + lw * 8192;
    unsigned va = lbase + SLOT + ABYTES + lw * 4096 + l * 16, fb = lbase + L_F + lw * 4, ia, wi[4], ko = kvo(), da = lbase + L_DN + wt * 128 + g * 8,
             zo = WS_ZP + ((hp * 4 + lw) * 16 + panel) * 128 + (r15 * 4 + g) * 16, m4a = lbase + L_MN + (wt * 64 + 4 * g) * 4, cva = lbase + L_C + (64 + wc * 32 + r15) * 4;
    {
      const int row = 4 * g + tq, sx = (((row >> 1) & 3) << 2) | (row & 1) | (((row >> 3) & 1) << 1);
      ia = LDS_OFF(img) + row * 128 + (((8 * wt + tp) ^ sx) * 8);
      const int sw = (((r15 >> 1) & 3) << 2) | (r15 & 1) | (((r15 >> 3) & 1) << 1);
#pragma unroll
      for (int i = 0; i < 4; ++i) wi[i] = LDS_OFF(img) + r15 * 128 + (((4 * i + g) ^ sw) * 8);
    }
    {
#pragma unroll
      for (int t = 0; t < 4; ++t)
#pragma unroll
        for (int c = 0; c < 4; ++c) acc[t][c] = z4;
      auto rd = [&](Frag<4>& y, auto I, auto OW, unsigned pa, const unsigned (&pw)[2]) {
        constexpr int i = decltype(I)::value, ow = decltype(OW)::value;
        if constexpr (i < 4) ds_128<i * 2048>(y.af[i], pa);
        else {
          constexpr int c = (i - 4) >> 1;
          if constexpr (i & 1) ds_tr<ow + (c >> 1) * WSLAB + 512>(y.wh[c], pw[c & 1]);
          else ds_tr<ow + (c >> 1) * WSLAB>(y.wl[c], pw[c & 1]);
        }
      };
      auto step = [&](Frag<4>& c, Frag<4>& y, auto OW, unsigned pa, const unsigned (&pw)[2], auto&& more) {
        static_for<16>([&](auto I) {
          constexpr int i = decltype(I)::value, t = i & 3, cb = i >> 2;
          if constexpr (cb < 2) mfma_t(acc[t][cb], cat8(c.wl[cb], c.wh[cb]), __builtin_bit_cast(u32x4, c.af[t]));
          else mfma_t(acc[t][cb], c.af[t], __builtin_bit_cast(u32x4, cat8(c.wl[cb], c.wh[cb])));
          if constexpr (i < 12) rd(y, I, OW, pa, pw); else more(I);
        });
      };
      auto none = [](auto) {};
      __builtin_amdgcn_s_setprio(2);
#pragma unroll
      for (int t = 0; t < 4; ++t) asm volatile("" : "+v"(acc[t][0]), "+v"(acc[t][1]), "+v"(acc[t][2]), "+v"(acc[t][3]));
      asm volatile("" : "+v"(mra), "+v"(cca), "+v"(wpa), "+v"(fa), "+v"(va), "+v"(fb), "+v"(ia), "+v"(wi[0]), "+v"(wi[1]), "+v"(wi[2]), "+v"(wi[3]),
                   "+v"(ko), "+v"(da), "+v"(zo), "+v"(m4a), "+v"(cva));
      TICK();
      static_for<12>([&](auto I) { rd(f[0], I, IC(0), asw[0][0], wa[0]); });
#pragma unroll 1
      for (int it = 0; it < 5; ++it) {
        static_for<3>([&](auto J) {
          constexpr int j = decltype(J)::value, n = (j + 1) % 3;
          f[0].wait();
          step(f[0], f[1], IC(4096), asw[j][1], wa[j], none);
          if constexpr (j == 1) {
            const unsigned dx = it == 4 ? L_EX : 0, dw = it == 4 ? L_EW - ABYTES : 0;
            asw[0][0] += dx; asw[0][1] += dx; wa[0][0] += dw; wa[0][1] += dw;
            asm volatile("" : "+v"(asw[0][0]), "+v"(asw[0][1]), "+v"(wa[0][0]), "+v"(wa[0][1]));
          }
          f[1].wait();
          if (j < 2 || __builtin_expect(it < 4, 1)) __builtin_amdgcn_s_barrier();
          asm volatile("" ::: "memory");
          step(f[1], f[0], IC(0), asw[n][0], wa[n], none);
        });
      }
      f[0].wait();
      step(f[0], f[1], IC(4096), asw[0][1], wa[0], [&](auto I) {
        constexpr int i = decltype(I)::value;
        if constexpr (i == 12) { ds_tr<0>(wq[0], wpa); ds_tr<1024>(wq[1], wpa); ds_tr<32>(wq[2], wpa); }
        if constexpr (i == 13) { ds_tr<1056>(wq[3], wpa); ds_32<F_T * 4>(T, fa); }
      });
      asm volatile("s_waitcnt lgkmcnt(5)" : "+v"(f[1].af[0]), "+v"(f[1].af[1]), "+v"(f[1].af[2]), "+v"(f[1].af[3]), "+v"(f[1].wl[0]), "+v"(f[1].wh[0]), "+v"(f[1].wl[1]), "+v"(f[1].wh[1]),
                   "+v"(f[1].wl[2]), "+v"(f[1].wh[2]), "+v"(f[1].wl[3]), "+v"(f[1].wh[3]));
      static_for<8>([&](auto I) {
        constexpr int i = decltype(I)::value, t = i >> 1, cb = i & 1;
        acc[t][cb] = MFMA(cat8(f[1].wl[cb], f[1].wh[cb]), f[1].af[t], acc[t][cb]);
        SB();
        if constexpr (i == 0) { ds_128v<F_ST * 4>(fs, fa); ds_128v<0>(c0, cca); ds_128v<64>(c1, cca); }
        if constexpr (i == 1) { ds_2x32<0>(mr[0], mra); ds_2x32<16>(mr[1], mra); }
        if constexpr (i == 2) { ds_2x32<32>(mr[2], mra); ds_2x32<48>(mr[3], mra); }
        if constexpr (i == 3) raise<F_LD>(fb);
        SB();
      });
    }
    asm volatile("s_waitcnt lgkmcnt(0)" : "+v"(fs), "+v"(c0), "+v"(c1), "+v"(mr[0]), "+v"(mr[1]), "+v"(mr[2]), "+v"(mr[3]), "+v"(wq[0]), "+v"(wq[1]), "+v"(wq[2]), "+v"(wq[3]), "+v"(T));
    if (__builtin_expect(!__builtin_amdgcn_readfirstlane(fs[0] & fs[1] & fs[2] & fs[3]), 0)) {
      lds_wait4(flg + F_ST);
      asm volatile("ds_read_b128 %0, %2\n\tds_read_b128 %1, %2 offset:64\n\ts_waitcnt lgkmcnt(0)" : "=&v"(c0), "=&v"(c1) : "v"(cca) : "memory");
      rowst(mr, mra);
    }
    T = __builtin_amdgcn_readfirstlane(T);
    const unsigned t12 = T & 0xfffu;
    const bf16x8 w0 = cat8(wq[0], wq[1]), w1 = cat8(wq[2], wq[3]);
    u32x4 vp[2][4];
    {
      f32x4 p[4][2], m4[4];
      f32x2 cv;
      float sc[4], nrm[2];
#pragma unroll
      for (int t = 0; t < 4; ++t) {
        sc[t] = mr[t][1] * L2E;
        if (t < 2) nrm[t] = selm(0xffff0000ffff0000ull, mr[2 * t + 1][1], mr[2 * t][1]);
        const f32x4 v0 = mr[t][0] * c0 + acc[t][0], v1 = mr[t][0] * c1 + acc[t][1];
        const bf16x8 kc = as8(u32x4{pk2(v0[0], v0[1]), pk2(v0[2], v0[3]), pk2(v1[0], v1[1]), pk2(v1[2], v1[3])});
        p[t][0] = MFMA(w0, kc, z4); p[t][1] = MFMA(w1, kc, z4);
      }
      auto vl = [&](int i0, int i1) {
#pragma unroll
        for (int i = i0; i < i1; ++i)
          acc[i & 3][2 + (i >> 2)] = MFMA(f[1].af[i & 3], cat8(f[1].wl[2 + (i >> 2)], f[1].wh[2 + (i >> 2)]), acc[i & 3][2 + (i >> 2)]);
      };
      SB();
      PAD(3);
      SB();
      phi_map<true>(p, sc, nrm, [&](int t, const unsigned (&pe)[8]) {
        if (t < 2) { SB(); vl(4 * t, 4 * t + 4); SB(); }
        if (t == 2) asm volatile("s_waitcnt lgkmcnt(0)" : "+v"(m4[0]), "+v"(m4[1]), "+v"(m4[2]), "+v"(m4[3]), "+v"(cv) :: "memory");
#pragma unroll
        for (int i = 0; i < 4; ++i) *(u32x2*)(smem + (wi[i] - lbase) + t * 2048) = u32x2{pe[2 * i], pe[2 * i + 1]};
        if (t == 1) {
          asm volatile("ds_read_b128 %0, %5\n\tds_read_b128 %1, %5 offset:64\n\tds_read_b128 %2, %5 offset:128\n\tds_read_b128 %3, %5 offset:192\n\tds_read2_b32 %4, %6 offset1:16"
                       : "=&v"(m4[0]), "=&v"(m4[1]), "=&v"(m4[2]), "=&v"(m4[3]), "=&v"(cv)
                       : "v"(m4a), "v"(cva) : "memory");
          SB();
        }
        if (t == 2) {
#pragma unroll
          for (int d = 0; d < 2; ++d)
#pragma unroll
            for (int q = 0; q < 2; ++q) {
              const f32x4 a = m4[2 * q] * cv[d] + acc[2 * q][2 + d], c = m4[2 * q + 1] * cv[d] + acc[2 * q + 1][2 + d];
              vp[0][2 * q + d] = u32x4{pk2(a[0], a[1]), pk2(a[2], a[3]), pk2(c[0], c[1]), pk2(c[2], c[3])};
            }
          asm volatile("ds_write_b128 %0, %1\n\tds_write_b128 %0, %2 offset:1024\n\tds_write_b128 %0, %3 offset:2048\n\tds_write_b128 %0, %4 offset:3072"
                       :: "v"(va), "v"(vp[0][0]), "v"(vp[0][1]), "v"(vp[0][2]), "v"(vp[0][3]) : "memory");
        }
      });
    }
    const char* kb = ws + WS_KVP + (hp * 4 + lw) * 32768;
    {
      s16x4 tl[2][2], th[2][2];
#define IMG8 "ds_read_b64_tr_b16 %0, %8\n\tds_read_b64_tr_b16 %1, %8 offset:2048\n\tds_read_b64_tr_b16 %2, %9\n\tds_read_b64_tr_b16 %3, %9 offset:2048\n\t" \
             "ds_read_b64_tr_b16 %4, %8 offset:4096\n\tds_read_b64_tr_b16 %5, %8 offset:6144\n\tds_read_b64_tr_b16 %6, %9 offset:4096\n\tds_read_b64_tr_b16 %7, %9 offset:6144\n\t"
#define IMGO(c) c(tl[0][0]), c(th[0][0]), c(tl[0][1]), c(th[0][1]), c(tl[1][0]), c(th[1][0]), c(tl[1][1]), c(th[1][1])
      raise<F_EX>(fb);
      asm volatile(IMG8 : IMGO("=&v") : "v"(ia), "v"(ia ^ 32u) : "memory");
      {
        u32x4 d0, d1, d2, d3;
        asm volatile("ds_read2_b64 %0, %4 offset1:4\n\tds_read2_b64 %1, %4 offset0:8 offset1:12\n\tds_read2_b64 %2, %5 offset1:4\n\tds_read2_b64 %3, %5 offset0:8 offset1:12"
                     : "=&v"(d0), "=&v"(d1), "=&v"(d2), "=&v"(d3) : "v"(da), "v"(da ^ 128u) : "memory");
        unsigned o1 = ko ^ 17408u, ib = ia ^ 8192u, ic = ia ^ 8224u, fx, fe = (fb ^ 4u) + F_EX * 4;
        va ^= 4096u;
        asm volatile("s_waitcnt lgkmcnt(4)" : IMGO("+v"), "+v"(o1), "+v"(ib), "+v"(ic), "+v"(va), "+v"(fe), "+s"(kb) :: "memory");
        f32x4 kva[2][2], za[2];
        bf16x8 a[2][2];
#pragma unroll
        for (int q = 0; q < 2; ++q)
#pragma unroll
          for (int fb = 0; fb < 2; ++fb) {
            a[q][fb] = cat8(tl[q][fb], th[q][fb]);
#pragma unroll
            for (int d = 0; d < 2; ++d) kva[fb][d] = MFMA(a[q][fb], as8(vp[0][2 * q + d]), q ? kva[fb][d] : z4);
          }
        SB();
        s16x4 ul[2][2], uh[2][2];
#define IMGP(c) c(ul[0][0]), c(uh[0][0]), c(ul[0][1]), c(uh[0][1]), c(ul[1][0]), c(uh[1][0]), c(ul[1][1]), c(uh[1][1])
#define OTH "ds_read_b32 %10, %15\n\t" IMG8 "ds_read_b128 %11, %16\n\tds_read_b128 %12, %16 offset:1024\n\tds_read_b128 %13, %16 offset:2048\n\tds_read_b128 %14, %16 offset:3072"
        asm volatile("s_waitcnt lgkmcnt(0)" : "+v"(d0), "+v"(d1), "+v"(d2), "+v"(d3) :: "memory");
        asm volatile(OTH : IMGP("=&v"), "+v"(ib), "+v"(ic), "=&v"(fx), "=&v"(vp[1][0]), "=&v"(vp[1][1]), "=&v"(vp[1][2]), "=&v"(vp[1][3]) : "v"(fe), "v"(va) : "memory");
        SB();
#pragma unroll
        for (int fb = 0; fb < 2; ++fb) za[fb] = MFMA(a[1][fb], as8(d1), MFMA(a[0][fb], as8(d0), z4));
        SB();
        asm volatile("s_waitcnt lgkmcnt(0)" : IMGP("+v"), "+v"(fx), "+v"(vp[1][0]), "+v"(vp[1][1]), "+v"(vp[1][2]), "+v"(vp[1][3]) :: "memory");
        if (__builtin_expect(__builtin_amdgcn_readfirstlane(fx) == 0u, 0)) {
          asm volatile(".Lkf_cold:");
#pragma unroll 1
          for (unsigned spins = 0; spins < SPIN && __builtin_amdgcn_readfirstlane(fx) == 0u; ++spins)
            asm volatile(OTH "\n\ts_waitcnt lgkmcnt(0)" : IMGP("+v"), "+v"(ib), "+v"(ic), "+v"(fx), "+v"(vp[1][0]), "+v"(vp[1][1]), "+v"(vp[1][2]), "+v"(vp[1][3]) : "v"(fe), "v"(va) : "memory");
        }
#pragma unroll
        for (int d = 0; d < 2; ++d)
#pragma unroll
          for (int q = 0; q < 2; ++q)
#pragma unroll
            for (int fb = 0; fb < 2; ++fb) kva[fb][d] = MFMA(cat8(ul[q][fb], uh[q][fb]), as8(vp[1][2 * q + d]), kva[fb][d]);
        SB();
#pragma unroll
        for (int d = 0; d < 2; ++d) {
          za[d] = MFMA(cat8(ul[1][d], uh[1][d]), as8(d3), MFMA(cat8(ul[0][d], uh[0][d]), as8(d2), za[d]));
          const f32x4 u = kva[0][d], v = kva[1][d];
          gs4(d ? o1 : ko, kb, u32x4{pk2(u[0], u[1]), pk2(u[2], u[3]), pk2(v[0], v[1]), nosent(pk2(v[2], v[3]))});
        }
        raise<F_IM>(fb);
        relane();
        const f32x4 z = (l & 1) ? za[1] : za[0];
        if (r15 < 2) gs4(zo, ws, u32x4{tag12(z[0], t12), tag12(z[1], t12), tag12(z[2], t12), tag12(z[3], t12)});
      }
    }
    GL4(xv[0], xo, xfb, 0, P_DEF); GL4(xv[1], xo, xfb, 64, P_DEF);
    const char* zb = ws + WS_ZP + (hp * 4 + lw) * 2048;
    u32x4 zp[2], pl[4];
    {
      u32x4 p[2];
      const unsigned one = (l & 1) ? 0x3f800000u : 0x3f80u;
      u32x4 s0 = {(l & 2) ? 0u : one, (l & 2) ? one : 0u, 0u, 0u}, s1 = {0u, 0u, s0[0], s0[1]};
      asm volatile("" : "+v"(s0), "+v"(s1));
      GAP(7);
      VISIT(7);
      if (PD1) __builtin_amdgcn_s_sleep(PD1);
      poll2<PP1>(p, pl, l * 16u + panel * 2048, kb);
      asm volatile("" : "+v"(xv[0]), "+v"(xv[1]));
      GL4(zp[0], l * 16u, zb, 0, P_SC1); GL4(zp[1], l * 16u, zb, 1024, P_SC1);
      f32x4 u = MFMA(as8(s0), as8(p[1]), MFMA(as8(s0), as8(p[0]), z4)), v = MFMA(as8(s1), as8(p[1]), MFMA(as8(s1), as8(p[0]), z4));
#pragma unroll
      for (int e = 0; e < 4; ++e) { u[e] += ror8(u[e]); v[e] += ror8(v[e]); }
      gs4m(l * 16u, ws + WS_KVR + hp * 8192 + lw * 2048 + panel * 128, u32x4{pk2(u[0], u[1]), pk2(u[2], u[3]), pk2(v[0], v[1]), nosent(pk2(v[2], v[3]))});
      asm volatile("ds_write_b32 %0, %1 offset:%2" :: "v"(fb), "v"(now() | 1u), "n"(F_SL * 4) : "memory");
    }
    {
      asm volatile("s_waitcnt vmcnt(1)" : "+v"(zp[0]), "+v"(zp[1]) :: "memory");
      asm volatile("" :: "v"(pl[0]), "v"(pl[1]), "v"(pl[2]), "v"(pl[3]));
#pragma unroll 1
      for (unsigned spins = 0; spins < SPIN; ++spins) {
        const unsigned long long m0 = __ballot(bad4(zp[0], t12) & 0xfffu), m1 = __ballot(bad4(zp[1], t12) & 0xfffu);
        if ((m0 | m1) == 0ull) break;
        reload2(zp, l * 16u, zb, m0, m1);
      }
      float z[4];
#pragma unroll
      for (int e = 0; e < 4; ++e) { const float q = sum_g(untag12(zp[0][e]) + untag12(zp[1][e])); z[e] = q + ror8(q); }
      if (l < 8) *(u32x2*)(smem + L_Z + lw * 64 + (l & 3) * 16 + (l >> 2) * 8) = u32x2{pk2(z[0], z[1]), pk2(z[2], z[3])};
      lds_set(flg + F_KV + lw, 1u, l);
    }
    __builtin_amdgcn_s_setprio(0);
    gate();
  }
  {
    relane();
    u32x4 of[NS][2], o0[2];
    f32x4 acc[2][4];
#pragma unroll
    for (int t = 0; t < 2; ++t)
#pragma unroll
      for (int c = 0; c < 4; ++c) acc[t][c] = z4;
    tie8<1>(acc);
    unsigned wb[4], wx[4];
    {
      const int row = 4 * g + tq, f = ((row >> 1) & 1) | ((((row >> 2) ^ (row >> 3)) & 1) << 2);
#pragma unroll
      for (int nb = 0; nb < 4; ++nb) wb[nb] = lbase + s * 4096 + row * 128 + ((((tp & 2) + (nb & 1) + 4 * (nb >> 1)) ^ f) * 16) + (tp & 1) * 8;
    }
    const unsigned v0 = l * 16u + s * 3072u, v1 = l * 16u + s * 1024u + 1024u, v3 = l * 16u + 2048u - s * 1024u;
    auto oadr = [&](int h) { return ob + ptile(h) * 4096; };
    s16x4 wl[5], wh[5], el[2][4], eh[2][4];
    auto wrd = [&](auto K, auto HI) {
      constexpr int k = decltype(K)::value, h = k >> 2, nb = k & 3, o = (h & 7) * 8192;
      if constexpr (decltype(HI)::value) ds_tr<o + 2048>(wh[k % 5], wb[nb]);
      else {
        if constexpr (h == 8) { wb[nb] += 65536u; asm volatile("" : "+v"(wb[nb])); }
        ds_tr<o>(wl[k % 5], wb[nb]);
      }
    };
    unsigned xa = lbase + lw * 8192 + s * 4096 + l * 16, fa = LDS_OFF(flg + F_X + s * 4 + lw), fg = LDS_OFF(flg + F_PG + s * 4);
    f32x2 xf[8], gf[8];
    char* op = (char*)(out + (size_t)(panel * 128 + lw * 32 + s * 16) * DM + hp * 64);
    unsigned oo = (l & 7) * 2048u + (l & 8) * 8u + g * 16u, oa = lbase + L_EX + s * 9216 + lw * 2048 + l * 16;
    char* op2 = op + 16384;
    int idx;
    auto pre = [&]() {
#pragma unroll
      for (int i = 0; i < 8; ++i) {
        xf[i] = f32x2{bflo(xv[i >> 2][i & 3]), bfhi(xv[i >> 2][i & 3])}; gf[i] = f32x2{bflo(gq[i >> 2][i & 3]), bfhi(gq[i >> 2][i & 3])};
        asm volatile("" : "+v"(xf[i]), "+v"(gf[i]));
      }
      asm volatile("" : "+v"(oo), "+s"(op), "+s"(op2), "+v"(xa), "+v"(fa), "+v"(fg), "+v"(oa), "+v"(wb[0]), "+v"(wb[1]), "+v"(wb[2]), "+v"(wb[3]));
    };
    pre();
    lds_wait4(flg + F_OS);
    if (!s) __builtin_amdgcn_s_sleep(PD3);
    static_for<NS>([&](auto H) { opair(of[decltype(H)::value], v0, v1, oadr(decltype(H)::value + 1)); });
    lds_wait4(flg + F_WO);
    idx = 2047 - (int)(lds_get(flg + F_CA + 1) & 2047u);
    asm volatile("ds_read_b128 %0, %2\n\tds_read_b128 %1, %3" : "=&v"(o0[0]), "=&v"(o0[1]) : "v"(oa), "v"(oa ^ 1024u) : "memory");
    static_for<4>([&](auto NB) { wrd(NB, IC(0)); wrd(NB, IC(1)); });
    u32x4 pg;
    static_for<14>([&](auto H) {
      constexpr int h = decltype(H)::value, x0 = h == XH || h == XH + 1, x1 = h == XH + 1 || h == XH + 2;
      u32x4 (&o)[2] = h ? of[(h + NS - 1) % NS] : o0;
      if constexpr (h == BH0) { if (s) __builtin_amdgcn_s_setprio(1); }
      static_for<4>([&](auto NB) {
        constexpr int nb = decltype(NB)::value, k = 4 * h + nb;
        constexpr bool ld = nb == 3 && h && h + NS < 16;
        const bf16x8 wf = cat8(wl[k % 5], wh[k % 5]);
        mfma_w<x1 + (3 - nb) * (2 + 2 * x1) + nb * (2 + 2 * x0) + (h == 5 || h == 13)>(acc[1][nb], wf, o[1]);
        wrd(IC(k + 4), IC(0));
        if constexpr (x0) ds_tr<(h - XH + 6) * 8192>(el[h - XH][nb], wx[nb]);
        if constexpr (ld) GL4(o[1], h + NS < 14 ? v1 : v3, oadr(h + NS), 0, P_SC1);
        mfma_t(acc[0][nb], wf, o[0]);
        wrd(IC(k + 4), IC(1));
        if constexpr (x0) ds_tr<(h - XH + 6) * 8192 + 2048>(eh[h - XH][nb], wx[nb]);
        if constexpr (ld) GL4(o[0], v0, oadr(h + NS), 0, P_SC1);
      });
      if constexpr (h == 4) asm volatile("ds_write_b32 %0, %0" :: "v"(fg + lw * 4u) : "memory");
      if constexpr (h == LM7) PAD(7);
      if constexpr (h == 3) {
      }
      if constexpr (h == XH - 1) {
#pragma unroll
        for (int nb = 0; nb < 4; ++nb) { wx[nb] = wb[nb] ^ 4096u; asm volatile("" : "+v"(wx[nb])); }
      }
      if constexpr (h == 12) asm volatile("ds_read_b128 %0, %1" : "=v"(pg) : "v"(fg) : "memory");
      if constexpr (h < OC3) ocheck3<4>(of[h % 3], of[(h + 1) % 3], of[(h + 2) % 3], v0, v1, oadr(h + 1), oadr(h + 2), oadr(h + 3));
      else if constexpr (h < 13) ocheck<2 * ((h + NS < 15 ? h + NS : 15) - h - 1), true>(of[h % NS], v0, v1, oadr(h + 1));
    });
    asm volatile("s_waitcnt lgkmcnt(%1)" : "+v"(pg) : "n"(XH == 12 ? 15 : 8));
    if (__builtin_expect(!__builtin_amdgcn_readfirstlane(pg[0] & pg[1] & pg[2] & pg[3]), 0)) lds_wait4(flg + F_PG + s * 4);
    asm volatile("s_nop 1\n\tds_write_b128 %0, %1\n\tds_write_b128 %0, %2 offset:1024\n\tds_write_b128 %0, %3 offset:2048\n\tds_write_b128 %0, %4 offset:3072\n\tds_write_b32 %5, %5"
                 :: "v"(xa), "v"(acc[1][0]), "v"(acc[1][1]), "v"(acc[1][2]), "v"(acc[1][3]), "v"(fa) : "memory");
    __builtin_amdgcn_s_setprio(0);
    ocheck<2, true>(of[13 % NS], v0, v3, oadr(14));
    static_for<4>([&](auto NB) {
      constexpr int nb = decltype(NB)::value, k = 56 + nb;
      mfma_w<nb ? 15 : 5>(acc[0][nb], cat8(wl[k % 5], wh[k % 5]), of[13 % NS][0]);
      wrd(IC(k + 4), IC(0)); wrd(IC(k + 4), IC(1));
    });
#pragma unroll
    for (int nb = 0; nb < 4; ++nb) mfma_t(acc[0][nb], cat8(el[0][nb], eh[0][nb]), of[13 % NS][1]);
    unsigned fx;
    fa ^= 16u; xa ^= 4096u;
    asm volatile("ds_read_b32 %0, %5\n\tds_read_b128 %1, %6\n\tds_read_b128 %2, %6 offset:1024\n\tds_read_b128 %3, %6 offset:2048\n\tds_read_b128 %4, %6 offset:3072"
                 : "=&v"(fx), "=&v"(acc[1][0]), "=&v"(acc[1][1]), "=&v"(acc[1][2]), "=&v"(acc[1][3]) : "v"(fa), "v"(xa) : "memory");
    ocheck<0, true>(of[14 % NS], v0, v3, oadr(15));
#pragma unroll
    for (int nb = 0; nb < 4; ++nb) mfma_t(acc[0][nb], cat8(el[1][nb], eh[1][nb]), of[14 % NS][1]);
    static_for<4>([&](auto NB) { constexpr int nb = decltype(NB)::value, k = 60 + nb; mfma_w<5>(acc[0][nb], cat8(wl[k % 5], wh[k % 5]), of[14 % NS][0]); });
    if (__builtin_expect((idx >> 4) == panel * 8 + lw * 2 + s, 0)) {
      lds_wait1(flg + F_GT);
      relane();
      if (r15 == (idx & 15)) {
        const u32x2* gi = (const u32x2*)(smem + L_GI + (g >> 1) * 32 + (g & 1) * 8);
#pragma unroll
        for (int i = 0; i < 4; ++i) {
          const u32x2 a = gi[(i & 1) * 2 + (i >> 1) * 8];
          gf[2 * i] = f32x2{bflo(a[0]), bfhi(a[0])}; gf[2 * i + 1] = f32x2{bflo(a[1]), bfhi(a[1])};
        }
      }
    }
    asm volatile("s_waitcnt lgkmcnt(0)" : "+v"(fx), "+v"(acc[1][0]), "+v"(acc[1][1]), "+v"(acc[1][2]), "+v"(acc[1][3]) :: "memory");
    if (__builtin_expect(__builtin_amdgcn_readfirstlane(fx) == 0u, 0))
#pragma unroll 1
    for (unsigned spins = 0; spins < SPIN && __builtin_amdgcn_readfirstlane(fx) == 0u; ++spins)
      asm volatile("s_sleep 1\n\tds_read_b32 %0, %5\n\tds_read_b128 %1, %6\n\tds_read_b128 %2, %6 offset:1024\n\tds_read_b128 %3, %6 offset:2048\n\t"
                   "ds_read_b128 %4, %6 offset:3072\n\ts_waitcnt lgkmcnt(0)"
                   : "+v"(fx), "+v"(acc[1][0]), "+v"(acc[1][1]), "+v"(acc[1][2]), "+v"(acc[1][3]) : "v"(fa), "v"(xa) : "memory");
#pragma unroll
    for (int i = 0; i < 8; ++i) xf[i] = __builtin_elementwise_fma(f32x2{acc[1][i >> 1][2 * (i & 1)], acc[1][i >> 1][2 * (i & 1) + 1]}, gf[i], xf[i]);
    asm volatile("" : "+v"(xf[0]), "+v"(xf[1]), "+v"(xf[2]), "+v"(xf[3]), "+v"(xf[4]), "+v"(xf[5]), "+v"(xf[6]), "+v"(xf[7]),
                 "+v"(acc[0][0]), "+v"(acc[0][1]), "+v"(acc[0][2]), "+v"(acc[0][3]));
    unsigned rr[8];
#pragma unroll
    for (int i = 0; i < 8; ++i) {
      const f32x2 r = __builtin_elementwise_fma(f32x2{acc[0][i >> 1][2 * (i & 1)], acc[0][i >> 1][2 * (i & 1) + 1]}, gf[i], xf[i]);
      rr[i] = pk2(r[0], r[1]);
    }
    {
      sw16u(rr[0], rr[2]); sw16u(rr[1], rr[3]); sw16u(rr[4], rr[6]); sw16u(rr[5], rr[7]);
      unsigned a0, a1, a2, a3, c0, c1, c2, c3;
#define CD(d, x, y) "v_cndmask_b32_dpp " d ", " x ", " y ", vcc row_ror:8 row_mask:0xf bank_mask:0xf\n\t"
      asm volatile("s_mov_b32 vcc_lo, 0x00ff00ff\n\ts_mov_b32 vcc_hi, 0x00ff00ff\n\t" CD("%0", "%12", "%8") CD("%2", "%14", "%10") CD("%1", "%13", "%9") CD("%3", "%15", "%11")
                   "s_not_b64 vcc, vcc\n\t" CD("%4", "%8", "%12") CD("%5", "%9", "%13") CD("%6", "%10", "%14") CD("%7", "%11", "%15")
                   : "=&v"(a0), "=&v"(a1), "=&v"(a2), "=&v"(a3), "=&v"(c0), "=&v"(c1), "=&v"(c2), "=&v"(c3)
                   : "v"(rr[0]), "v"(rr[1]), "v"(rr[2]), "v"(rr[3]), "v"(rr[4]), "v"(rr[5]), "v"(rr[6]), "v"(rr[7]) : "vcc", "scc");
      const u32x4 a = {a0, a1, a2, a3}, c = {c0, c1, c2, c3};
      asm volatile("global_store_dwordx4 %0, %1, %3 sc1\n.Lkf_last:\n\tglobal_store_dwordx4 %0, %2, %4 sc1\n\ts_nop 1" :: "v"(oo), "v"(a), "v"(c), "s"(op), "s"(op2) : "memory");
    }
    if (s) {
    } else {
    }
  }
}

extern "C" void kernel_launch(void* const* d_in, const int* in_sizes, int n_in,
                              void* d_out, int out_size, void* d_ws, size_t ws_size,
                              hipStream_t stream) {
  (void)hipFuncSetAttribute((const void*)kf_main, hipFuncAttributeMaxDynamicSharedMemorySize, LDSM);
  kf_main<<<256, 768, LDSM, stream>>>((const u16*)d_in[0], (const u16*)d_in[1], (char*)d_ws, (const u16*)d_in[5], (const u16*)d_in[2],
                                      (const u16*)d_in[3], (const u16*)d_in[6], (const u16*)d_in[7], (const u16*)d_in[8], (u16*)d_out);
}
```
